```python
import torch

def _softmax(x, axis=-1):
    return torch.softmax(x, dim=axis)
def _gelu(x):
    return torch.nn.functional.gelu(x, approximate="tanh")
def _silu(x):
    return torch.nn.functional.silu(x)
def _sigmoid(x):
    return torch.sigmoid(x)
def _layernorm(x, axis=-1, eps=1e-5):
    m = x.mean(dim=axis, keepdim=True)
    v = x.var(dim=axis, keepdim=True, unbiased=False)
    return (x - m) / torch.sqrt(v + eps)
def _scatter_add(tgt, idx, src, axis=0):
    return tgt.clone().index_add_(axis, idx, src)
def _segment_sum(src, seg, n):
    out = torch.zeros((n,) + src.shape[1:], dtype=src.dtype)
    return out.index_add_(0, seg, src)
def _pad(x, pw):
    flat = tuple(v for pair in reversed(pw) for v in pair)
    return torch.nn.functional.pad(x, flat)

kernel_name = 'sequential_preLN__linear_attn_block__mixer_s_mlp__prompt'

def setup_inputs():

    _w = (lambda a: (lambda t: t.to(torch.bfloat16) if t.is_floating_point() else t)(torch.as_tensor(a)))
    def _A_setup():
        B, S, D = 1, 2048, 576
        return {
            "x": _w((lambda *s: torch.randn(*s, dtype=torch.bfloat16) * 0.05)(B, S, D)),
            "wqkv": _w((lambda *s: torch.randn(*s, dtype=torch.bfloat16) * 0.05)(D, 3 * D)),
            "wo": _w((lambda *s: torch.randn(*s, dtype=torch.bfloat16) * 0.05)(D, D)),
        }
    _A_in = _A_setup()

    def _B_setup():
        B, S, D, F = 1, 2048, 576, 4608
        return {
            "x":  _w((lambda *s: torch.randn(*s, dtype=torch.bfloat16) * 0.05)(B, S, D)),
            "w1": _w((lambda *s: torch.randn(*s, dtype=torch.bfloat16) * 3.2)(D, D) * (D ** -0.5)),
            "w2": _w((lambda *s: torch.randn(*s, dtype=torch.bfloat16) * 3.2)(D, D) * (D ** -0.5)),
            "w3": _w((lambda *s: torch.randn(*s, dtype=torch.bfloat16) * 3.2)(D, F) * (D ** -0.5)),
            "w4": _w((lambda *s: torch.randn(*s, dtype=torch.bfloat16) * 1.6)(F, D) * (F ** -0.5)),
        }
    _B_in = _B_setup()

    return {
        "A_x": _A_in["x"],
        "A_wqkv": _A_in["wqkv"],
        "A_wo": _A_in["wo"],
        "B_x": _B_in["x"],
        "B_w1": _B_in["w1"],
        "B_w2": _B_in["w2"],
        "B_w3": _B_in["w3"],
        "B_w4": _B_in["w4"],
    }

def reference(A_x, A_wqkv, A_wo, B_x, B_w1, B_w2, B_w3, B_w4):
    def _A_ref(x, wqkv, wo):
        B, S, D = x.shape
        H = 32; Dh = D // H
        qkv = (lambda x, *d: x.permute(*d))((x @ wqkv).reshape(B, S, 3, H, Dh), 2, 0, 3, 1, 4)
        q, k, v = qkv[0], qkv[1], qkv[2]
        phi = lambda t: torch.where(t > 0, t + 1, torch.exp(t)) + 1e-6
        pq, pk = phi(q), phi(k)
        kv = torch.einsum("bhsd,bhse->bhde", pk, v)
        z = torch.einsum("bhsd->bhd", pk)
        num = torch.einsum("bhqd,bhde->bhqe", pq, kv)
        den = torch.einsum("bhqd,bhd->bhq", pq, z)[..., None] + 1e-6
        o = num / den
        return (lambda x, *d: x.permute(*d))(o, 0, 2, 1, 3).reshape(B, S, D) @ wo

    def _B_ref(x, w1, w2, w3, w4):
        B, S, D = x.shape
        h = _gelu(_layernorm(x, axis=-1) @ w1)
        c = D // 4
        g0 = _pad(h[:, :, 0*c:1*c], ((0, 0), (1, 0), (0, 0)))[:, :S, :]
        g1 = _pad(h[:, :, 1*c:2*c], ((0, 0), (0, 1), (0, 0)))[:, 1:, :]
        g2 = _pad(h[:, :, 2*c:3*c], ((0, 0), (2, 0), (0, 0)))[:, :S, :]
        g3 = _pad(h[:, :, 3*c:   ], ((0, 0), (0, 2), (0, 0)))[:, 2:, :]
        hs = torch.concatenate([g0, g1, g2, g3], axis=-1) @ w2
        x1 = x + hs
        h2 = _gelu(_layernorm(x1, axis=-1) @ w3) @ w4
        return x1 + h2

    x = A_x
    h = x + _A_ref(_layernorm(x, axis=-1), A_wqkv, A_wo)
    h2 = h + _B_ref(_layernorm(h, axis=-1), B_w1, B_w2, B_w3, B_w4)
    return h2
```

```cpp
#include <hip/hip_runtime.h>
#include <hip/hip_bf16.h>
typedef unsigned short u16;
constexpr int S=2048, D=576, F=4608, H=32, DH=18;
__device__ __forceinline__ float b2f(u16 x){ return __uint_as_float(((unsigned)x)<<16); }
__device__ __forceinline__ u16 f2b(float f){ unsigned u=__float_as_uint(f); u += 0x7fffu + ((u>>16)&1u); return (u16)(u>>16); }
__device__ __forceinline__ float gelu(float x){ float u=0.7978845608f*(x+0.044715f*x*x*x); return 0.5f*x*(1.f+tanhf(u)); }

__global__ void k_ln(const float* in, float* out){
  int r=blockIdx.x; int t=threadIdx.x; __shared__ float red[64];
  float s=0; for(int c=t;c<D;c+=64) s+=in[r*D+c];
  for(int o=32;o;o>>=1) s+=__shfl_xor(s,o); float m=s/D;
  float v=0; for(int c=t;c<D;c+=64){float d=in[r*D+c]-m; v+=d*d;}
  for(int o=32;o;o>>=1) v+=__shfl_xor(v,o); v/=D;
  float rs=1.f/sqrtf(v+1e-5f);
  for(int c=t;c<D;c+=64) out[r*D+c]=(in[r*D+c]-m)*rs;
}
__global__ void k_cvt(const u16* in, float* out, int n){ int i=blockIdx.x*256+threadIdx.x; if(i<n) out[i]=b2f(in[i]); }
template<int MODE> __global__ void k_gemm(const float* A, const u16* W, float* C, int N, int K){
  __shared__ float As[32][33]; __shared__ float Ws[32][33];
  int tx=threadIdx.x&31, ty=threadIdx.x>>5; int r0=blockIdx.y*32, c0=blockIdx.x*32;
  float acc[4]={0,0,0,0};
  for(int k0=0;k0<K;k0+=32){
    for(int i=0;i<4;i++){ As[ty+8*i][tx]=b2f(f2b(A[(size_t)(r0+ty+8*i)*K+k0+tx])); Ws[ty+8*i][tx]=b2f(W[(size_t)(k0+ty+8*i)*N+c0+tx]); }
    __syncthreads();
    for(int k=0;k<32;k++){ float w=Ws[k][tx]; for(int i=0;i<4;i++) acc[i]+=As[ty+8*i][k]*w; }
    __syncthreads();
  }
  for(int i=0;i<4;i++){ float v=acc[i]; if(MODE==1) v=gelu(v); C[(size_t)(r0+ty+8*i)*N+c0+tx]=v; }
}
__device__ __forceinline__ float phi(float t){ return (t>0? t+1.f : __expf(t)) + 1e-6f; }
__global__ void k_kv(const float* qkv, float* kv){
  int h=blockIdx.x, t=threadIdx.x; if(t>=DH*(DH+1)) return; int d=t/(DH+1), e=t%(DH+1);
  float s=0; for(int r=0;r<S;r++){ float pk=phi(qkv[r*3*D+D+h*DH+d]); float v= e<DH? qkv[r*3*D+2*D+h*DH+e]:1.f; s+=pk*v; }
  kv[h*DH*(DH+1)+t]=s;
}
__global__ void k_o(const float* qkv, const float* kv, float* o){ int r=blockIdx.x, c=threadIdx.x; int h=c/DH, e=c%DH;
  float num=0, den=0; for(int d=0;d<DH;d++){ float pq=phi(qkv[r*3*D+h*DH+d]); num+=pq*kv[h*DH*(DH+1)+d*(DH+1)+e]; den+=pq*kv[h*DH*(DH+1)+d*(DH+1)+DH]; }
  o[r*D+c]=num/(den+1e-6f);
}
__global__ void k_add(const float* a, const float* b, float* c, int n){ int i=blockIdx.x*256+threadIdx.x; if(i<n) c[i]=a[i]+b[i]; }
__global__ void k_shift(const float* in, float* out){ int r=blockIdx.x, c=threadIdx.x; int g=c/(D/4); int sr = g==0? r-1: g==1? r+1: g==2? r-2: r+2; out[r*D+c] = (sr>=0&&sr<S)? in[sr*D+c]:0.f; }
__global__ void k_fin(const float* h, const float* x1, const float* h2, u16* out, int n){ int i=blockIdx.x*256+threadIdx.x; if(i<n) out[i]=f2b(h[i]+x1[i]+h2[i]); }

extern "C" void kernel_launch(void* const* d_in, const int* in_sizes, int n_in, void* d_out, int out_size, void* d_ws, size_t ws_size, hipStream_t stream){
  const u16* x=(const u16*)d_in[0]; const u16* wqkv=(const u16*)d_in[1]; const u16* wo=(const u16*)d_in[2];
  const u16* w1=(const u16*)d_in[4]; const u16* w2=(const u16*)d_in[5]; const u16* w3=(const u16*)d_in[6]; const u16* w4=(const u16*)d_in[7];
  float* ws=(float*)d_ws; size_t n=S*D;
  float* xf=ws; float* ln=ws+n; float* qkv=ws+2*n; float* kv=ws+5*n; float* o=ws+6*n; float* t=ws+7*n; float* h=ws+8*n; float* xb=ws+9*n; float* hh=ws+10*n; float* sh=ws+11*n; float* x1=ws+12*n; float* big=ws+13*n;
  int g=(n+255)/256;
  k_cvt<<<g,256,0,stream>>>(x,xf,n);
  k_ln<<<S,64,0,stream>>>(xf,ln);
  k_gemm<0><<<dim3(3*D/32,S/32),256,0,stream>>>(ln,wqkv,qkv,3*D,D);
  k_kv<<<H,384,0,stream>>>(qkv,kv);
  k_o<<<S,D,0,stream>>>(qkv,kv,o);
  k_gemm<0><<<dim3(D/32,S/32),256,0,stream>>>(o,wo,t,D,D);
  k_add<<<g,256,0,stream>>>(xf,t,h,n);
  k_ln<<<S,64,0,stream>>>(h,xb);
  k_ln<<<S,64,0,stream>>>(xb,ln);
  k_gemm<1><<<dim3(D/32,S/32),256,0,stream>>>(ln,w1,hh,D,D);
  k_shift<<<S,D,0,stream>>>(hh,sh);
  k_gemm<0><<<dim3(D/32,S/32),256,0,stream>>>(sh,w2,t,D,D);
  k_add<<<g,256,0,stream>>>(xb,t,x1,n);
  k_ln<<<S,64,0,stream>>>(x1,ln);
  k_gemm<1><<<dim3(F/32,S/32),256,0,stream>>>(ln,w3,big,F,D);
  k_gemm<0><<<dim3(D/32,S/32),256,0,stream>>>(big,w4,t,D,F);
  k_fin<<<g,256,0,stream>>>(h,x1,t,(u16*)d_out,n);
}
```

```cpp
#include <hip/hip_runtime.h>

__global__ __launch_bounds__(256, 1) void fused_block_kernel(const void* x, const void* wqkv, const void* wo, const void* w1, const void* w2,
                                                const void* w3, const void* w4, void* out, void* ws) {
  __shared__ __attribute__((aligned(16))) char smem[163840];
  asm volatile(
"s_load_dwordx16 s[36:51], %0, 0\n"
"s_load_dwordx2 s[52:53], %0, 64\n"
"s_mov_b32 s27, %1\n"
"s_getpc_b64 s[92:93]\n"
".Lfused_block_kernel_pc:\n"
"v_lshrrev_b32 v1, 6, %2\n"
"v_and_b32 v0, 63, %2\n"
"v_readfirstlane_b32 s26, v1\n"
"v_and_b32 v2, 15, v0\n"
"s_and_b32 s72, s27, 7\n"
"s_lshr_b32 s73, s27, 3\n"
"s_cmp_eq_u32 s26, 3\n"
"s_cbranch_scc0 .Lfused_block_kernel_s1\n"
"s_lshl_b32 s74, s73, 11\n"
"v_lshlrev_b32 v4, 7, v2\n"
"v_add_u32 v4, s74, v4\n"
"v_min_u32 v4, .Lfused_block_kernel_end-.Lfused_block_kernel_pc, v4\n"
"global_load_dword v216, v4, s[92:93]\n"
".Lfused_block_kernel_s1:\n"
"s_lshr_b32 s74, s72, 1\n"
"s_lshl_b32 s74, s74, 3\n"
"s_lshr_b32 s75, s73, 2\n"
"s_add_u32 s28, s74, s75\n"
"s_lshl_b32 s32, s28, 2\n"
"s_add_u32 s32, s32, s26\n"
"s_mul_i32 s74, s28, 1\n"
"s_mul_i32 s75, s74, 7282\n"
"s_lshr_b32 s75, s75, 16\n"
"s_mul_i32 s35, s75, 9\n"
"s_sub_u32 s35, s74, s35\n"
"v_lshrrev_b32 v4, 3, v0\n"
"v_and_b32 v5, 7, v0\n"
"v_mul_u32_u24 v10, 1152, v4\n"
"v_lshl_add_u32 v10, v5, 4, v10\n"
"v_add_u32 v11, 9216, v10\n"
"s_mul_i32 s88, s32, 18432\n"
"s_lshl_b32 s74, s35, 7\n"
"s_add_u32 s88, s88, s74\n"
"s_sub_u32 s89, s88, 1152\n"
"s_mov_b32 s14, 2359296\n"
"s_mov_b32 s15, 0x20000\n"
"s_waitcnt lgkmcnt(0)\n"
"s_mov_b32 s12, s36\n"
"s_and_b32 s13, s37, 0xffff\n"
"s_cmp_ge_u32 s35, 9\n"
"s_cselect_b32 s86, s89, s88\n"
"s_add_u32 s86, s86, 0\n"
"buffer_load_dwordx4 v[100:103], v10, s[12:15], s86 offen\n"
"buffer_load_dwordx4 v[104:107], v11, s[12:15], s86 offen\n"
"s_and_b32 s74, s72, 1\n"
"s_lshl_b32 s74, s74, 2\n"
"s_and_b32 s75, s73, 3\n"
"s_add_u32 s29, s74, s75\n"
"s_lshl_b32 s33, s28, 3\n"
"s_add_u32 s33, s33, s29\n"
"s_load_dwordx2 s[24:25], s[52:53], 0\n"
"v_lshlrev_b32 v1, 4, v0\n"
"v_lshrrev_b32 v3, 4, v0\n"
"v_mov_b32 v8, 2146435072\n"
"s_mov_b32 s16, s38\n"
"s_and_b32 s17, s39, 0xffff\n"
"s_cmp_ge_u32 s35, 8\n"
"s_cselect_b32 s86, s89, s88\n"
"s_add_u32 s86, s86, 128\n"
"buffer_load_dwordx4 v[108:111], v10, s[12:15], s86 offen\n"
"buffer_load_dwordx4 v[112:115], v11, s[12:15], s86 offen\n"
"s_mov_b32 s18, 1990656\n"
"s_mov_b32 s19, 0x20000\n"
"s_mov_b32 s8, s52\n"
"s_and_b32 s9, s53, 0xffff\n"
"s_mov_b32 s10, 47185920\n"
"s_mov_b32 s11, 0x20000\n"
"v_mul_u32_u24 v6, 7282, v0\n"
"s_mov_b32 s74, 3312\n"
"v_add_u32 v7, 0, v6\n"
"v_lshrrev_b32 v7, 16, v7\n"
"v_mad_u32_u24 v12, v7, s74, v1\n"
"v_add_u32 v7, 466048, v6\n"
"s_cmp_ge_u32 s35, 7\n"
"s_cselect_b32 s86, s89, s88\n"
"s_add_u32 s86, s86, 256\n"
"buffer_load_dwordx4 v[116:119], v10, s[12:15], s86 offen\n"
"buffer_load_dwordx4 v[120:123], v11, s[12:15], s86 offen\n"
"v_lshrrev_b32 v7, 16, v7\n"
"v_mad_u32_u24 v13, v7, s74, v1\n"
"v_add_u32 v7, 932096, v6\n"
"v_lshrrev_b32 v7, 16, v7\n"
"v_mad_u32_u24 v14, v7, s74, v1\n"
"v_add_u32 v7, 1398144, v6\n"
"v_lshrrev_b32 v7, 16, v7\n"
"v_mad_u32_u24 v15, v7, s74, v1\n"
"v_add_u32 v7, 1864192, v6\n"
"v_lshrrev_b32 v7, 16, v7\n"
"v_mad_u32_u24 v16, v7, s74, v1\n"
"s_cmp_ge_u32 s35, 6\n"
"s_cselect_b32 s86, s89, s88\n"
"s_add_u32 s86, s86, 384\n"
"buffer_load_dwordx4 v[124:127], v10, s[12:15], s86 offen\n"
"buffer_load_dwordx4 v[128:131], v11, s[12:15], s86 offen\n"
"v_add_u32 v16, 4096, v16\n"
"v_add_u32 v7, 2330240, v6\n"
"v_lshrrev_b32 v7, 16, v7\n"
"v_mad_u32_u24 v17, v7, s74, v1\n"
"v_add_u32 v17, 4096, v17\n"
"v_add_u32 v7, 2796288, v6\n"
"v_lshrrev_b32 v7, 16, v7\n"
"v_mad_u32_u24 v18, v7, s74, v1\n"
"v_add_u32 v18, 4096, v18\n"
"v_add_u32 v7, 3262336, v6\n"
"v_lshrrev_b32 v7, 16, v7\n"
"v_mad_u32_u24 v19, v7, s74, v1\n"
"s_cmp_ge_u32 s35, 5\n"
"s_cselect_b32 s86, s89, s88\n"
"s_add_u32 s86, s86, 512\n"
"buffer_load_dwordx4 v[132:135], v10, s[12:15], s86 offen\n"
"buffer_load_dwordx4 v[136:139], v11, s[12:15], s86 offen\n"
"v_add_u32 v19, 4096, v19\n"
"v_add_u32 v7, 3728384, v6\n"
"v_lshrrev_b32 v7, 16, v7\n"
"v_mad_u32_u24 v20, v7, s74, v1\n"
"v_add_u32 v20, 8192, v20\n"
"s_mov_b32 s4, 0x05040100\n"
"s_mov_b32 s5, 0x07060302\n"
"s_mov_b32 s6, 0x3f803f80\n"
"s_mov_b32 s7, 0x00003f80\n"
"s_mov_b32 s34, 0x3f800000\n"
"s_cmp_ge_u32 s26, 2\n"
"s_cselect_b32 s76, 2, 0\n"
"s_cselect_b32 s91, 36864, 0\n"
"s_cmp_ge_u32 s35, 4\n"
"s_cselect_b32 s86, s89, s88\n"
"s_add_u32 s86, s86, 640\n"
"buffer_load_dwordx4 v[140:143], v10, s[12:15], s86 offen\n"
"buffer_load_dwordx4 v[144:147], v11, s[12:15], s86 offen\n"
"s_add_u32 s76, s76, s26\n"
"s_mul_i32 s90, s29, 144\n"
"s_add_u32 s90, s90, 1152\n"
"s_add_u32 s76, s76, s35\n"
"s_cmp_ge_u32 s76, 9\n"
"s_cselect_b32 s77, 9, 0\n"
"s_sub_u32 s76, s76, s77\n"
"s_mul_i32 s76, s76, 221184\n"
"s_add_u32 s90, s90, s76\n"
"s_mul_i32 s76, s26, 9216\n"
"s_add_u32 s91, s91, s76\n"
"s_lshl_b32 s78, s26, 11\n"
"s_add_u32 s78, s78, 131072\n"
"s_cmp_ge_u32 s35, 3\n"
"s_cselect_b32 s86, s89, s88\n"
"s_add_u32 s86, s86, 768\n"
"buffer_load_dwordx4 v[148:151], v10, s[12:15], s86 offen\n"
"buffer_load_dwordx4 v[152:155], v11, s[12:15], s86 offen\n"
"v_and_b32 v6, 3, v5\n"
"v_lshlrev_b32 v7, 1, v6\n"
"v_xor_b32 v7, v7, v4\n"
"v_lshlrev_b32 v7, 4, v7\n"
"v_lshl_add_u32 v7, v6, 8, v7\n"
"v_lshrrev_b32 v6, 2, v5\n"
"v_lshl_add_u32 v7, v6, 3, v7\n"
"v_add_u32 v24, s78, v7\n"
"v_lshlrev_b32 v7, 1, v3\n"
"v_xor_b32 v7, v7, v2\n"
"v_lshlrev_b32 v7, 4, v7\n"
"s_cmp_ge_u32 s35, 2\n"
"s_cselect_b32 s86, s89, s88\n"
"s_add_u32 s86, s86, 896\n"
"buffer_load_dwordx4 v[156:159], v10, s[12:15], s86 offen\n"
"buffer_load_dwordx4 v[160:163], v11, s[12:15], s86 offen\n"
"v_lshl_add_u32 v7, v3, 8, v7\n"
"v_add_u32 v25, s78, v7\n"
"s_lshl_b32 s78, s26, 6\n"
"s_add_u32 s78, s78, 139264\n"
"v_lshl_add_u32 v62, v4, 2, s78\n"
"v_lshl_add_u32 v63, v2, 2, s78\n"
"v_bfe_u32 v68, v0, 2, 4\n"
"v_mul_u32_u24 v68, 288, v68\n"
"v_and_b32 v69, 3, v0\n"
"v_lshl_add_u32 v4, v69, 3, v68\n"
"v_add_u32 v5, 0x10000, v4\n"
"v_and_b32 v69, 1, v69\n"
"s_cmp_ge_u32 s35, 1\n"
"s_cselect_b32 s86, s89, s88\n"
"s_add_u32 s86, s86, 1024\n"
"buffer_load_dwordx4 v[164:167], v10, s[12:15], s86 offen\n"
"buffer_load_dwordx4 v[168:171], v11, s[12:15], s86 offen\n"
"v_mul_u32_u24 v69, 72, v69\n"
"v_add3_u32 v6, v68, v69, 32\n"
"v_add_u32 v7, 0x10000, v6\n"
"v_mov_b32 v72, 0\n"
"v_mov_b32 v73, 0\n"
"v_mov_b32 v74, 0\n"
"v_mov_b32 v75, 0\n"
"v_mov_b32 v76, 0\n"
"v_mov_b32 v77, 0\n"
"v_mov_b32 v78, 0\n"
"v_mov_b32 v79, 0\n"
"s_add_u32 m0, s91, 0\n"
"s_nop 0\n"
"buffer_load_dwordx4 v12, s[16:19], s90 offen offset:0 lds\n"
"s_lshl_b32 s31, s33, 11\n"
"s_add_u32 s31, s31, 65536\n"
"s_lshl_b32 s30, s28, 14\n"
"s_lshl_b32 s72, s29, 3\n"
"s_add_u32 s30, s30, s72\n"
"s_add_u32 s30, s30, 65536\n"
"buffer_load_dwordx4 v13, s[16:19], s90 offen offset:1024 lds\n"
"s_mul_i32 s71, s32, 20480\n"
"s_lshr_b32 s72, s26, 1\n"
"s_lshl_b32 s72, s72, 10\n"
"s_and_b32 s73, s26, 1\n"
"s_lshl_b32 s73, s73, 3\n"
"buffer_load_dwordx4 v14, s[16:19], s90 offen offset:2048 lds\n"
"s_add_u32 s72, s72, s73\n"
"s_add_u32 s72, s72, 110592\n"
"v_add_u32 v26, s72, v1\n"
"s_lshl_b32 s72, s29, 2\n"
"s_add_u32 s72, s72, s26\n"
"buffer_load_dwordx4 v15, s[16:19], s90 offen offset:3072 lds\n"
"s_mul_i32 s100, s72, 24576\n"
"s_add_u32 s100, s100, 1048576\n"
"s_mul_i32 s101, s28, 768\n"
"s_add_u32 s101, s101, s100\n"
"v_lshlrev_b32 v27, 3, v0\n"
"s_add_u32 m0, s91, 4096\n"
"s_nop 0\n"
"buffer_load_dwordx4 v16, s[16:19], s90 offen offset:0 lds\n"
"s_lshl_b32 s72, s26, 1\n"
"v_subrev_u32 v68, s72, v2\n"
"v_cmp_eq_u32 vcc, 8, v2\n"
"v_cmp_gt_u32 s[74:75], 2, v68\n"
"s_nop 0\n"
"v_cndmask_b32 v69, v68, 2, vcc\n"
"s_or_b64 s[74:75], s[74:75], vcc\n"
"s_lshr_b32 s72, s26, 1\n"
"v_cmp_eq_u32 s[78:79], s72, v3\n"
"buffer_load_dwordx4 v17, s[16:19], s90 offen offset:1024 lds\n"
"v_lshlrev_b32 v28, 5, v69\n"
"s_and_b64 s[80:81], s[74:75], s[78:79]\n"
"v_lshl_add_u32 v28, v3, 3, v28\n"
"v_cndmask_b32 v28, v8, v28, s[74:75]\n"
"v_lshlrev_b32 v29, 2, v2\n"
"buffer_load_dwordx4 v18, s[16:19], s90 offen offset:2048 lds\n"
"v_cndmask_b32 v29, v8, v29, s[78:79]\n"
"v_lshlrev_b32 v30, 2, v69\n"
"v_cndmask_b32 v30, v8, v30, s[80:81]\n"
"v_add_u32 v32, 110592, v1\n"
"s_lshl_b32 s72, s26, 11\n"
"buffer_load_dwordx4 v19, s[16:19], s90 offen offset:3072 lds\n"
"v_add_u32 v31, s72, v32\n"
"v_lshlrev_b32 v33, 14, v0\n"
"v_cmp_gt_u32 vcc, 32, v0\n"
"s_nop 1\n"
"v_cndmask_b32 v33, v8, v33, vcc\n"
"v_lshlrev_b32 v47, 11, v0\n"
"v_cmp_gt_u32 vcc, 8, v0\n"
"s_nop 1\n"
"v_cndmask_b32 v47, v8, v47, vcc\n"
"s_add_u32 m0, s91, 8192\n"
"s_nop 0\n"
"buffer_load_dwordx4 v20, s[16:19], s90 offen offset:0 lds\n"
"v_mov_b32 v52, 0\n"
"v_cmp_eq_u32 vcc, 0, v0\n"
"s_nop 1\n"
"v_cndmask_b32 v52, v8, v52, vcc\n"
"v_and_b32 v51, 0xf8, v27\n"
"v_and_b32 v50, 0x78, v27\n"
"v_and_b32 v49, 56, v27\n"
"s_cmp_lt_u32 s26, 2\n"
"s_cbranch_scc0 .Lfused_block_kernel_s2\n"
"s_add_u32 s76, s26, 2\n"
"s_mov_b32 s91, 36864\n"
"s_mul_i32 s90, s29, 144\n"
"s_add_u32 s90, s90, 1152\n"
"s_add_u32 s76, s76, s35\n"
"s_cmp_ge_u32 s76, 9\n"
"s_cselect_b32 s77, 9, 0\n"
"s_sub_u32 s76, s76, s77\n"
"s_mul_i32 s76, s76, 221184\n"
"s_add_u32 s90, s90, s76\n"
"s_mul_i32 s76, s26, 9216\n"
"s_add_u32 s91, s91, s76\n"
"s_waitcnt vmcnt(25)\n"
"v_dot2c_f32_bf16 v72, s6, v100\n"
"v_dot2c_f32_bf16 v76, v100, v100\n"
"v_dot2c_f32_bf16 v74, s6, v104\n"
"v_dot2c_f32_bf16 v78, v104, v104\n"
"v_dot2c_f32_bf16 v73, s6, v101\n"
"v_dot2c_f32_bf16 v77, v101, v101\n"
"v_dot2c_f32_bf16 v75, s6, v105\n"
"v_dot2c_f32_bf16 v79, v105, v105\n"
"v_dot2c_f32_bf16 v72, s6, v102\n"
"v_dot2c_f32_bf16 v76, v102, v102\n"
"v_dot2c_f32_bf16 v74, s6, v106\n"
"v_dot2c_f32_bf16 v78, v106, v106\n"
"v_dot2c_f32_bf16 v73, s6, v103\n"
"v_dot2c_f32_bf16 v77, v103, v103\n"
"v_dot2c_f32_bf16 v75, s6, v107\n"
"v_dot2c_f32_bf16 v79, v107, v107\n"
"s_add_u32 m0, s91, 0\n"
"s_nop 0\n"
"buffer_load_dwordx4 v12, s[16:19], s90 offen offset:0 lds\n"
"s_waitcnt vmcnt(24)\n"
"v_dot2c_f32_bf16 v72, s6, v108\n"
"v_dot2c_f32_bf16 v76, v108, v108\n"
"v_dot2c_f32_bf16 v74, s6, v112\n"
"v_dot2c_f32_bf16 v78, v112, v112\n"
"v_dot2c_f32_bf16 v73, s6, v109\n"
"v_dot2c_f32_bf16 v77, v109, v109\n"
"v_dot2c_f32_bf16 v75, s6, v113\n"
"v_dot2c_f32_bf16 v79, v113, v113\n"
"v_dot2c_f32_bf16 v72, s6, v110\n"
"v_dot2c_f32_bf16 v76, v110, v110\n"
"v_dot2c_f32_bf16 v74, s6, v114\n"
"v_dot2c_f32_bf16 v78, v114, v114\n"
"v_dot2c_f32_bf16 v73, s6, v111\n"
"v_dot2c_f32_bf16 v77, v111, v111\n"
"v_dot2c_f32_bf16 v75, s6, v115\n"
"v_dot2c_f32_bf16 v79, v115, v115\n"
"buffer_load_dwordx4 v13, s[16:19], s90 offen offset:1024 lds\n"
"s_waitcnt vmcnt(23)\n"
"v_dot2c_f32_bf16 v72, s6, v116\n"
"v_dot2c_f32_bf16 v76, v116, v116\n"
"v_dot2c_f32_bf16 v74, s6, v120\n"
"v_dot2c_f32_bf16 v78, v120, v120\n"
"v_dot2c_f32_bf16 v73, s6, v117\n"
"v_dot2c_f32_bf16 v77, v117, v117\n"
"v_dot2c_f32_bf16 v75, s6, v121\n"
"v_dot2c_f32_bf16 v79, v121, v121\n"
"v_dot2c_f32_bf16 v72, s6, v118\n"
"v_dot2c_f32_bf16 v76, v118, v118\n"
"v_dot2c_f32_bf16 v74, s6, v122\n"
"v_dot2c_f32_bf16 v78, v122, v122\n"
"v_dot2c_f32_bf16 v73, s6, v119\n"
"v_dot2c_f32_bf16 v77, v119, v119\n"
"v_dot2c_f32_bf16 v75, s6, v123\n"
"v_dot2c_f32_bf16 v79, v123, v123\n"
"buffer_load_dwordx4 v14, s[16:19], s90 offen offset:2048 lds\n"
"s_waitcnt vmcnt(22)\n"
"v_dot2c_f32_bf16 v72, s6, v124\n"
"v_dot2c_f32_bf16 v76, v124, v124\n"
"v_dot2c_f32_bf16 v74, s6, v128\n"
"v_dot2c_f32_bf16 v78, v128, v128\n"
"v_dot2c_f32_bf16 v73, s6, v125\n"
"v_dot2c_f32_bf16 v77, v125, v125\n"
"v_dot2c_f32_bf16 v75, s6, v129\n"
"v_dot2c_f32_bf16 v79, v129, v129\n"
"v_dot2c_f32_bf16 v72, s6, v126\n"
"v_dot2c_f32_bf16 v76, v126, v126\n"
"v_dot2c_f32_bf16 v74, s6, v130\n"
"v_dot2c_f32_bf16 v78, v130, v130\n"
"v_dot2c_f32_bf16 v73, s6, v127\n"
"v_dot2c_f32_bf16 v77, v127, v127\n"
"v_dot2c_f32_bf16 v75, s6, v131\n"
"v_dot2c_f32_bf16 v79, v131, v131\n"
"buffer_load_dwordx4 v15, s[16:19], s90 offen offset:3072 lds\n"
"s_waitcnt vmcnt(21)\n"
"v_dot2c_f32_bf16 v72, s6, v132\n"
"v_dot2c_f32_bf16 v76, v132, v132\n"
"v_dot2c_f32_bf16 v74, s6, v136\n"
"v_dot2c_f32_bf16 v78, v136, v136\n"
"v_dot2c_f32_bf16 v73, s6, v133\n"
"v_dot2c_f32_bf16 v77, v133, v133\n"
"v_dot2c_f32_bf16 v75, s6, v137\n"
"v_dot2c_f32_bf16 v79, v137, v137\n"
"v_dot2c_f32_bf16 v72, s6, v134\n"
"v_dot2c_f32_bf16 v76, v134, v134\n"
"v_dot2c_f32_bf16 v74, s6, v138\n"
"v_dot2c_f32_bf16 v78, v138, v138\n"
"v_dot2c_f32_bf16 v73, s6, v135\n"
"v_dot2c_f32_bf16 v77, v135, v135\n"
"v_dot2c_f32_bf16 v75, s6, v139\n"
"v_dot2c_f32_bf16 v79, v139, v139\n"
"s_add_u32 m0, s91, 4096\n"
"s_nop 0\n"
"buffer_load_dwordx4 v16, s[16:19], s90 offen offset:0 lds\n"
"s_waitcnt vmcnt(20)\n"
"v_dot2c_f32_bf16 v72, s6, v140\n"
"v_dot2c_f32_bf16 v76, v140, v140\n"
"v_dot2c_f32_bf16 v74, s6, v144\n"
"v_dot2c_f32_bf16 v78, v144, v144\n"
"v_dot2c_f32_bf16 v73, s6, v141\n"
"v_dot2c_f32_bf16 v77, v141, v141\n"
"v_dot2c_f32_bf16 v75, s6, v145\n"
"v_dot2c_f32_bf16 v79, v145, v145\n"
"v_dot2c_f32_bf16 v72, s6, v142\n"
"v_dot2c_f32_bf16 v76, v142, v142\n"
"v_dot2c_f32_bf16 v74, s6, v146\n"
"v_dot2c_f32_bf16 v78, v146, v146\n"
"v_dot2c_f32_bf16 v73, s6, v143\n"
"v_dot2c_f32_bf16 v77, v143, v143\n"
"v_dot2c_f32_bf16 v75, s6, v147\n"
"v_dot2c_f32_bf16 v79, v147, v147\n"
"buffer_load_dwordx4 v17, s[16:19], s90 offen offset:1024 lds\n"
"s_waitcnt vmcnt(19)\n"
"v_dot2c_f32_bf16 v72, s6, v148\n"
"v_dot2c_f32_bf16 v76, v148, v148\n"
"v_dot2c_f32_bf16 v74, s6, v152\n"
"v_dot2c_f32_bf16 v78, v152, v152\n"
"v_dot2c_f32_bf16 v73, s6, v149\n"
"v_dot2c_f32_bf16 v77, v149, v149\n"
"v_dot2c_f32_bf16 v75, s6, v153\n"
"v_dot2c_f32_bf16 v79, v153, v153\n"
"v_dot2c_f32_bf16 v72, s6, v150\n"
"v_dot2c_f32_bf16 v76, v150, v150\n"
"v_dot2c_f32_bf16 v74, s6, v154\n"
"v_dot2c_f32_bf16 v78, v154, v154\n"
"v_dot2c_f32_bf16 v73, s6, v151\n"
"v_dot2c_f32_bf16 v77, v151, v151\n"
"v_dot2c_f32_bf16 v75, s6, v155\n"
"v_dot2c_f32_bf16 v79, v155, v155\n"
"buffer_load_dwordx4 v18, s[16:19], s90 offen offset:2048 lds\n"
"s_waitcnt vmcnt(18)\n"
"v_dot2c_f32_bf16 v72, s6, v156\n"
"v_dot2c_f32_bf16 v76, v156, v156\n"
"v_dot2c_f32_bf16 v74, s6, v160\n"
"v_dot2c_f32_bf16 v78, v160, v160\n"
"v_dot2c_f32_bf16 v73, s6, v157\n"
"v_dot2c_f32_bf16 v77, v157, v157\n"
"v_dot2c_f32_bf16 v75, s6, v161\n"
"v_dot2c_f32_bf16 v79, v161, v161\n"
"v_dot2c_f32_bf16 v72, s6, v158\n"
"v_dot2c_f32_bf16 v76, v158, v158\n"
"v_dot2c_f32_bf16 v74, s6, v162\n"
"v_dot2c_f32_bf16 v78, v162, v162\n"
"v_dot2c_f32_bf16 v73, s6, v159\n"
"v_dot2c_f32_bf16 v77, v159, v159\n"
"v_dot2c_f32_bf16 v75, s6, v163\n"
"v_dot2c_f32_bf16 v79, v163, v163\n"
"buffer_load_dwordx4 v19, s[16:19], s90 offen offset:3072 lds\n"
"s_waitcnt vmcnt(17)\n"
"v_dot2c_f32_bf16 v72, s6, v164\n"
"v_dot2c_f32_bf16 v76, v164, v164\n"
"v_dot2c_f32_bf16 v74, s6, v168\n"
"v_dot2c_f32_bf16 v78, v168, v168\n"
"v_dot2c_f32_bf16 v73, s6, v165\n"
"v_dot2c_f32_bf16 v77, v165, v165\n"
"v_dot2c_f32_bf16 v75, s6, v169\n"
"v_dot2c_f32_bf16 v79, v169, v169\n"
"v_dot2c_f32_bf16 v72, s6, v166\n"
"v_dot2c_f32_bf16 v76, v166, v166\n"
"v_dot2c_f32_bf16 v74, s6, v170\n"
"v_dot2c_f32_bf16 v78, v170, v170\n"
"v_dot2c_f32_bf16 v73, s6, v167\n"
"v_dot2c_f32_bf16 v77, v167, v167\n"
"v_dot2c_f32_bf16 v75, s6, v171\n"
"v_dot2c_f32_bf16 v79, v171, v171\n"
"s_add_u32 m0, s91, 8192\n"
"s_nop 0\n"
"buffer_load_dwordx4 v20, s[16:19], s90 offen offset:0 lds\n"
".Lfused_block_kernel_s2:\n"
"s_cmp_eq_u32 s26, 2\n"
"s_cbranch_scc0 .Lfused_block_kernel_s3\n"
"v_lshlrev_b32 v68, 7, v0\n"
"v_cmp_gt_u32 vcc, 10, v0\n"
"s_nop 1\n"
"v_cndmask_b32 v68, v8, v68, vcc\n"
"buffer_load_dword v217, v68, s[8:11], s31 offen sc1\n"
"s_mov_b32 s70, 0\n"
"s_cmp_lt_u32 s33, 243\n"
"s_cbranch_scc0 .Lfused_block_kernel_s4\n"
"s_mul_i32 s92, s33, 7282\n"
"s_lshr_b32 s92, s92, 16\n"
"s_mul_i32 s89, s92, 9\n"
"s_sub_u32 s89, s33, s89\n"
"s_mul_i32 s88, s92, 7282\n"
"s_lshr_b32 s88, s88, 16\n"
"s_mul_i32 s93, s88, 9\n"
"s_sub_u32 s93, s92, s93\n"
"s_mov_b32 s94, s88\n"
"s_mov_b32 s92, s89\n"
"s_cmp_eq_u32 s89, 8\n"
"s_cselect_b32 s79, 4, 0\n"
"s_add_u32 s79, s79, s88\n"
"s_cmp_eq_u32 s79, 0\n"
"s_cbranch_scc0 .Lfused_block_kernel_s5\n"
"s_mul_i32 s72, s89, 72\n"
"s_mov_b32 s73, 4\n"
"s_mov_b32 s74, 8\n"
"s_mov_b32 s75, 36\n"
"s_mov_b32 s76, 1\n"
"s_mov_b32 s77, 2\n"
"s_mov_b32 s78, 20\n"
".Lfused_block_kernel_s5:\n"
"s_cmp_eq_u32 s79, 4\n"
"s_cbranch_scc0 .Lfused_block_kernel_s6\n"
"s_mov_b32 s72, 16\n"
"s_mov_b32 s73, 72\n"
"s_mov_b32 s74, 144\n"
"s_mov_b32 s75, 288\n"
"s_mov_b32 s76, 1\n"
"s_mov_b32 s77, 2\n"
"s_mov_b32 s78, 36\n"
".Lfused_block_kernel_s6:\n"
"s_cmp_eq_u32 s79, 2\n"
"s_cbranch_scc0 .Lfused_block_kernel_s7\n"
"s_mul_i32 s72, s89, 72\n"
"s_mov_b32 s73, 32\n"
"s_mov_b32 s74, 1\n"
"s_mov_b32 s75, 4\n"
"s_mov_b32 s76, 8\n"
"s_mov_b32 s77, 16\n"
"s_mov_b32 s78, 2\n"
".Lfused_block_kernel_s7:\n"
"s_cmp_eq_u32 s79, 6\n"
"s_cbranch_scc0 .Lfused_block_kernel_s8\n"
"s_mov_b32 s72, 64\n"
"s_mov_b32 s73, 72\n"
"s_mov_b32 s74, 144\n"
"s_mov_b32 s75, 288\n"
"s_mov_b32 s76, 1\n"
"s_mov_b32 s77, 2\n"
"s_mov_b32 s78, 4\n"
".Lfused_block_kernel_s8:\n"
"s_cmp_eq_u32 s79, 5\n"
"s_cbranch_scc0 .Lfused_block_kernel_s9\n"
"s_mov_b32 s72, 512\n"
"s_mov_b32 s73, 1\n"
"s_mov_b32 s74, 2\n"
"s_mov_b32 s75, 4\n"
"s_mov_b32 s76, 8\n"
"s_mov_b32 s77, 16\n"
"s_mov_b32 s78, 32\n"
".Lfused_block_kernel_s9:\n"
"s_cmp_eq_u32 s79, 1\n"
"s_cbranch_scc0 .Lfused_block_kernel_s10\n"
"s_mul_i32 s72, s89, 64\n"
"s_mov_b32 s73, 32\n"
"s_mov_b32 s74, 1\n"
"s_mov_b32 s75, 4\n"
"s_mov_b32 s76, 8\n"
"s_mov_b32 s77, 16\n"
"s_mov_b32 s78, 2\n"
".Lfused_block_kernel_s10:\n"
"s_cmp_eq_u32 s94, 1\n"
"s_cselect_b32 s20, s42, s40\n"
"s_cselect_b32 s21, s43, s41\n"
"s_mov_b32 s83, 2097152\n"
"s_cmov_b32 s83, 2818048\n"
"s_mov_b32 s81, 128\n"
"s_cmov_b32 s81, 144\n"
"s_cmp_eq_u32 s94, 2\n"
"s_cselect_b32 s20, s44, s20\n"
"s_cselect_b32 s21, s45, s21\n"
"s_cmov_b32 s83, 3538944\n"
"s_and_b32 s21, s21, 0xffff\n"
"s_mov_b32 s22, 663552\n"
"s_mov_b32 s23, 0x20000\n"
"s_mov_b32 s70, 1\n"
"s_mul_i32 s81, s81, s93\n"
"s_mov_b32 s80, 1152\n"
"s_mov_b32 s82, 16\n"
"s_mul_i32 s92, s92, 9216\n"
"s_add_u32 s83, s83, s92\n"
"s_mov_b32 s84, 16\n"
"s_mov_b32 s85, 128\n"
"s_mov_b32 s86, 4608\n"
"s_mov_b32 s87, 512\n"
"s_mov_b32 s88, 1024\n"
"s_mov_b32 s89, 2048\n"
"s_mul_i32 s92, s93, 82944\n"
"s_cmp_eq_u32 s93, 8\n"
"s_cbranch_scc0 .Lfused_block_kernel_s11\n"
"s_mov_b32 s92, 4096\n"
"s_mov_b32 s87, 82944\n"
"s_mov_b32 s88, 165888\n"
"s_mov_b32 s89, 331776\n"
"s_cmp_eq_u32 s94, 1\n"
"s_cbranch_scc0 .Lfused_block_kernel_s12\n"
"s_mov_b32 s81, 128\n"
"s_mov_b32 s82, 144\n"
"s_mov_b32 s84, 82944\n"
"s_mov_b32 s85, 128\n"
"s_mov_b32 s86, 4608\n"
"s_mov_b32 s87, 16\n"
"s_mov_b32 s88, 32\n"
"s_mov_b32 s89, 64\n"
".Lfused_block_kernel_s12:\n"
".Lfused_block_kernel_s11:\n"
"s_add_u32 s83, s83, s92\n"
"s_mul_i32 s72, s72, s80\n"
"s_mul_i32 s73, s73, s80\n"
"s_mul_i32 s74, s74, s80\n"
"s_mul_i32 s75, s75, s80\n"
"s_mul_i32 s76, s76, s80\n"
"s_mul_i32 s77, s77, s80\n"
"s_mul_i32 s78, s78, s80\n"
"s_add_u32 s54, s72, s81\n"
"s_mov_b32 s62, s83\n"
"s_add_u32 s55, s54, s76\n"
"s_add_u32 s63, s62, s87\n"
"s_add_u32 s56, s54, s77\n"
"s_add_u32 s64, s62, s88\n"
"s_add_u32 s57, s55, s77\n"
"s_add_u32 s65, s63, s88\n"
"s_add_u32 s58, s54, s78\n"
"s_add_u32 s66, s62, s89\n"
"s_add_u32 s59, s55, s78\n"
"s_add_u32 s67, s63, s89\n"
"s_add_u32 s60, s56, s78\n"
"s_add_u32 s68, s64, s89\n"
"s_add_u32 s61, s57, s78\n"
"s_add_u32 s69, s65, s89\n"
"v_and_b32 v68, 7, v0\n"
"v_mul_u32_u24 v60, s82, v68\n"
"v_mul_u32_u24 v61, s84, v68\n"
"v_bfe_u32 v68, v0, 3, 1\n"
"v_mad_u32_u24 v60, v68, s73, v60\n"
"v_bfe_u32 v68, v0, 4, 1\n"
"v_mad_u32_u24 v60, v68, s74, v60\n"
"v_bfe_u32 v68, v0, 3, 2\n"
"v_mad_u32_u24 v61, v68, s85, v61\n"
"v_lshrrev_b32 v68, 5, v0\n"
"v_mad_u32_u24 v60, v68, s75, v60\n"
"v_mad_u32_u24 v61, v68, s86, v61\n"
"buffer_load_dwordx4 v[224:227], v60, s[20:23], s54 offen\n"
"buffer_load_dwordx4 v[228:231], v60, s[20:23], s55 offen\n"
"buffer_load_dwordx4 v[232:235], v60, s[20:23], s56 offen\n"
"buffer_load_dwordx4 v[236:239], v60, s[20:23], s57 offen\n"
"buffer_load_dwordx4 v[240:243], v60, s[20:23], s58 offen\n"
"buffer_load_dwordx4 v[244:247], v60, s[20:23], s59 offen\n"
"buffer_load_dwordx4 v[248:251], v60, s[20:23], s60 offen\n"
"buffer_load_dwordx4 v[252:255], v60, s[20:23], s61 offen\n"
".Lfused_block_kernel_s4:\n"
".Lfused_block_kernel_s3:\n"
"s_cmp_eq_u32 s26, 3\n"
"s_cbranch_scc0 .Lfused_block_kernel_s13\n"
"s_cmp_lt_u32 s33, 144\n"
"s_cbranch_scc0 .Lfused_block_kernel_s14\n"
"s_cmp_ge_u32 s33, 72\n"
"s_cselect_b32 s88, 2, 0\n"
"s_cselect_b32 s86, 72, 0\n"
"s_cselect_b32 s20, s44, s40\n"
"s_cselect_b32 s21, s45, s41\n"
"s_mov_b32 s85, 720896\n"
"s_cmov_b32 s85, 724992\n"
"s_and_b32 s21, s21, 0xffff\n"
"s_mov_b32 s22, 663552\n"
"s_mov_b32 s23, 0x20000\n"
"s_sub_u32 s86, s33, s86\n"
"s_lshr_b32 s89, s86, 3\n"
"s_cmp_eq_u32 s89, 8\n"
"s_cselect_b32 s79, 4, 0\n"
"s_add_u32 s79, s79, s88\n"
"s_cmp_eq_u32 s79, 0\n"
"s_cbranch_scc0 .Lfused_block_kernel_s15\n"
"s_mul_i32 s72, s89, 72\n"
"s_mov_b32 s73, 4\n"
"s_mov_b32 s74, 8\n"
"s_mov_b32 s75, 36\n"
"s_mov_b32 s76, 1\n"
"s_mov_b32 s77, 2\n"
"s_mov_b32 s78, 20\n"
".Lfused_block_kernel_s15:\n"
"s_cmp_eq_u32 s79, 4\n"
"s_cbranch_scc0 .Lfused_block_kernel_s16\n"
"s_mov_b32 s72, 16\n"
"s_mov_b32 s73, 72\n"
"s_mov_b32 s74, 144\n"
"s_mov_b32 s75, 288\n"
"s_mov_b32 s76, 1\n"
"s_mov_b32 s77, 2\n"
"s_mov_b32 s78, 36\n"
".Lfused_block_kernel_s16:\n"
"s_cmp_eq_u32 s79, 2\n"
"s_cbranch_scc0 .Lfused_block_kernel_s17\n"
"s_mul_i32 s72, s89, 72\n"
"s_mov_b32 s73, 32\n"
"s_mov_b32 s74, 1\n"
"s_mov_b32 s75, 4\n"
"s_mov_b32 s76, 8\n"
"s_mov_b32 s77, 16\n"
"s_mov_b32 s78, 2\n"
".Lfused_block_kernel_s17:\n"
"s_cmp_eq_u32 s79, 6\n"
"s_cbranch_scc0 .Lfused_block_kernel_s18\n"
"s_mov_b32 s72, 64\n"
"s_mov_b32 s73, 72\n"
"s_mov_b32 s74, 144\n"
"s_mov_b32 s75, 288\n"
"s_mov_b32 s76, 1\n"
"s_mov_b32 s77, 2\n"
"s_mov_b32 s78, 4\n"
".Lfused_block_kernel_s18:\n"
"s_bfe_u32 s80, s86, 65536\n"
"s_mul_i32 s80, s80, s73\n"
"s_add_u32 s72, s72, s80\n"
"s_bfe_u32 s80, s86, 65537\n"
"s_mul_i32 s80, s80, s74\n"
"s_add_u32 s72, s72, s80\n"
"s_bfe_u32 s80, s86, 65538\n"
"s_mul_i32 s80, s80, s75\n"
"s_add_u32 s72, s72, s80\n"
"s_mul_i32 s72, s72, 1152\n"
"v_bfe_u32 v68, v0, 3, 1\n"
"v_mul_u32_u24 v60, s76, v68\n"
"v_bfe_u32 v68, v0, 4, 1\n"
"v_mad_u32_u24 v60, v68, s77, v60\n"
"v_lshrrev_b32 v68, 5, v0\n"
"v_mad_u32_u24 v60, v68, s78, v60\n"
"v_mul_u32_u24 v60, 1152, v60\n"
"v_and_b32 v68, 7, v0\n"
"v_lshl_add_u32 v60, v68, 4, v60\n"
"buffer_load_dwordx4 v[220:223], v60, s[20:23], s72 offen offset:0\n"
"buffer_load_dwordx4 v[224:227], v60, s[20:23], s72 offen offset:128\n"
"buffer_load_dwordx4 v[228:231], v60, s[20:23], s72 offen offset:256\n"
"buffer_load_dwordx4 v[232:235], v60, s[20:23], s72 offen offset:384\n"
"buffer_load_dwordx4 v[236:239], v60, s[20:23], s72 offen offset:512\n"
"buffer_load_dwordx4 v[240:243], v60, s[20:23], s72 offen offset:640\n"
"buffer_load_dwordx4 v[244:247], v60, s[20:23], s72 offen offset:768\n"
"buffer_load_dwordx4 v[248:251], v60, s[20:23], s72 offen offset:896\n"
"buffer_load_dwordx4 v[252:255], v60, s[20:23], s72 offen offset:1024\n"
"s_lshr_b32 s80, s86, 2\n"
"s_lshl_b32 s80, s80, 7\n"
"s_add_u32 s85, s85, s80\n"
"s_and_b32 s80, s86, 3\n"
"s_lshl_b32 s80, s80, 4\n"
"s_add_u32 s85, s85, s80\n"
"v_lshrrev_b32 v69, 3, v0\n"
"v_lshlrev_b32 v69, 1, v69\n"
"v_lshl_add_u32 v69, v68, 6, v69\n"
"v_add_u32 v61, s85, v69\n"
"v_cmp_gt_u32 vcc, 2, v68\n"
"s_nop 1\n"
"v_cndmask_b32 v61, v8, v61, vcc\n"
".Lfused_block_kernel_s14:\n"
".Lfused_block_kernel_s13:\n"
"s_cmp_ge_u32 s26, 2\n"
"s_cbranch_scc0 .Lfused_block_kernel_s19\n"
"s_waitcnt vmcnt(25)\n"
"v_dot2c_f32_bf16 v72, s6, v100\n"
"v_dot2c_f32_bf16 v76, v100, v100\n"
"v_dot2c_f32_bf16 v74, s6, v104\n"
"v_dot2c_f32_bf16 v78, v104, v104\n"
"v_dot2c_f32_bf16 v73, s6, v101\n"
"v_dot2c_f32_bf16 v77, v101, v101\n"
"v_dot2c_f32_bf16 v75, s6, v105\n"
"v_dot2c_f32_bf16 v79, v105, v105\n"
"v_dot2c_f32_bf16 v72, s6, v102\n"
"v_dot2c_f32_bf16 v76, v102, v102\n"
"v_dot2c_f32_bf16 v74, s6, v106\n"
"v_dot2c_f32_bf16 v78, v106, v106\n"
"v_dot2c_f32_bf16 v73, s6, v103\n"
"v_dot2c_f32_bf16 v77, v103, v103\n"
"v_dot2c_f32_bf16 v75, s6, v107\n"
"v_dot2c_f32_bf16 v79, v107, v107\n"
"s_waitcnt vmcnt(23)\n"
"v_dot2c_f32_bf16 v72, s6, v108\n"
"v_dot2c_f32_bf16 v76, v108, v108\n"
"v_dot2c_f32_bf16 v74, s6, v112\n"
"v_dot2c_f32_bf16 v78, v112, v112\n"
"v_dot2c_f32_bf16 v73, s6, v109\n"
"v_dot2c_f32_bf16 v77, v109, v109\n"
"v_dot2c_f32_bf16 v75, s6, v113\n"
"v_dot2c_f32_bf16 v79, v113, v113\n"
"v_dot2c_f32_bf16 v72, s6, v110\n"
"v_dot2c_f32_bf16 v76, v110, v110\n"
"v_dot2c_f32_bf16 v74, s6, v114\n"
"v_dot2c_f32_bf16 v78, v114, v114\n"
"v_dot2c_f32_bf16 v73, s6, v111\n"
"v_dot2c_f32_bf16 v77, v111, v111\n"
"v_dot2c_f32_bf16 v75, s6, v115\n"
"v_dot2c_f32_bf16 v79, v115, v115\n"
"s_waitcnt vmcnt(21)\n"
"v_dot2c_f32_bf16 v72, s6, v116\n"
"v_dot2c_f32_bf16 v76, v116, v116\n"
"v_dot2c_f32_bf16 v74, s6, v120\n"
"v_dot2c_f32_bf16 v78, v120, v120\n"
"v_dot2c_f32_bf16 v73, s6, v117\n"
"v_dot2c_f32_bf16 v77, v117, v117\n"
"v_dot2c_f32_bf16 v75, s6, v121\n"
"v_dot2c_f32_bf16 v79, v121, v121\n"
"v_dot2c_f32_bf16 v72, s6, v118\n"
"v_dot2c_f32_bf16 v76, v118, v118\n"
"v_dot2c_f32_bf16 v74, s6, v122\n"
"v_dot2c_f32_bf16 v78, v122, v122\n"
"v_dot2c_f32_bf16 v73, s6, v119\n"
"v_dot2c_f32_bf16 v77, v119, v119\n"
"v_dot2c_f32_bf16 v75, s6, v123\n"
"v_dot2c_f32_bf16 v79, v123, v123\n"
"s_waitcnt vmcnt(19)\n"
"v_dot2c_f32_bf16 v72, s6, v124\n"
"v_dot2c_f32_bf16 v76, v124, v124\n"
"v_dot2c_f32_bf16 v74, s6, v128\n"
"v_dot2c_f32_bf16 v78, v128, v128\n"
"v_dot2c_f32_bf16 v73, s6, v125\n"
"v_dot2c_f32_bf16 v77, v125, v125\n"
"v_dot2c_f32_bf16 v75, s6, v129\n"
"v_dot2c_f32_bf16 v79, v129, v129\n"
"v_dot2c_f32_bf16 v72, s6, v126\n"
"v_dot2c_f32_bf16 v76, v126, v126\n"
"v_dot2c_f32_bf16 v74, s6, v130\n"
"v_dot2c_f32_bf16 v78, v130, v130\n"
"v_dot2c_f32_bf16 v73, s6, v127\n"
"v_dot2c_f32_bf16 v77, v127, v127\n"
"v_dot2c_f32_bf16 v75, s6, v131\n"
"v_dot2c_f32_bf16 v79, v131, v131\n"
"s_waitcnt vmcnt(17)\n"
"v_dot2c_f32_bf16 v72, s6, v132\n"
"v_dot2c_f32_bf16 v76, v132, v132\n"
"v_dot2c_f32_bf16 v74, s6, v136\n"
"v_dot2c_f32_bf16 v78, v136, v136\n"
"v_dot2c_f32_bf16 v73, s6, v133\n"
"v_dot2c_f32_bf16 v77, v133, v133\n"
"v_dot2c_f32_bf16 v75, s6, v137\n"
"v_dot2c_f32_bf16 v79, v137, v137\n"
"v_dot2c_f32_bf16 v72, s6, v134\n"
"v_dot2c_f32_bf16 v76, v134, v134\n"
"v_dot2c_f32_bf16 v74, s6, v138\n"
"v_dot2c_f32_bf16 v78, v138, v138\n"
"v_dot2c_f32_bf16 v73, s6, v135\n"
"v_dot2c_f32_bf16 v77, v135, v135\n"
"v_dot2c_f32_bf16 v75, s6, v139\n"
"v_dot2c_f32_bf16 v79, v139, v139\n"
"s_waitcnt vmcnt(15)\n"
"v_dot2c_f32_bf16 v72, s6, v140\n"
"v_dot2c_f32_bf16 v76, v140, v140\n"
"v_dot2c_f32_bf16 v74, s6, v144\n"
"v_dot2c_f32_bf16 v78, v144, v144\n"
"v_dot2c_f32_bf16 v73, s6, v141\n"
"v_dot2c_f32_bf16 v77, v141, v141\n"
"v_dot2c_f32_bf16 v75, s6, v145\n"
"v_dot2c_f32_bf16 v79, v145, v145\n"
"v_dot2c_f32_bf16 v72, s6, v142\n"
"v_dot2c_f32_bf16 v76, v142, v142\n"
"v_dot2c_f32_bf16 v74, s6, v146\n"
"v_dot2c_f32_bf16 v78, v146, v146\n"
"v_dot2c_f32_bf16 v73, s6, v143\n"
"v_dot2c_f32_bf16 v77, v143, v143\n"
"v_dot2c_f32_bf16 v75, s6, v147\n"
"v_dot2c_f32_bf16 v79, v147, v147\n"
"s_waitcnt vmcnt(13)\n"
"v_dot2c_f32_bf16 v72, s6, v148\n"
"v_dot2c_f32_bf16 v76, v148, v148\n"
"v_dot2c_f32_bf16 v74, s6, v152\n"
"v_dot2c_f32_bf16 v78, v152, v152\n"
"v_dot2c_f32_bf16 v73, s6, v149\n"
"v_dot2c_f32_bf16 v77, v149, v149\n"
"v_dot2c_f32_bf16 v75, s6, v153\n"
"v_dot2c_f32_bf16 v79, v153, v153\n"
"v_dot2c_f32_bf16 v72, s6, v150\n"
"v_dot2c_f32_bf16 v76, v150, v150\n"
"v_dot2c_f32_bf16 v74, s6, v154\n"
"v_dot2c_f32_bf16 v78, v154, v154\n"
"v_dot2c_f32_bf16 v73, s6, v151\n"
"v_dot2c_f32_bf16 v77, v151, v151\n"
"v_dot2c_f32_bf16 v75, s6, v155\n"
"v_dot2c_f32_bf16 v79, v155, v155\n"
"s_waitcnt vmcnt(11)\n"
"v_dot2c_f32_bf16 v72, s6, v156\n"
"v_dot2c_f32_bf16 v76, v156, v156\n"
"v_dot2c_f32_bf16 v74, s6, v160\n"
"v_dot2c_f32_bf16 v78, v160, v160\n"
"v_dot2c_f32_bf16 v73, s6, v157\n"
"v_dot2c_f32_bf16 v77, v157, v157\n"
"v_dot2c_f32_bf16 v75, s6, v161\n"
"v_dot2c_f32_bf16 v79, v161, v161\n"
"v_dot2c_f32_bf16 v72, s6, v158\n"
"v_dot2c_f32_bf16 v76, v158, v158\n"
"v_dot2c_f32_bf16 v74, s6, v162\n"
"v_dot2c_f32_bf16 v78, v162, v162\n"
"v_dot2c_f32_bf16 v73, s6, v159\n"
"v_dot2c_f32_bf16 v77, v159, v159\n"
"v_dot2c_f32_bf16 v75, s6, v163\n"
"v_dot2c_f32_bf16 v79, v163, v163\n"
"s_waitcnt vmcnt(9)\n"
"v_dot2c_f32_bf16 v72, s6, v164\n"
"v_dot2c_f32_bf16 v76, v164, v164\n"
"v_dot2c_f32_bf16 v74, s6, v168\n"
"v_dot2c_f32_bf16 v78, v168, v168\n"
"v_dot2c_f32_bf16 v73, s6, v165\n"
"v_dot2c_f32_bf16 v77, v165, v165\n"
"v_dot2c_f32_bf16 v75, s6, v169\n"
"v_dot2c_f32_bf16 v79, v169, v169\n"
"v_dot2c_f32_bf16 v72, s6, v166\n"
"v_dot2c_f32_bf16 v76, v166, v166\n"
"v_dot2c_f32_bf16 v74, s6, v170\n"
"v_dot2c_f32_bf16 v78, v170, v170\n"
"v_dot2c_f32_bf16 v73, s6, v167\n"
"v_dot2c_f32_bf16 v77, v167, v167\n"
"v_dot2c_f32_bf16 v75, s6, v171\n"
"v_dot2c_f32_bf16 v79, v171, v171\n"
".Lfused_block_kernel_s19:\n"
"v_add_f32 v72, v72, v73\n"
"s_add_u32 s76, s26, 6\n"
"v_add_f32 v74, v74, v75\n"
"v_add_f32 v76, v76, v77\n"
"s_mov_b32 s91, 73728\n"
"v_add_f32 v78, v78, v79\n"
"v_add_f32_dpp v72, v72, v72 quad_perm:[1,0,3,2] row_mask:0xf bank_mask:0xf\n"
"s_cmp_ge_u32 s76, 9\n"
"s_cselect_b32 s77, 9, 0\n"
"s_cselect_b32 s90, 16, 8\n"
"v_add_f32_dpp v74, v74, v74 quad_perm:[1,0,3,2] row_mask:0xf bank_mask:0xf\n"
"v_add_f32_dpp v76, v76, v76 quad_perm:[1,0,3,2] row_mask:0xf bank_mask:0xf\n"
"v_add_f32_dpp v78, v78, v78 quad_perm:[1,0,3,2] row_mask:0xf bank_mask:0xf\n"
"s_sub_u32 s76, s76, s77\n"
"v_add_f32_dpp v72, v72, v72 quad_perm:[2,3,0,1] row_mask:0xf bank_mask:0xf\n"
"v_add_f32_dpp v74, v74, v74 quad_perm:[2,3,0,1] row_mask:0xf bank_mask:0xf\n"
"s_add_u32 s90, s90, s29\n"
"v_add_f32_dpp v76, v76, v76 quad_perm:[2,3,0,1] row_mask:0xf bank_mask:0xf\n"
"v_add_f32_dpp v78, v78, v78 quad_perm:[2,3,0,1] row_mask:0xf bank_mask:0xf\n"
"v_add_f32_dpp v72, v72, v72 row_half_mirror row_mask:0xf bank_mask:0xf\n"
"s_mul_i32 s90, s90, 144\n"
"v_add_f32_dpp v74, v74, v74 row_half_mirror row_mask:0xf bank_mask:0xf\n"
"v_add_f32_dpp v76, v76, v76 row_half_mirror row_mask:0xf bank_mask:0xf\n"
"s_add_u32 s76, s76, s35\n"
"v_add_f32_dpp v78, v78, v78 row_half_mirror row_mask:0xf bank_mask:0xf\n"
"v_mul_f32 v72, 0x3ae38e39, v72\n"
"v_mul_f32 v74, 0x3ae38e39, v74\n"
"s_cmp_ge_u32 s76, 9\n"
"s_cselect_b32 s77, 9, 0\n"
"v_mul_f32 v76, 0x3ae38e39, v76\n"
"v_mul_f32 v78, 0x3ae38e39, v78\n"
"s_sub_u32 s76, s76, s77\n"
"v_fma_f32 v76, -v72, v72, v76\n"
"v_fma_f32 v78, -v74, v74, v78\n"
"v_max_f32 v76, 0, v76\n"
"s_mul_i32 s76, s76, 221184\n"
"v_max_f32 v78, 0, v78\n"
"v_add_f32 v76, 0x3727c5ac, v76\n"
"s_add_u32 s90, s90, s76\n"
"v_add_f32 v78, 0x3727c5ac, v78\n"
"v_rsq_f32 v76, v76\n"
"v_rsq_f32 v78, v78\n"
"s_mul_i32 s76, s26, 9216\n"
"ds_write_b32 v62, v72 offset:0\n"
"ds_write_b32 v62, v74 offset:32\n"
"s_add_u32 s91, s91, s76\n"
"v_mul_f32_e64 v77, -v72, v76\n"
"v_mul_f32_e64 v79, -v74, v78\n"
"ds_read_b32 v62, v63\n"
"s_add_u32 m0, s91, 0\n"
"s_nop 0\n"
"buffer_load_dwordx4 v12, s[16:19], s90 offen offset:0 lds\n"
"v_lshlrev_b32 v80, 16, v100\n"
"v_and_b32 v81, 0xffff0000, v100\n"
"v_lshlrev_b32 v82, 16, v101\n"
"v_and_b32 v83, 0xffff0000, v101\n"
"v_lshlrev_b32 v84, 16, v102\n"
"v_and_b32 v85, 0xffff0000, v102\n"
"v_lshlrev_b32 v86, 16, v103\n"
"v_and_b32 v87, 0xffff0000, v103\n"
"v_pk_fma_f32 v[80:81], v[80:81], v[76:77], v[76:77] op_sel:[0,0,1] op_sel_hi:[1,0,1]\n"
"v_pk_fma_f32 v[82:83], v[82:83], v[76:77], v[76:77] op_sel:[0,0,1] op_sel_hi:[1,0,1]\n"
"v_pk_fma_f32 v[84:85], v[84:85], v[76:77], v[76:77] op_sel:[0,0,1] op_sel_hi:[1,0,1]\n"
"v_pk_fma_f32 v[86:87], v[86:87], v[76:77], v[76:77] op_sel:[0,0,1] op_sel_hi:[1,0,1]\n"
"v_cvt_pk_bf16_f32 v100, v80, v82\n"
"v_cvt_pk_bf16_f32 v101, v84, v86\n"
"ds_write_b64 v24, v[100:101] offset:0\n"
"v_cvt_pk_bf16_f32 v102, v81, v83\n"
"v_cvt_pk_bf16_f32 v103, v85, v87\n"
"ds_write_b64 v24, v[102:103] offset:1024\n"
"v_lshlrev_b32 v80, 16, v104\n"
"v_and_b32 v81, 0xffff0000, v104\n"
"v_lshlrev_b32 v82, 16, v105\n"
"v_and_b32 v83, 0xffff0000, v105\n"
"v_lshlrev_b32 v84, 16, v106\n"
"v_and_b32 v85, 0xffff0000, v106\n"
"v_lshlrev_b32 v86, 16, v107\n"
"v_and_b32 v87, 0xffff0000, v107\n"
"v_pk_fma_f32 v[80:81], v[80:81], v[78:79], v[78:79] op_sel:[0,0,1] op_sel_hi:[1,0,1]\n"
"v_pk_fma_f32 v[82:83], v[82:83], v[78:79], v[78:79] op_sel:[0,0,1] op_sel_hi:[1,0,1]\n"
"v_pk_fma_f32 v[84:85], v[84:85], v[78:79], v[78:79] op_sel:[0,0,1] op_sel_hi:[1,0,1]\n"
"v_pk_fma_f32 v[86:87], v[86:87], v[78:79], v[78:79] op_sel:[0,0,1] op_sel_hi:[1,0,1]\n"
"v_cvt_pk_bf16_f32 v104, v80, v82\n"
"v_cvt_pk_bf16_f32 v105, v84, v86\n"
"ds_write_b64 v24, v[104:105] offset:128\n"
"v_cvt_pk_bf16_f32 v106, v81, v83\n"
"v_cvt_pk_bf16_f32 v107, v85, v87\n"
"ds_write_b64 v24, v[106:107] offset:1152\n"
"ds_read_b128 a[0:3], v25\n"
"ds_read_b128 a[4:7], v25 offset:1024\n"
"buffer_load_dwordx4 v13, s[16:19], s90 offen offset:1024 lds\n"
"v_lshlrev_b32 v80, 16, v108\n"
"v_and_b32 v81, 0xffff0000, v108\n"
"v_lshlrev_b32 v82, 16, v109\n"
"v_and_b32 v83, 0xffff0000, v109\n"
"v_lshlrev_b32 v84, 16, v110\n"
"v_and_b32 v85, 0xffff0000, v110\n"
"v_lshlrev_b32 v86, 16, v111\n"
"v_and_b32 v87, 0xffff0000, v111\n"
"v_pk_fma_f32 v[80:81], v[80:81], v[76:77], v[76:77] op_sel:[0,0,1] op_sel_hi:[1,0,1]\n"
"v_pk_fma_f32 v[82:83], v[82:83], v[76:77], v[76:77] op_sel:[0,0,1] op_sel_hi:[1,0,1]\n"
"v_pk_fma_f32 v[84:85], v[84:85], v[76:77], v[76:77] op_sel:[0,0,1] op_sel_hi:[1,0,1]\n"
"v_pk_fma_f32 v[86:87], v[86:87], v[76:77], v[76:77] op_sel:[0,0,1] op_sel_hi:[1,0,1]\n"
"v_cvt_pk_bf16_f32 v108, v80, v82\n"
"v_cvt_pk_bf16_f32 v109, v84, v86\n"
"ds_write_b64 v24, v[108:109] offset:0\n"
"v_cvt_pk_bf16_f32 v110, v81, v83\n"
"v_cvt_pk_bf16_f32 v111, v85, v87\n"
"ds_write_b64 v24, v[110:111] offset:1024\n"
"v_lshlrev_b32 v80, 16, v112\n"
"v_and_b32 v81, 0xffff0000, v112\n"
"v_lshlrev_b32 v82, 16, v113\n"
"v_and_b32 v83, 0xffff0000, v113\n"
"v_lshlrev_b32 v84, 16, v114\n"
"v_and_b32 v85, 0xffff0000, v114\n"
"v_lshlrev_b32 v86, 16, v115\n"
"v_and_b32 v87, 0xffff0000, v115\n"
"v_pk_fma_f32 v[80:81], v[80:81], v[78:79], v[78:79] op_sel:[0,0,1] op_sel_hi:[1,0,1]\n"
"v_pk_fma_f32 v[82:83], v[82:83], v[78:79], v[78:79] op_sel:[0,0,1] op_sel_hi:[1,0,1]\n"
"v_pk_fma_f32 v[84:85], v[84:85], v[78:79], v[78:79] op_sel:[0,0,1] op_sel_hi:[1,0,1]\n"
"v_pk_fma_f32 v[86:87], v[86:87], v[78:79], v[78:79] op_sel:[0,0,1] op_sel_hi:[1,0,1]\n"
"v_cvt_pk_bf16_f32 v112, v80, v82\n"
"v_cvt_pk_bf16_f32 v113, v84, v86\n"
"ds_write_b64 v24, v[112:113] offset:128\n"
"v_cvt_pk_bf16_f32 v114, v81, v83\n"
"v_cvt_pk_bf16_f32 v115, v85, v87\n"
"ds_write_b64 v24, v[114:115] offset:1152\n"
"ds_read_b128 a[8:11], v25\n"
"ds_read_b128 a[12:15], v25 offset:1024\n"
"buffer_load_dwordx4 v14, s[16:19], s90 offen offset:2048 lds\n"
"v_lshlrev_b32 v80, 16, v116\n"
"v_and_b32 v81, 0xffff0000, v116\n"
"v_lshlrev_b32 v82, 16, v117\n"
"v_and_b32 v83, 0xffff0000, v117\n"
"v_lshlrev_b32 v84, 16, v118\n"
"v_and_b32 v85, 0xffff0000, v118\n"
"v_lshlrev_b32 v86, 16, v119\n"
"v_and_b32 v87, 0xffff0000, v119\n"
"v_pk_fma_f32 v[80:81], v[80:81], v[76:77], v[76:77] op_sel:[0,0,1] op_sel_hi:[1,0,1]\n"
"v_pk_fma_f32 v[82:83], v[82:83], v[76:77], v[76:77] op_sel:[0,0,1] op_sel_hi:[1,0,1]\n"
"v_pk_fma_f32 v[84:85], v[84:85], v[76:77], v[76:77] op_sel:[0,0,1] op_sel_hi:[1,0,1]\n"
"v_pk_fma_f32 v[86:87], v[86:87], v[76:77], v[76:77] op_sel:[0,0,1] op_sel_hi:[1,0,1]\n"
"v_cvt_pk_bf16_f32 v116, v80, v82\n"
"v_cvt_pk_bf16_f32 v117, v84, v86\n"
"ds_write_b64 v24, v[116:117] offset:0\n"
"v_cvt_pk_bf16_f32 v118, v81, v83\n"
"v_cvt_pk_bf16_f32 v119, v85, v87\n"
"ds_write_b64 v24, v[118:119] offset:1024\n"
"v_lshlrev_b32 v80, 16, v120\n"
"v_and_b32 v81, 0xffff0000, v120\n"
"v_lshlrev_b32 v82, 16, v121\n"
"v_and_b32 v83, 0xffff0000, v121\n"
"v_lshlrev_b32 v84, 16, v122\n"
"v_and_b32 v85, 0xffff0000, v122\n"
"v_lshlrev_b32 v86, 16, v123\n"
"v_and_b32 v87, 0xffff0000, v123\n"
"v_pk_fma_f32 v[80:81], v[80:81], v[78:79], v[78:79] op_sel:[0,0,1] op_sel_hi:[1,0,1]\n"
"v_pk_fma_f32 v[82:83], v[82:83], v[78:79], v[78:79] op_sel:[0,0,1] op_sel_hi:[1,0,1]\n"
"v_pk_fma_f32 v[84:85], v[84:85], v[78:79], v[78:79] op_sel:[0,0,1] op_sel_hi:[1,0,1]\n"
"v_pk_fma_f32 v[86:87], v[86:87], v[78:79], v[78:79] op_sel:[0,0,1] op_sel_hi:[1,0,1]\n"
"v_cvt_pk_bf16_f32 v120, v80, v82\n"
"v_cvt_pk_bf16_f32 v121, v84, v86\n"
"ds_write_b64 v24, v[120:121] offset:128\n"
"v_cvt_pk_bf16_f32 v122, v81, v83\n"
"v_cvt_pk_bf16_f32 v123, v85, v87\n"
"ds_write_b64 v24, v[122:123] offset:1152\n"
"ds_read_b128 a[16:19], v25\n"
"ds_read_b128 a[20:23], v25 offset:1024\n"
"buffer_load_dwordx4 v15, s[16:19], s90 offen offset:3072 lds\n"
"v_lshlrev_b32 v80, 16, v124\n"
"v_and_b32 v81, 0xffff0000, v124\n"
"v_lshlrev_b32 v82, 16, v125\n"
"v_and_b32 v83, 0xffff0000, v125\n"
"v_lshlrev_b32 v84, 16, v126\n"
"v_and_b32 v85, 0xffff0000, v126\n"
"v_lshlrev_b32 v86, 16, v127\n"
"v_and_b32 v87, 0xffff0000, v127\n"
"v_pk_fma_f32 v[80:81], v[80:81], v[76:77], v[76:77] op_sel:[0,0,1] op_sel_hi:[1,0,1]\n"
"v_pk_fma_f32 v[82:83], v[82:83], v[76:77], v[76:77] op_sel:[0,0,1] op_sel_hi:[1,0,1]\n"
"v_pk_fma_f32 v[84:85], v[84:85], v[76:77], v[76:77] op_sel:[0,0,1] op_sel_hi:[1,0,1]\n"
"v_pk_fma_f32 v[86:87], v[86:87], v[76:77], v[76:77] op_sel:[0,0,1] op_sel_hi:[1,0,1]\n"
"v_cvt_pk_bf16_f32 v124, v80, v82\n"
"v_cvt_pk_bf16_f32 v125, v84, v86\n"
"ds_write_b64 v24, v[124:125] offset:0\n"
"v_cvt_pk_bf16_f32 v126, v81, v83\n"
"v_cvt_pk_bf16_f32 v127, v85, v87\n"
"ds_write_b64 v24, v[126:127] offset:1024\n"
"v_lshlrev_b32 v80, 16, v128\n"
"v_and_b32 v81, 0xffff0000, v128\n"
"v_lshlrev_b32 v82, 16, v129\n"
"v_and_b32 v83, 0xffff0000, v129\n"
"v_lshlrev_b32 v84, 16, v130\n"
"v_and_b32 v85, 0xffff0000, v130\n"
"v_lshlrev_b32 v86, 16, v131\n"
"v_and_b32 v87, 0xffff0000, v131\n"
"v_pk_fma_f32 v[80:81], v[80:81], v[78:79], v[78:79] op_sel:[0,0,1] op_sel_hi:[1,0,1]\n"
"v_pk_fma_f32 v[82:83], v[82:83], v[78:79], v[78:79] op_sel:[0,0,1] op_sel_hi:[1,0,1]\n"
"v_pk_fma_f32 v[84:85], v[84:85], v[78:79], v[78:79] op_sel:[0,0,1] op_sel_hi:[1,0,1]\n"
"v_pk_fma_f32 v[86:87], v[86:87], v[78:79], v[78:79] op_sel:[0,0,1] op_sel_hi:[1,0,1]\n"
"v_cvt_pk_bf16_f32 v128, v80, v82\n"
"v_cvt_pk_bf16_f32 v129, v84, v86\n"
"ds_write_b64 v24, v[128:129] offset:128\n"
"v_cvt_pk_bf16_f32 v130, v81, v83\n"
"v_cvt_pk_bf16_f32 v131, v85, v87\n"
"ds_write_b64 v24, v[130:131] offset:1152\n"
"ds_read_b128 a[24:27], v25\n"
"ds_read_b128 a[28:31], v25 offset:1024\n"
"s_add_u32 m0, s91, 4096\n"
"s_nop 0\n"
"buffer_load_dwordx4 v16, s[16:19], s90 offen offset:0 lds\n"
"v_lshlrev_b32 v80, 16, v132\n"
"v_and_b32 v81, 0xffff0000, v132\n"
"v_lshlrev_b32 v82, 16, v133\n"
"v_and_b32 v83, 0xffff0000, v133\n"
"v_lshlrev_b32 v84, 16, v134\n"
"v_and_b32 v85, 0xffff0000, v134\n"
"v_lshlrev_b32 v86, 16, v135\n"
"v_and_b32 v87, 0xffff0000, v135\n"
"v_pk_fma_f32 v[80:81], v[80:81], v[76:77], v[76:77] op_sel:[0,0,1] op_sel_hi:[1,0,1]\n"
"v_pk_fma_f32 v[82:83], v[82:83], v[76:77], v[76:77] op_sel:[0,0,1] op_sel_hi:[1,0,1]\n"
"v_pk_fma_f32 v[84:85], v[84:85], v[76:77], v[76:77] op_sel:[0,0,1] op_sel_hi:[1,0,1]\n"
"v_pk_fma_f32 v[86:87], v[86:87], v[76:77], v[76:77] op_sel:[0,0,1] op_sel_hi:[1,0,1]\n"
"v_cvt_pk_bf16_f32 v132, v80, v82\n"
"v_cvt_pk_bf16_f32 v133, v84, v86\n"
"ds_write_b64 v24, v[132:133] offset:0\n"
"v_cvt_pk_bf16_f32 v134, v81, v83\n"
"v_cvt_pk_bf16_f32 v135, v85, v87\n"
"ds_write_b64 v24, v[134:135] offset:1024\n"
"v_lshlrev_b32 v80, 16, v136\n"
"v_and_b32 v81, 0xffff0000, v136\n"
"v_lshlrev_b32 v82, 16, v137\n"
"v_and_b32 v83, 0xffff0000, v137\n"
"v_lshlrev_b32 v84, 16, v138\n"
"v_and_b32 v85, 0xffff0000, v138\n"
"v_lshlrev_b32 v86, 16, v139\n"
"v_and_b32 v87, 0xffff0000, v139\n"
"v_pk_fma_f32 v[80:81], v[80:81], v[78:79], v[78:79] op_sel:[0,0,1] op_sel_hi:[1,0,1]\n"
"v_pk_fma_f32 v[82:83], v[82:83], v[78:79], v[78:79] op_sel:[0,0,1] op_sel_hi:[1,0,1]\n"
"v_pk_fma_f32 v[84:85], v[84:85], v[78:79], v[78:79] op_sel:[0,0,1] op_sel_hi:[1,0,1]\n"
"v_pk_fma_f32 v[86:87], v[86:87], v[78:79], v[78:79] op_sel:[0,0,1] op_sel_hi:[1,0,1]\n"
"v_cvt_pk_bf16_f32 v136, v80, v82\n"
"v_cvt_pk_bf16_f32 v137, v84, v86\n"
"ds_write_b64 v24, v[136:137] offset:128\n"
"v_cvt_pk_bf16_f32 v138, v81, v83\n"
"v_cvt_pk_bf16_f32 v139, v85, v87\n"
"ds_write_b64 v24, v[138:139] offset:1152\n"
"ds_read_b128 a[32:35], v25\n"
"ds_read_b128 a[36:39], v25 offset:1024\n"
"buffer_load_dwordx4 v17, s[16:19], s90 offen offset:1024 lds\n"
"v_lshlrev_b32 v80, 16, v140\n"
"v_and_b32 v81, 0xffff0000, v140\n"
"v_lshlrev_b32 v82, 16, v141\n"
"v_and_b32 v83, 0xffff0000, v141\n"
"v_lshlrev_b32 v84, 16, v142\n"
"v_and_b32 v85, 0xffff0000, v142\n"
"v_lshlrev_b32 v86, 16, v143\n"
"v_and_b32 v87, 0xffff0000, v143\n"
"v_pk_fma_f32 v[80:81], v[80:81], v[76:77], v[76:77] op_sel:[0,0,1] op_sel_hi:[1,0,1]\n"
"v_pk_fma_f32 v[82:83], v[82:83], v[76:77], v[76:77] op_sel:[0,0,1] op_sel_hi:[1,0,1]\n"
"v_pk_fma_f32 v[84:85], v[84:85], v[76:77], v[76:77] op_sel:[0,0,1] op_sel_hi:[1,0,1]\n"
"v_pk_fma_f32 v[86:87], v[86:87], v[76:77], v[76:77] op_sel:[0,0,1] op_sel_hi:[1,0,1]\n"
"v_cvt_pk_bf16_f32 v140, v80, v82\n"
"v_cvt_pk_bf16_f32 v141, v84, v86\n"
"ds_write_b64 v24, v[140:141] offset:0\n"
"v_cvt_pk_bf16_f32 v142, v81, v83\n"
"v_cvt_pk_bf16_f32 v143, v85, v87\n"
"ds_write_b64 v24, v[142:143] offset:1024\n"
"v_lshlrev_b32 v80, 16, v144\n"
"v_and_b32 v81, 0xffff0000, v144\n"
"v_lshlrev_b32 v82, 16, v145\n"
"v_and_b32 v83, 0xffff0000, v145\n"
"v_lshlrev_b32 v84, 16, v146\n"
"v_and_b32 v85, 0xffff0000, v146\n"
"v_lshlrev_b32 v86, 16, v147\n"
"v_and_b32 v87, 0xffff0000, v147\n"
"v_pk_fma_f32 v[80:81], v[80:81], v[78:79], v[78:79] op_sel:[0,0,1] op_sel_hi:[1,0,1]\n"
"v_pk_fma_f32 v[82:83], v[82:83], v[78:79], v[78:79] op_sel:[0,0,1] op_sel_hi:[1,0,1]\n"
"v_pk_fma_f32 v[84:85], v[84:85], v[78:79], v[78:79] op_sel:[0,0,1] op_sel_hi:[1,0,1]\n"
"v_pk_fma_f32 v[86:87], v[86:87], v[78:79], v[78:79] op_sel:[0,0,1] op_sel_hi:[1,0,1]\n"
"v_cvt_pk_bf16_f32 v144, v80, v82\n"
"v_cvt_pk_bf16_f32 v145, v84, v86\n"
"ds_write_b64 v24, v[144:145] offset:128\n"
"v_cvt_pk_bf16_f32 v146, v81, v83\n"
"v_cvt_pk_bf16_f32 v147, v85, v87\n"
"ds_write_b64 v24, v[146:147] offset:1152\n"
"ds_read_b128 a[40:43], v25\n"
"ds_read_b128 a[44:47], v25 offset:1024\n"
"buffer_load_dwordx4 v18, s[16:19], s90 offen offset:2048 lds\n"
"v_lshlrev_b32 v80, 16, v148\n"
"v_and_b32 v81, 0xffff0000, v148\n"
"v_lshlrev_b32 v82, 16, v149\n"
"v_and_b32 v83, 0xffff0000, v149\n"
"v_lshlrev_b32 v84, 16, v150\n"
"v_and_b32 v85, 0xffff0000, v150\n"
"v_lshlrev_b32 v86, 16, v151\n"
"v_and_b32 v87, 0xffff0000, v151\n"
"v_pk_fma_f32 v[80:81], v[80:81], v[76:77], v[76:77] op_sel:[0,0,1] op_sel_hi:[1,0,1]\n"
"v_pk_fma_f32 v[82:83], v[82:83], v[76:77], v[76:77] op_sel:[0,0,1] op_sel_hi:[1,0,1]\n"
"v_pk_fma_f32 v[84:85], v[84:85], v[76:77], v[76:77] op_sel:[0,0,1] op_sel_hi:[1,0,1]\n"
"v_pk_fma_f32 v[86:87], v[86:87], v[76:77], v[76:77] op_sel:[0,0,1] op_sel_hi:[1,0,1]\n"
"v_cvt_pk_bf16_f32 v148, v80, v82\n"
"v_cvt_pk_bf16_f32 v149, v84, v86\n"
"ds_write_b64 v24, v[148:149] offset:0\n"
"v_cvt_pk_bf16_f32 v150, v81, v83\n"
"v_cvt_pk_bf16_f32 v151, v85, v87\n"
"ds_write_b64 v24, v[150:151] offset:1024\n"
"v_lshlrev_b32 v80, 16, v152\n"
"v_and_b32 v81, 0xffff0000, v152\n"
"v_lshlrev_b32 v82, 16, v153\n"
"v_and_b32 v83, 0xffff0000, v153\n"
"v_lshlrev_b32 v84, 16, v154\n"
"v_and_b32 v85, 0xffff0000, v154\n"
"v_lshlrev_b32 v86, 16, v155\n"
"v_and_b32 v87, 0xffff0000, v155\n"
"v_pk_fma_f32 v[80:81], v[80:81], v[78:79], v[78:79] op_sel:[0,0,1] op_sel_hi:[1,0,1]\n"
"v_pk_fma_f32 v[82:83], v[82:83], v[78:79], v[78:79] op_sel:[0,0,1] op_sel_hi:[1,0,1]\n"
"v_pk_fma_f32 v[84:85], v[84:85], v[78:79], v[78:79] op_sel:[0,0,1] op_sel_hi:[1,0,1]\n"
"v_pk_fma_f32 v[86:87], v[86:87], v[78:79], v[78:79] op_sel:[0,0,1] op_sel_hi:[1,0,1]\n"
"v_cvt_pk_bf16_f32 v152, v80, v82\n"
"v_cvt_pk_bf16_f32 v153, v84, v86\n"
"ds_write_b64 v24, v[152:153] offset:128\n"
"v_cvt_pk_bf16_f32 v154, v81, v83\n"
"v_cvt_pk_bf16_f32 v155, v85, v87\n"
"ds_write_b64 v24, v[154:155] offset:1152\n"
"ds_read_b128 a[48:51], v25\n"
"ds_read_b128 a[52:55], v25 offset:1024\n"
"buffer_load_dwordx4 v19, s[16:19], s90 offen offset:3072 lds\n"
"v_lshlrev_b32 v80, 16, v156\n"
"v_and_b32 v81, 0xffff0000, v156\n"
"v_lshlrev_b32 v82, 16, v157\n"
"v_and_b32 v83, 0xffff0000, v157\n"
"v_lshlrev_b32 v84, 16, v158\n"
"v_and_b32 v85, 0xffff0000, v158\n"
"v_lshlrev_b32 v86, 16, v159\n"
"v_and_b32 v87, 0xffff0000, v159\n"
"v_pk_fma_f32 v[80:81], v[80:81], v[76:77], v[76:77] op_sel:[0,0,1] op_sel_hi:[1,0,1]\n"
"v_pk_fma_f32 v[82:83], v[82:83], v[76:77], v[76:77] op_sel:[0,0,1] op_sel_hi:[1,0,1]\n"
"v_pk_fma_f32 v[84:85], v[84:85], v[76:77], v[76:77] op_sel:[0,0,1] op_sel_hi:[1,0,1]\n"
"v_pk_fma_f32 v[86:87], v[86:87], v[76:77], v[76:77] op_sel:[0,0,1] op_sel_hi:[1,0,1]\n"
"v_cvt_pk_bf16_f32 v156, v80, v82\n"
"v_cvt_pk_bf16_f32 v157, v84, v86\n"
"ds_write_b64 v24, v[156:157] offset:0\n"
"v_cvt_pk_bf16_f32 v158, v81, v83\n"
"v_cvt_pk_bf16_f32 v159, v85, v87\n"
"ds_write_b64 v24, v[158:159] offset:1024\n"
"v_lshlrev_b32 v80, 16, v160\n"
"v_and_b32 v81, 0xffff0000, v160\n"
"v_lshlrev_b32 v82, 16, v161\n"
"v_and_b32 v83, 0xffff0000, v161\n"
"v_lshlrev_b32 v84, 16, v162\n"
"v_and_b32 v85, 0xffff0000, v162\n"
"v_lshlrev_b32 v86, 16, v163\n"
"v_and_b32 v87, 0xffff0000, v163\n"
"v_pk_fma_f32 v[80:81], v[80:81], v[78:79], v[78:79] op_sel:[0,0,1] op_sel_hi:[1,0,1]\n"
"v_pk_fma_f32 v[82:83], v[82:83], v[78:79], v[78:79] op_sel:[0,0,1] op_sel_hi:[1,0,1]\n"
"v_pk_fma_f32 v[84:85], v[84:85], v[78:79], v[78:79] op_sel:[0,0,1] op_sel_hi:[1,0,1]\n"
"v_pk_fma_f32 v[86:87], v[86:87], v[78:79], v[78:79] op_sel:[0,0,1] op_sel_hi:[1,0,1]\n"
"v_cvt_pk_bf16_f32 v160, v80, v82\n"
"v_cvt_pk_bf16_f32 v161, v84, v86\n"
"ds_write_b64 v24, v[160:161] offset:128\n"
"v_cvt_pk_bf16_f32 v162, v81, v83\n"
"v_cvt_pk_bf16_f32 v163, v85, v87\n"
"ds_write_b64 v24, v[162:163] offset:1152\n"
"ds_read_b128 a[56:59], v25\n"
"ds_read_b128 a[60:63], v25 offset:1024\n"
"s_add_u32 m0, s91, 8192\n"
"s_nop 0\n"
"buffer_load_dwordx4 v20, s[16:19], s90 offen offset:0 lds\n"
"v_lshlrev_b32 v80, 16, v164\n"
"v_and_b32 v81, 0xffff0000, v164\n"
"v_lshlrev_b32 v82, 16, v165\n"
"v_and_b32 v83, 0xffff0000, v165\n"
"v_lshlrev_b32 v84, 16, v166\n"
"v_and_b32 v85, 0xffff0000, v166\n"
"v_lshlrev_b32 v86, 16, v167\n"
"v_and_b32 v87, 0xffff0000, v167\n"
"v_pk_fma_f32 v[80:81], v[80:81], v[76:77], v[76:77] op_sel:[0,0,1] op_sel_hi:[1,0,1]\n"
"v_pk_fma_f32 v[82:83], v[82:83], v[76:77], v[76:77] op_sel:[0,0,1] op_sel_hi:[1,0,1]\n"
"v_pk_fma_f32 v[84:85], v[84:85], v[76:77], v[76:77] op_sel:[0,0,1] op_sel_hi:[1,0,1]\n"
"v_pk_fma_f32 v[86:87], v[86:87], v[76:77], v[76:77] op_sel:[0,0,1] op_sel_hi:[1,0,1]\n"
"v_cvt_pk_bf16_f32 v164, v80, v82\n"
"v_cvt_pk_bf16_f32 v165, v84, v86\n"
"ds_write_b64 v24, v[164:165] offset:0\n"
"v_cvt_pk_bf16_f32 v166, v81, v83\n"
"v_cvt_pk_bf16_f32 v167, v85, v87\n"
"ds_write_b64 v24, v[166:167] offset:1024\n"
"v_lshlrev_b32 v80, 16, v168\n"
"v_and_b32 v81, 0xffff0000, v168\n"
"v_lshlrev_b32 v82, 16, v169\n"
"v_and_b32 v83, 0xffff0000, v169\n"
"v_lshlrev_b32 v84, 16, v170\n"
"v_and_b32 v85, 0xffff0000, v170\n"
"v_lshlrev_b32 v86, 16, v171\n"
"v_and_b32 v87, 0xffff0000, v171\n"
"v_pk_fma_f32 v[80:81], v[80:81], v[78:79], v[78:79] op_sel:[0,0,1] op_sel_hi:[1,0,1]\n"
"v_pk_fma_f32 v[82:83], v[82:83], v[78:79], v[78:79] op_sel:[0,0,1] op_sel_hi:[1,0,1]\n"
"v_pk_fma_f32 v[84:85], v[84:85], v[78:79], v[78:79] op_sel:[0,0,1] op_sel_hi:[1,0,1]\n"
"v_pk_fma_f32 v[86:87], v[86:87], v[78:79], v[78:79] op_sel:[0,0,1] op_sel_hi:[1,0,1]\n"
"v_cvt_pk_bf16_f32 v168, v80, v82\n"
"v_cvt_pk_bf16_f32 v169, v84, v86\n"
"ds_write_b64 v24, v[168:169] offset:128\n"
"v_cvt_pk_bf16_f32 v170, v81, v83\n"
"v_cvt_pk_bf16_f32 v171, v85, v87\n"
"ds_write_b64 v24, v[170:171] offset:1152\n"
"ds_read_b128 a[64:67], v25\n"
"ds_read_b128 a[68:71], v25 offset:1024\n"
"s_waitcnt vmcnt(9) lgkmcnt(0)\n"
"s_add_u32 s24, s24, 1\n"
"s_addc_u32 s25, s25, 0\n"
"s_barrier\n"
"ds_read_b64_tr_b16 v[100:101], v4 offset:0\n"
"ds_read_b64_tr_b16 v[102:103], v4 offset:4608\n"
"ds_read_b64_tr_b16 v[104:105], v4 offset:40\n"
"ds_read_b64_tr_b16 v[106:107], v4 offset:4648\n"
"ds_read_b64_tr_b16 v[108:109], v4 offset:72\n"
"ds_read_b64_tr_b16 v[110:111], v4 offset:4680\n"
"ds_read_b64_tr_b16 v[112:113], v4 offset:112\n"
"ds_read_b64_tr_b16 v[114:115], v4 offset:4720\n"
"ds_read_b64_tr_b16 v[116:117], v6 offset:0\n"
"ds_read_b64_tr_b16 v[118:119], v6 offset:4608\n"
"s_waitcnt lgkmcnt(8)\n"
"v_mfma_f32_16x16x32_bf16 v[140:143], a[0:3], v[100:103], 0\n"
"s_waitcnt lgkmcnt(6)\n"
"ds_read_b64_tr_b16 v[120:121], v4 offset:144\n"
"ds_read_b64_tr_b16 v[122:123], v4 offset:4752\n"
"v_mfma_f32_16x16x32_bf16 v[144:147], a[0:3], v[104:107], 0\n"
"s_waitcnt lgkmcnt(6)\n"
"ds_read_b64_tr_b16 v[124:125], v4 offset:184\n"
"ds_read_b64_tr_b16 v[126:127], v4 offset:4792\n"
"v_mfma_f32_16x16x32_bf16 v[148:151], a[0:3], v[108:111], 0\n"
"s_waitcnt lgkmcnt(6)\n"
"ds_read_b64_tr_b16 v[128:129], v4 offset:216\n"
"ds_read_b64_tr_b16 v[130:131], v4 offset:4824\n"
"v_mfma_f32_16x16x32_bf16 v[152:155], a[0:3], v[112:115], 0\n"
"s_waitcnt lgkmcnt(6)\n"
"ds_read_b64_tr_b16 v[132:133], v4 offset:256\n"
"ds_read_b64_tr_b16 v[134:135], v4 offset:4864\n"
"v_mfma_f32_16x16x32_bf16 v[156:159], a[0:3], v[116:119], 0\n"
"s_waitcnt lgkmcnt(6)\n"
"ds_read_b64_tr_b16 v[136:137], v6 offset:144\n"
"ds_read_b64_tr_b16 v[138:139], v6 offset:4752\n"
"v_mfma_f32_16x16x32_bf16 v[140:143], a[4:7], v[120:123], v[140:143]\n"
"s_waitcnt lgkmcnt(6)\n"
"ds_read_b64_tr_b16 v[100:101], v4 offset:9216\n"
"ds_read_b64_tr_b16 v[102:103], v4 offset:13824\n"
"v_mfma_f32_16x16x32_bf16 v[144:147], a[4:7], v[124:127], v[144:147]\n"
"s_waitcnt lgkmcnt(6)\n"
"ds_read_b64_tr_b16 v[104:105], v4 offset:9256\n"
"ds_read_b64_tr_b16 v[106:107], v4 offset:13864\n"
"v_mfma_f32_16x16x32_bf16 v[148:151], a[4:7], v[128:131], v[148:151]\n"
"s_waitcnt lgkmcnt(6)\n"
"ds_read_b64_tr_b16 v[108:109], v4 offset:9288\n"
"ds_read_b64_tr_b16 v[110:111], v4 offset:13896\n"
"v_mfma_f32_16x16x32_bf16 v[152:155], a[4:7], v[132:135], v[152:155]\n"
"s_waitcnt lgkmcnt(6)\n"
"ds_read_b64_tr_b16 v[112:113], v4 offset:9328\n"
"ds_read_b64_tr_b16 v[114:115], v4 offset:13936\n"
"v_mfma_f32_16x16x32_bf16 v[156:159], a[4:7], v[136:139], v[156:159]\n"
"s_waitcnt lgkmcnt(6)\n"
"ds_read_b64_tr_b16 v[116:117], v6 offset:9216\n"
"ds_read_b64_tr_b16 v[118:119], v6 offset:13824\n"
"v_mfma_f32_16x16x32_bf16 v[140:143], a[8:11], v[100:103], v[140:143]\n"
"s_waitcnt lgkmcnt(6)\n"
"ds_read_b64_tr_b16 v[120:121], v4 offset:9360\n"
"ds_read_b64_tr_b16 v[122:123], v4 offset:13968\n"
"v_mfma_f32_16x16x32_bf16 v[144:147], a[8:11], v[104:107], v[144:147]\n"
"s_waitcnt lgkmcnt(6)\n"
"ds_read_b64_tr_b16 v[124:125], v4 offset:9400\n"
"ds_read_b64_tr_b16 v[126:127], v4 offset:14008\n"
"v_mfma_f32_16x16x32_bf16 v[148:151], a[8:11], v[108:111], v[148:151]\n"
"s_waitcnt lgkmcnt(6)\n"
"ds_read_b64_tr_b16 v[128:129], v4 offset:9432\n"
"ds_read_b64_tr_b16 v[130:131], v4 offset:14040\n"
"v_mfma_f32_16x16x32_bf16 v[152:155], a[8:11], v[112:115], v[152:155]\n"
"s_waitcnt lgkmcnt(6)\n"
"ds_read_b64_tr_b16 v[132:133], v4 offset:9472\n"
"ds_read_b64_tr_b16 v[134:135], v4 offset:14080\n"
"v_mfma_f32_16x16x32_bf16 v[156:159], a[8:11], v[116:119], v[156:159]\n"
"s_waitcnt lgkmcnt(6)\n"
"ds_read_b64_tr_b16 v[136:137], v6 offset:9360\n"
"ds_read_b64_tr_b16 v[138:139], v6 offset:13968\n"
"v_mfma_f32_16x16x32_bf16 v[140:143], a[12:15], v[120:123], v[140:143]\n"
"s_waitcnt lgkmcnt(6)\n"
"v_mfma_f32_16x16x32_bf16 v[144:147], a[12:15], v[124:127], v[144:147]\n"
"s_waitcnt lgkmcnt(4)\n"
"v_mfma_f32_16x16x32_bf16 v[148:151], a[12:15], v[128:131], v[148:151]\n"
"s_waitcnt lgkmcnt(2)\n"
"v_mfma_f32_16x16x32_bf16 v[152:155], a[12:15], v[132:135], v[152:155]\n"
"s_waitcnt lgkmcnt(0)\n"
"v_mfma_f32_16x16x32_bf16 v[156:159], a[12:15], v[136:139], v[156:159]\n"
"s_add_u32 s76, s26, 10\n"
"s_mov_b32 s91, 0\n"
"s_mul_i32 s90, s29, 144\n"
"s_add_u32 s90, s90, 2304\n"
"s_sub_u32 s76, s76, 9\n"
"s_add_u32 s76, s76, s35\n"
"s_cmp_ge_u32 s76, 9\n"
"s_cselect_b32 s77, 9, 0\n"
"s_sub_u32 s76, s76, s77\n"
"s_mul_i32 s76, s76, 221184\n"
"s_add_u32 s90, s90, s76\n"
"s_mul_i32 s76, s26, 9216\n"
"s_add_u32 s91, s91, s76\n"
"s_waitcnt vmcnt(9)\n"
"s_barrier\n"
"ds_read_b64_tr_b16 v[100:101], v4 offset:36864\n"
"ds_read_b64_tr_b16 v[102:103], v4 offset:41472\n"
"ds_read_b64_tr_b16 v[104:105], v4 offset:36904\n"
"ds_read_b64_tr_b16 v[106:107], v4 offset:41512\n"
"ds_read_b64_tr_b16 v[108:109], v4 offset:36936\n"
"ds_read_b64_tr_b16 v[110:111], v4 offset:41544\n"
"ds_read_b64_tr_b16 v[112:113], v4 offset:36976\n"
"ds_read_b64_tr_b16 v[114:115], v4 offset:41584\n"
"ds_read_b64_tr_b16 v[116:117], v6 offset:36864\n"
"ds_read_b64_tr_b16 v[118:119], v6 offset:41472\n"
"s_waitcnt lgkmcnt(8)\n"
"v_mfma_f32_16x16x32_bf16 v[140:143], a[16:19], v[100:103], v[140:143]\n"
"s_waitcnt lgkmcnt(6)\n"
"ds_read_b64_tr_b16 v[120:121], v4 offset:37008\n"
"ds_read_b64_tr_b16 v[122:123], v4 offset:41616\n"
"v_mfma_f32_16x16x32_bf16 v[144:147], a[16:19], v[104:107], v[144:147]\n"
"s_waitcnt lgkmcnt(6)\n"
"ds_read_b64_tr_b16 v[124:125], v4 offset:37048\n"
"ds_read_b64_tr_b16 v[126:127], v4 offset:41656\n"
"v_mfma_f32_16x16x32_bf16 v[148:151], a[16:19], v[108:111], v[148:151]\n"
"s_waitcnt lgkmcnt(6)\n"
"s_add_u32 m0, s91, 0\n"
"s_nop 0\n"
"buffer_load_dwordx4 v12, s[16:19], s90 offen offset:0 lds\n"
"ds_read_b64_tr_b16 v[128:129], v4 offset:37080\n"
"ds_read_b64_tr_b16 v[130:131], v4 offset:41688\n"
"v_mfma_f32_16x16x32_bf16 v[152:155], a[16:19], v[112:115], v[152:155]\n"
"s_waitcnt lgkmcnt(6)\n"
"ds_read_b64_tr_b16 v[132:133], v4 offset:37120\n"
"ds_read_b64_tr_b16 v[134:135], v4 offset:41728\n"
"v_mfma_f32_16x16x32_bf16 v[156:159], a[16:19], v[116:119], v[156:159]\n"
"s_waitcnt lgkmcnt(6)\n"
"ds_read_b64_tr_b16 v[136:137], v6 offset:37008\n"
"ds_read_b64_tr_b16 v[138:139], v6 offset:41616\n"
"v_mfma_f32_16x16x32_bf16 v[140:143], a[20:23], v[120:123], v[140:143]\n"
"s_waitcnt lgkmcnt(6)\n"
"buffer_load_dwordx4 v13, s[16:19], s90 offen offset:1024 lds\n"
"ds_read_b64_tr_b16 v[100:101], v4 offset:46080\n"
"ds_read_b64_tr_b16 v[102:103], v4 offset:50688\n"
"v_mfma_f32_16x16x32_bf16 v[144:147], a[20:23], v[124:127], v[144:147]\n"
"s_waitcnt lgkmcnt(6)\n"
"ds_read_b64_tr_b16 v[104:105], v4 offset:46120\n"
"ds_read_b64_tr_b16 v[106:107], v4 offset:50728\n"
"v_mfma_f32_16x16x32_bf16 v[148:151], a[20:23], v[128:131], v[148:151]\n"
"s_waitcnt lgkmcnt(6)\n"
"ds_read_b64_tr_b16 v[108:109], v4 offset:46152\n"
"ds_read_b64_tr_b16 v[110:111], v4 offset:50760\n"
"v_mfma_f32_16x16x32_bf16 v[152:155], a[20:23], v[132:135], v[152:155]\n"
"s_waitcnt lgkmcnt(6)\n"
"buffer_load_dwordx4 v14, s[16:19], s90 offen offset:2048 lds\n"
"ds_read_b64_tr_b16 v[112:113], v4 offset:46192\n"
"ds_read_b64_tr_b16 v[114:115], v4 offset:50800\n"
"v_mfma_f32_16x16x32_bf16 v[156:159], a[20:23], v[136:139], v[156:159]\n"
"s_waitcnt lgkmcnt(6)\n"
"ds_read_b64_tr_b16 v[116:117], v6 offset:46080\n"
"ds_read_b64_tr_b16 v[118:119], v6 offset:50688\n"
"v_mfma_f32_16x16x32_bf16 v[140:143], a[24:27], v[100:103], v[140:143]\n"
"s_waitcnt lgkmcnt(6)\n"
"ds_read_b64_tr_b16 v[120:121], v4 offset:46224\n"
"ds_read_b64_tr_b16 v[122:123], v4 offset:50832\n"
"v_mfma_f32_16x16x32_bf16 v[144:147], a[24:27], v[104:107], v[144:147]\n"
"s_waitcnt lgkmcnt(6)\n"
"buffer_load_dwordx4 v15, s[16:19], s90 offen offset:3072 lds\n"
"ds_read_b64_tr_b16 v[124:125], v4 offset:46264\n"
"ds_read_b64_tr_b16 v[126:127], v4 offset:50872\n"
"v_mfma_f32_16x16x32_bf16 v[148:151], a[24:27], v[108:111], v[148:151]\n"
"s_waitcnt lgkmcnt(6)\n"
"ds_read_b64_tr_b16 v[128:129], v4 offset:46296\n"
"ds_read_b64_tr_b16 v[130:131], v4 offset:50904\n"
"v_mfma_f32_16x16x32_bf16 v[152:155], a[24:27], v[112:115], v[152:155]\n"
"s_waitcnt lgkmcnt(6)\n"
"ds_read_b64_tr_b16 v[132:133], v4 offset:46336\n"
"ds_read_b64_tr_b16 v[134:135], v4 offset:50944\n"
"v_mfma_f32_16x16x32_bf16 v[156:159], a[24:27], v[116:119], v[156:159]\n"
"s_waitcnt lgkmcnt(6)\n"
"s_add_u32 m0, s91, 4096\n"
"s_nop 0\n"
"buffer_load_dwordx4 v16, s[16:19], s90 offen offset:0 lds\n"
"ds_read_b64_tr_b16 v[136:137], v6 offset:46224\n"
"ds_read_b64_tr_b16 v[138:139], v6 offset:50832\n"
"v_mfma_f32_16x16x32_bf16 v[140:143], a[28:31], v[120:123], v[140:143]\n"
"s_waitcnt lgkmcnt(6)\n"
"ds_read_b64_tr_b16 v[100:101], v4 offset:55296\n"
"ds_read_b64_tr_b16 v[102:103], v4 offset:59904\n"
"v_mfma_f32_16x16x32_bf16 v[144:147], a[28:31], v[124:127], v[144:147]\n"
"s_waitcnt lgkmcnt(6)\n"
"ds_read_b64_tr_b16 v[104:105], v4 offset:55336\n"
"ds_read_b64_tr_b16 v[106:107], v4 offset:59944\n"
"v_mfma_f32_16x16x32_bf16 v[148:151], a[28:31], v[128:131], v[148:151]\n"
"s_waitcnt lgkmcnt(6)\n"
"buffer_load_dwordx4 v17, s[16:19], s90 offen offset:1024 lds\n"
"ds_read_b64_tr_b16 v[108:109], v4 offset:55368\n"
"ds_read_b64_tr_b16 v[110:111], v4 offset:59976\n"
"v_mfma_f32_16x16x32_bf16 v[152:155], a[28:31], v[132:135], v[152:155]\n"
"s_waitcnt lgkmcnt(6)\n"
"ds_read_b64_tr_b16 v[112:113], v4 offset:55408\n"
"ds_read_b64_tr_b16 v[114:115], v4 offset:60016\n"
"v_mfma_f32_16x16x32_bf16 v[156:159], a[28:31], v[136:139], v[156:159]\n"
"s_waitcnt lgkmcnt(6)\n"
"ds_read_b64_tr_b16 v[116:117], v6 offset:55296\n"
"ds_read_b64_tr_b16 v[118:119], v6 offset:59904\n"
"v_mfma_f32_16x16x32_bf16 v[140:143], a[32:35], v[100:103], v[140:143]\n"
"s_waitcnt lgkmcnt(6)\n"
"buffer_load_dwordx4 v18, s[16:19], s90 offen offset:2048 lds\n"
"ds_read_b64_tr_b16 v[120:121], v4 offset:55440\n"
"ds_read_b64_tr_b16 v[122:123], v4 offset:60048\n"
"v_mfma_f32_16x16x32_bf16 v[144:147], a[32:35], v[104:107], v[144:147]\n"
"s_waitcnt lgkmcnt(6)\n"
"ds_read_b64_tr_b16 v[124:125], v4 offset:55480\n"
"ds_read_b64_tr_b16 v[126:127], v4 offset:60088\n"
"v_mfma_f32_16x16x32_bf16 v[148:151], a[32:35], v[108:111], v[148:151]\n"
"s_waitcnt lgkmcnt(6)\n"
"ds_read_b64_tr_b16 v[128:129], v4 offset:55512\n"
"ds_read_b64_tr_b16 v[130:131], v4 offset:60120\n"
"v_mfma_f32_16x16x32_bf16 v[152:155], a[32:35], v[112:115], v[152:155]\n"
"s_waitcnt lgkmcnt(6)\n"
"buffer_load_dwordx4 v19, s[16:19], s90 offen offset:3072 lds\n"
"ds_read_b64_tr_b16 v[132:133], v4 offset:55552\n"
"ds_read_b64_tr_b16 v[134:135], v4 offset:60160\n"
"v_mfma_f32_16x16x32_bf16 v[156:159], a[32:35], v[116:119], v[156:159]\n"
"s_waitcnt lgkmcnt(6)\n"
"ds_read_b64_tr_b16 v[136:137], v6 offset:55440\n"
"ds_read_b64_tr_b16 v[138:139], v6 offset:60048\n"
"v_mfma_f32_16x16x32_bf16 v[140:143], a[36:39], v[120:123], v[140:143]\n"
"s_waitcnt lgkmcnt(6)\n"
"ds_read_b64_tr_b16 v[100:101], v4 offset:64512\n"
"ds_read_b64_tr_b16 v[102:103], v5 offset:3584\n"
"v_mfma_f32_16x16x32_bf16 v[144:147], a[36:39], v[124:127], v[144:147]\n"
"s_waitcnt lgkmcnt(6)\n"
"s_add_u32 m0, s91, 8192\n"
"s_nop 0\n"
"buffer_load_dwordx4 v20, s[16:19], s90 offen offset:0 lds\n"
"ds_read_b64_tr_b16 v[104:105], v4 offset:64552\n"
"ds_read_b64_tr_b16 v[106:107], v5 offset:3624\n"
"v_mfma_f32_16x16x32_bf16 v[148:151], a[36:39], v[128:131], v[148:151]\n"
"s_waitcnt lgkmcnt(6)\n"
"ds_read_b64_tr_b16 v[108:109], v4 offset:64584\n"
"ds_read_b64_tr_b16 v[110:111], v5 offset:3656\n"
"v_mfma_f32_16x16x32_bf16 v[152:155], a[36:39], v[132:135], v[152:155]\n"
"s_waitcnt lgkmcnt(6)\n"
"ds_read_b64_tr_b16 v[112:113], v4 offset:64624\n"
"ds_read_b64_tr_b16 v[114:115], v5 offset:3696\n"
"v_mfma_f32_16x16x32_bf16 v[156:159], a[36:39], v[136:139], v[156:159]\n"
"s_waitcnt lgkmcnt(6)\n"
"ds_read_b64_tr_b16 v[116:117], v6 offset:64512\n"
"ds_read_b64_tr_b16 v[118:119], v7 offset:3584\n"
"v_mfma_f32_16x16x32_bf16 v[140:143], a[40:43], v[100:103], v[140:143]\n"
"s_waitcnt lgkmcnt(6)\n"
"ds_read_b64_tr_b16 v[120:121], v4 offset:64656\n"
"ds_read_b64_tr_b16 v[122:123], v5 offset:3728\n"
"v_mfma_f32_16x16x32_bf16 v[144:147], a[40:43], v[104:107], v[144:147]\n"
"s_waitcnt lgkmcnt(6)\n"
"ds_read_b64_tr_b16 v[124:125], v4 offset:64696\n"
"ds_read_b64_tr_b16 v[126:127], v5 offset:3768\n"
"v_mfma_f32_16x16x32_bf16 v[148:151], a[40:43], v[108:111], v[148:151]\n"
"s_waitcnt lgkmcnt(6)\n"
"ds_read_b64_tr_b16 v[128:129], v4 offset:64728\n"
"ds_read_b64_tr_b16 v[130:131], v5 offset:3800\n"
"v_mfma_f32_16x16x32_bf16 v[152:155], a[40:43], v[112:115], v[152:155]\n"
"s_waitcnt lgkmcnt(6)\n"
"ds_read_b64_tr_b16 v[132:133], v4 offset:64768\n"
"ds_read_b64_tr_b16 v[134:135], v5 offset:3840\n"
"v_mfma_f32_16x16x32_bf16 v[156:159], a[40:43], v[116:119], v[156:159]\n"
"s_waitcnt lgkmcnt(6)\n"
"ds_read_b64_tr_b16 v[136:137], v6 offset:64656\n"
"ds_read_b64_tr_b16 v[138:139], v7 offset:3728\n"
"v_mfma_f32_16x16x32_bf16 v[140:143], a[44:47], v[120:123], v[140:143]\n"
"s_waitcnt lgkmcnt(6)\n"
"v_mfma_f32_16x16x32_bf16 v[144:147], a[44:47], v[124:127], v[144:147]\n"
"s_waitcnt lgkmcnt(4)\n"
"v_mfma_f32_16x16x32_bf16 v[148:151], a[44:47], v[128:131], v[148:151]\n"
"s_waitcnt lgkmcnt(2)\n"
"v_mfma_f32_16x16x32_bf16 v[152:155], a[44:47], v[132:135], v[152:155]\n"
"s_waitcnt lgkmcnt(0)\n"
"v_mfma_f32_16x16x32_bf16 v[156:159], a[44:47], v[136:139], v[156:159]\n"
"v_mov_b32 v22, s24\n"
"v_mov_b32 v23, s25\n"
"s_cmp_eq_u32 s26, 2\n"
"s_cbranch_scc0 .Lfused_block_kernel_s20\n"
"s_cmp_eq_u32 s70, 1\n"
"s_cbranch_scc0 .Lfused_block_kernel_s21\n"
"s_waitcnt vmcnt(18)\n"
"v_perm_b32 v72, v228, v224, s4\n"
"v_perm_b32 v73, v236, v232, s4\n"
"v_perm_b32 v74, v244, v240, s4\n"
"v_perm_b32 v75, v252, v248, s4\n"
"buffer_store_dwordx4 v[72:75], v61, s[8:11], s62 offen sc1\n"
"v_perm_b32 v76, v228, v224, s5\n"
"v_perm_b32 v77, v236, v232, s5\n"
"v_perm_b32 v78, v244, v240, s5\n"
"v_perm_b32 v79, v252, v248, s5\n"
"buffer_store_dwordx4 v[76:79], v61, s[8:11], s63 offen sc1\n"
"v_perm_b32 v72, v229, v225, s4\n"
"v_perm_b32 v73, v237, v233, s4\n"
"v_perm_b32 v74, v245, v241, s4\n"
"v_perm_b32 v75, v253, v249, s4\n"
"buffer_store_dwordx4 v[72:75], v61, s[8:11], s64 offen sc1\n"
"v_perm_b32 v76, v229, v225, s5\n"
"v_perm_b32 v77, v237, v233, s5\n"
"v_perm_b32 v78, v245, v241, s5\n"
"v_perm_b32 v79, v253, v249, s5\n"
"buffer_store_dwordx4 v[76:79], v61, s[8:11], s65 offen sc1\n"
"v_perm_b32 v72, v230, v226, s4\n"
"v_perm_b32 v73, v238, v234, s4\n"
"v_perm_b32 v74, v246, v242, s4\n"
"v_perm_b32 v75, v254, v250, s4\n"
"buffer_store_dwordx4 v[72:75], v61, s[8:11], s66 offen sc1\n"
"v_perm_b32 v76, v230, v226, s5\n"
"v_perm_b32 v77, v238, v234, s5\n"
"v_perm_b32 v78, v246, v242, s5\n"
"v_perm_b32 v79, v254, v250, s5\n"
"buffer_store_dwordx4 v[76:79], v61, s[8:11], s67 offen sc1\n"
"v_perm_b32 v72, v231, v227, s4\n"
"v_perm_b32 v73, v239, v235, s4\n"
"v_perm_b32 v74, v247, v243, s4\n"
"v_perm_b32 v75, v255, v251, s4\n"
"buffer_store_dwordx4 v[72:75], v61, s[8:11], s68 offen sc1\n"
"v_perm_b32 v76, v231, v227, s5\n"
"v_perm_b32 v77, v239, v235, s5\n"
"v_perm_b32 v78, v247, v243, s5\n"
"v_perm_b32 v79, v255, v251, s5\n"
"buffer_store_dwordx4 v[76:79], v61, s[8:11], s69 offen sc1\n"
".Lfused_block_kernel_s21:\n"
".Lfused_block_kernel_s20:\n"
"s_cmp_eq_u32 s26, 3\n"
"s_cbranch_scc0 .Lfused_block_kernel_s22\n"
"s_cmp_lt_u32 s33, 144\n"
"s_cbranch_scc0 .Lfused_block_kernel_s23\n"
"v_mov_b32 v72, 0\n"
"v_mov_b32 v73, 0\n"
"s_waitcnt vmcnt(18)\n"
"v_dot2c_f32_bf16 v72, s6, v220\n"
"v_dot2c_f32_bf16 v73, s6, v221\n"
"v_dot2c_f32_bf16 v72, s6, v222\n"
"v_dot2c_f32_bf16 v73, s6, v223\n"
"v_dot2c_f32_bf16 v72, s6, v224\n"
"v_dot2c_f32_bf16 v73, s6, v225\n"
"v_dot2c_f32_bf16 v72, s6, v226\n"
"v_dot2c_f32_bf16 v73, s6, v227\n"
"v_dot2c_f32_bf16 v72, s6, v228\n"
"v_dot2c_f32_bf16 v73, s6, v229\n"
"v_dot2c_f32_bf16 v72, s6, v230\n"
"v_dot2c_f32_bf16 v73, s6, v231\n"
"v_dot2c_f32_bf16 v72, s6, v232\n"
"v_dot2c_f32_bf16 v73, s6, v233\n"
"v_dot2c_f32_bf16 v72, s6, v234\n"
"v_dot2c_f32_bf16 v73, s6, v235\n"
"v_dot2c_f32_bf16 v72, s6, v236\n"
"v_dot2c_f32_bf16 v73, s6, v237\n"
"v_dot2c_f32_bf16 v72, s6, v238\n"
"v_dot2c_f32_bf16 v73, s6, v239\n"
"v_dot2c_f32_bf16 v72, s6, v240\n"
"v_dot2c_f32_bf16 v73, s6, v241\n"
"v_dot2c_f32_bf16 v72, s6, v242\n"
"v_dot2c_f32_bf16 v73, s6, v243\n"
"v_dot2c_f32_bf16 v72, s6, v244\n"
"v_dot2c_f32_bf16 v73, s6, v245\n"
"v_dot2c_f32_bf16 v72, s6, v246\n"
"v_dot2c_f32_bf16 v73, s6, v247\n"
"v_dot2c_f32_bf16 v72, s6, v248\n"
"v_dot2c_f32_bf16 v73, s6, v249\n"
"v_dot2c_f32_bf16 v72, s6, v250\n"
"v_dot2c_f32_bf16 v73, s6, v251\n"
"v_dot2c_f32_bf16 v72, s6, v252\n"
"v_dot2c_f32_bf16 v73, s6, v253\n"
"v_dot2c_f32_bf16 v72, s6, v254\n"
"v_dot2c_f32_bf16 v73, s6, v255\n"
"s_nop 2\n"
"v_add_f32 v72, v72, v73\n"
"s_nop 1\n"
"v_add_f32_dpp v72, v72, v72 quad_perm:[1,0,3,2] row_mask:0xf bank_mask:0xf\n"
"s_nop 1\n"
"v_add_f32_dpp v72, v72, v72 quad_perm:[2,3,0,1] row_mask:0xf bank_mask:0xf\n"
"s_nop 1\n"
"v_add_f32_dpp v72, v72, v72 row_half_mirror row_mask:0xf bank_mask:0xf\n"
"v_mul_f32 v72, 0x3ae38e39, v72\n"
"v_cvt_pk_bf16_f32 v73, v72, v72\n"
"v_and_b32 v74, 0xffff0000, v73\n"
"v_sub_f32 v74, v72, v74\n"
"v_cvt_pk_bf16_f32 v74, v74, v74\n"
"v_and_b32 v75, 1, v0\n"
"v_cmp_eq_u32 vcc, 1, v75\n"
"s_nop 1\n"
"v_cndmask_b32 v73, v73, v74, vcc\n"
"buffer_store_short v73, v61, s[8:11], 0 offen sc1\n"
".Lfused_block_kernel_s23:\n"
".Lfused_block_kernel_s22:\n"
"s_add_u32 s76, s26, 14\n"
"s_mov_b32 s91, 36864\n"
"s_mul_i32 s90, s29, 144\n"
"s_add_u32 s90, s90, 2304\n"
"s_sub_u32 s76, s76, 9\n"
"s_add_u32 s76, s76, s35\n"
"s_cmp_ge_u32 s76, 9\n"
"s_cselect_b32 s77, 9, 0\n"
"s_sub_u32 s76, s76, s77\n"
"s_mul_i32 s76, s76, 221184\n"
"s_add_u32 s90, s90, s76\n"
"s_mul_i32 s76, s26, 9216\n"
"s_add_u32 s91, s91, s76\n"
"s_waitcnt vmcnt(9)\n"
"s_barrier\n"
"ds_read_b64_tr_b16 v[100:101], v5 offset:8192\n"
"ds_read_b64_tr_b16 v[102:103], v5 offset:12800\n"
"ds_read_b64_tr_b16 v[104:105], v5 offset:8232\n"
"ds_read_b64_tr_b16 v[106:107], v5 offset:12840\n"
"ds_read_b64_tr_b16 v[108:109], v5 offset:8264\n"
"ds_read_b64_tr_b16 v[110:111], v5 offset:12872\n"
"ds_read_b64_tr_b16 v[112:113], v5 offset:8304\n"
"ds_read_b64_tr_b16 v[114:115], v5 offset:12912\n"
"ds_read_b64_tr_b16 v[116:117], v7 offset:8192\n"
"ds_read_b64_tr_b16 v[118:119], v7 offset:12800\n"
"s_waitcnt lgkmcnt(8)\n"
"v_mfma_f32_16x16x32_bf16 v[140:143], a[48:51], v[100:103], v[140:143]\n"
"s_waitcnt lgkmcnt(6)\n"
"ds_read_b64_tr_b16 v[120:121], v5 offset:8336\n"
"ds_read_b64_tr_b16 v[122:123], v5 offset:12944\n"
"v_mfma_f32_16x16x32_bf16 v[144:147], a[48:51], v[104:107], v[144:147]\n"
"s_waitcnt lgkmcnt(6)\n"
"ds_read_b64_tr_b16 v[124:125], v5 offset:8376\n"
"ds_read_b64_tr_b16 v[126:127], v5 offset:12984\n"
"v_mfma_f32_16x16x32_bf16 v[148:151], a[48:51], v[108:111], v[148:151]\n"
"s_waitcnt lgkmcnt(6)\n"
"s_add_u32 m0, s91, 0\n"
"s_nop 0\n"
"buffer_load_dwordx4 v12, s[16:19], s90 offen offset:0 lds\n"
"ds_read_b64_tr_b16 v[128:129], v5 offset:8408\n"
"ds_read_b64_tr_b16 v[130:131], v5 offset:13016\n"
"v_mfma_f32_16x16x32_bf16 v[152:155], a[48:51], v[112:115], v[152:155]\n"
"s_waitcnt lgkmcnt(6)\n"
"ds_read_b64_tr_b16 v[132:133], v5 offset:8448\n"
"ds_read_b64_tr_b16 v[134:135], v5 offset:13056\n"
"v_mfma_f32_16x16x32_bf16 v[156:159], a[48:51], v[116:119], v[156:159]\n"
"s_waitcnt lgkmcnt(6)\n"
"ds_read_b64_tr_b16 v[136:137], v7 offset:8336\n"
"ds_read_b64_tr_b16 v[138:139], v7 offset:12944\n"
"v_mfma_f32_16x16x32_bf16 v[140:143], a[52:55], v[120:123], v[140:143]\n"
"s_waitcnt lgkmcnt(6)\n"
"buffer_load_dwordx4 v13, s[16:19], s90 offen offset:1024 lds\n"
"ds_read_b64_tr_b16 v[100:101], v5 offset:17408\n"
"ds_read_b64_tr_b16 v[102:103], v5 offset:22016\n"
"v_mfma_f32_16x16x32_bf16 v[144:147], a[52:55], v[124:127], v[144:147]\n"
"s_waitcnt lgkmcnt(6)\n"
"ds_read_b64_tr_b16 v[104:105], v5 offset:17448\n"
"ds_read_b64_tr_b16 v[106:107], v5 offset:22056\n"
"v_mfma_f32_16x16x32_bf16 v[148:151], a[52:55], v[128:131], v[148:151]\n"
"s_waitcnt lgkmcnt(6)\n"
"ds_read_b64_tr_b16 v[108:109], v5 offset:17480\n"
"ds_read_b64_tr_b16 v[110:111], v5 offset:22088\n"
"v_mfma_f32_16x16x32_bf16 v[152:155], a[52:55], v[132:135], v[152:155]\n"
"s_waitcnt lgkmcnt(6)\n"
"buffer_load_dwordx4 v14, s[16:19], s90 offen offset:2048 lds\n"
"ds_read_b64_tr_b16 v[112:113], v5 offset:17520\n"
"ds_read_b64_tr_b16 v[114:115], v5 offset:22128\n"
"v_mfma_f32_16x16x32_bf16 v[156:159], a[52:55], v[136:139], v[156:159]\n"
"s_waitcnt lgkmcnt(6)\n"
"ds_read_b64_tr_b16 v[116:117], v7 offset:17408\n"
"ds_read_b64_tr_b16 v[118:119], v7 offset:22016\n"
"v_mfma_f32_16x16x32_bf16 v[140:143], a[56:59], v[100:103], v[140:143]\n"
"s_waitcnt lgkmcnt(6)\n"
"ds_read_b64_tr_b16 v[120:121], v5 offset:17552\n"
"ds_read_b64_tr_b16 v[122:123], v5 offset:22160\n"
"v_mfma_f32_16x16x32_bf16 v[144:147], a[56:59], v[104:107], v[144:147]\n"
"s_waitcnt lgkmcnt(6)\n"
"buffer_load_dwordx4 v15, s[16:19], s90 offen offset:3072 lds\n"
"ds_read_b64_tr_b16 v[124:125], v5 offset:17592\n"
"ds_read_b64_tr_b16 v[126:127], v5 offset:22200\n"
"v_mfma_f32_16x16x32_bf16 v[148:151], a[56:59], v[108:111], v[148:151]\n"
"s_waitcnt lgkmcnt(6)\n"
"ds_read_b64_tr_b16 v[128:129], v5 offset:17624\n"
"ds_read_b64_tr_b16 v[130:131], v5 offset:22232\n"
"v_mfma_f32_16x16x32_bf16 v[152:155], a[56:59], v[112:115], v[152:155]\n"
"s_waitcnt lgkmcnt(6)\n"
"ds_read_b64_tr_b16 v[132:133], v5 offset:17664\n"
"ds_read_b64_tr_b16 v[134:135], v5 offset:22272\n"
"v_mfma_f32_16x16x32_bf16 v[156:159], a[56:59], v[116:119], v[156:159]\n"
"s_waitcnt lgkmcnt(6)\n"
"s_add_u32 m0, s91, 4096\n"
"s_nop 0\n"
"buffer_load_dwordx4 v16, s[16:19], s90 offen offset:0 lds\n"
"ds_read_b64_tr_b16 v[136:137], v7 offset:17552\n"
"ds_read_b64_tr_b16 v[138:139], v7 offset:22160\n"
"v_mfma_f32_16x16x32_bf16 v[140:143], a[60:63], v[120:123], v[140:143]\n"
"s_waitcnt lgkmcnt(6)\n"
"ds_read_b64_tr_b16 v[100:101], v5 offset:26624\n"
"ds_read_b64_tr_b16 v[102:103], v5 offset:31232\n"
"v_mfma_f32_16x16x32_bf16 v[144:147], a[60:63], v[124:127], v[144:147]\n"
"s_waitcnt lgkmcnt(6)\n"
"ds_read_b64_tr_b16 v[104:105], v5 offset:26664\n"
"ds_read_b64_tr_b16 v[106:107], v5 offset:31272\n"
"v_mfma_f32_16x16x32_bf16 v[148:151], a[60:63], v[128:131], v[148:151]\n"
"s_waitcnt lgkmcnt(6)\n"
"buffer_load_dwordx4 v17, s[16:19], s90 offen offset:1024 lds\n"
"ds_read_b64_tr_b16 v[108:109], v5 offset:26696\n"
"ds_read_b64_tr_b16 v[110:111], v5 offset:31304\n"
"v_mfma_f32_16x16x32_bf16 v[152:155], a[60:63], v[132:135], v[152:155]\n"
"s_waitcnt lgkmcnt(6)\n"
"ds_read_b64_tr_b16 v[112:113], v5 offset:26736\n"
"ds_read_b64_tr_b16 v[114:115], v5 offset:31344\n"
"v_mfma_f32_16x16x32_bf16 v[156:159], a[60:63], v[136:139], v[156:159]\n"
"s_waitcnt lgkmcnt(6)\n"
"ds_read_b64_tr_b16 v[116:117], v7 offset:26624\n"
"ds_read_b64_tr_b16 v[118:119], v7 offset:31232\n"
"v_mfma_f32_16x16x32_bf16 v[140:143], a[64:67], v[100:103], v[140:143]\n"
"s_waitcnt lgkmcnt(6)\n"
"buffer_load_dwordx4 v18, s[16:19], s90 offen offset:2048 lds\n"
"ds_read_b64_tr_b16 v[120:121], v5 offset:26768\n"
"ds_read_b64_tr_b16 v[122:123], v5 offset:31376\n"
"v_mfma_f32_16x16x32_bf16 v[144:147], a[64:67], v[104:107], v[144:147]\n"
"s_waitcnt lgkmcnt(6)\n"
"ds_read_b64_tr_b16 v[124:125], v5 offset:26808\n"
"ds_read_b64_tr_b16 v[126:127], v5 offset:31416\n"
"v_mfma_f32_16x16x32_bf16 v[148:151], a[64:67], v[108:111], v[148:151]\n"
"s_waitcnt lgkmcnt(6)\n"
"ds_read_b64_tr_b16 v[128:129], v5 offset:26840\n"
"ds_read_b64_tr_b16 v[130:131], v5 offset:31448\n"
"v_mfma_f32_16x16x32_bf16 v[152:155], a[64:67], v[112:115], v[152:155]\n"
"s_waitcnt lgkmcnt(6)\n"
"buffer_load_dwordx4 v19, s[16:19], s90 offen offset:3072 lds\n"
"ds_read_b64_tr_b16 v[132:133], v5 offset:26880\n"
"ds_read_b64_tr_b16 v[134:135], v5 offset:31488\n"
"v_mfma_f32_16x16x32_bf16 v[156:159], a[64:67], v[116:119], v[156:159]\n"
"s_waitcnt lgkmcnt(6)\n"
"ds_read_b64_tr_b16 v[136:137], v7 offset:26768\n"
"ds_read_b64_tr_b16 v[138:139], v7 offset:31376\n"
"v_mfma_f32_16x16x32_bf16 v[140:143], a[68:71], v[120:123], v[140:143]\n"
"s_waitcnt lgkmcnt(6)\n"
"ds_read_b64_tr_b16 v[100:101], v5 offset:35840\n"
"ds_read_b64_tr_b16 v[102:103], v5 offset:40448\n"
"v_mfma_f32_16x16x32_bf16 v[144:147], a[68:71], v[124:127], v[144:147]\n"
"s_waitcnt lgkmcnt(6)\n"
"s_add_u32 m0, s91, 8192\n"
"s_nop 0\n"
"buffer_load_dwordx4 v20, s[16:19], s90 offen offset:0 lds\n"
"ds_read_b64_tr_b16 v[104:105], v5 offset:35880\n"
"ds_read_b64_tr_b16 v[106:107], v5 offset:40488\n"
"v_mfma_f32_16x16x32_bf16 v[148:151], a[68:71], v[128:131], v[148:151]\n"
"s_waitcnt lgkmcnt(6)\n"
"ds_read_b64_tr_b16 v[108:109], v5 offset:35912\n"
"ds_read_b64_tr_b16 v[110:111], v5 offset:40520\n"
"v_mfma_f32_16x16x32_bf16 v[152:155], a[68:71], v[132:135], v[152:155]\n"
"s_waitcnt lgkmcnt(6)\n"
"ds_read_b64_tr_b16 v[112:113], v5 offset:35952\n"
"ds_read_b64_tr_b16 v[114:115], v5 offset:40560\n"
"v_mfma_f32_16x16x32_bf16 v[156:159], a[68:71], v[136:139], v[156:159]\n"
"s_waitcnt lgkmcnt(6)\n"
"ds_read_b64_tr_b16 v[116:117], v7 offset:35840\n"
"ds_read_b64_tr_b16 v[118:119], v7 offset:40448\n"
"v_mfma_f32_16x16x32_bf16 v[160:163], a[0:3], v[100:103], 0\n"
"s_waitcnt lgkmcnt(6)\n"
"ds_read_b64_tr_b16 v[120:121], v5 offset:35984\n"
"ds_read_b64_tr_b16 v[122:123], v5 offset:40592\n"
"v_mfma_f32_16x16x32_bf16 v[164:167], a[0:3], v[104:107], 0\n"
"s_waitcnt lgkmcnt(6)\n"
"ds_read_b64_tr_b16 v[124:125], v5 offset:36024\n"
"ds_read_b64_tr_b16 v[126:127], v5 offset:40632\n"
"v_mfma_f32_16x16x32_bf16 v[168:171], a[0:3], v[108:111], 0\n"
"s_waitcnt lgkmcnt(6)\n"
"ds_read_b64_tr_b16 v[128:129], v5 offset:36056\n"
"ds_read_b64_tr_b16 v[130:131], v5 offset:40664\n"
"v_mfma_f32_16x16x32_bf16 v[172:175], a[0:3], v[112:115], 0\n"
"s_waitcnt lgkmcnt(6)\n"
"ds_read_b64_tr_b16 v[132:133], v5 offset:36096\n"
"ds_read_b64_tr_b16 v[134:135], v5 offset:40704\n"
"v_mfma_f32_16x16x32_bf16 v[176:179], a[0:3], v[116:119], 0\n"
"s_waitcnt lgkmcnt(6)\n"
"ds_read_b64_tr_b16 v[136:137], v7 offset:35984\n"
"ds_read_b64_tr_b16 v[138:139], v7 offset:40592\n"
"v_mfma_f32_16x16x32_bf16 v[160:163], a[4:7], v[120:123], v[160:163]\n"
"s_waitcnt lgkmcnt(6)\n"
"v_mfma_f32_16x16x32_bf16 v[164:167], a[4:7], v[124:127], v[164:167]\n"
"s_waitcnt lgkmcnt(4)\n"
"v_mfma_f32_16x16x32_bf16 v[168:171], a[4:7], v[128:131], v[168:171]\n"
"s_waitcnt lgkmcnt(2)\n"
"v_mfma_f32_16x16x32_bf16 v[172:175], a[4:7], v[132:135], v[172:175]\n"
"s_waitcnt lgkmcnt(0)\n"
"v_mfma_f32_16x16x32_bf16 v[176:179], a[4:7], v[136:139], v[176:179]\n"
"s_mov_b32 s82, 0x3fb8aa3b\n"
"s_mov_b32 s83, s82\n"
"s_mov_b32 s88, 0x358637bd\n"
"s_mov_b32 s89, s88\n"
"v_pk_mul_f32 v[72:73], v[140:141], s[82:83]\n"
"v_pk_mul_f32 v[74:75], v[142:143], s[82:83]\n"
"v_max_f32 v140, 0, v140\n"
"v_max_f32 v141, 0, v141\n"
"v_max_f32 v142, 0, v142\n"
"v_max_f32 v143, 0, v143\n"
"v_exp_f32_e64 v72, v72 clamp\n"
"v_exp_f32_e64 v73, v73 clamp\n"
"v_exp_f32_e64 v74, v74 clamp\n"
"v_exp_f32_e64 v75, v75 clamp\n"
"v_pk_add_f32 v[140:141], v[140:141], s[88:89]\n"
"v_pk_add_f32 v[142:143], v[142:143], s[88:89]\n"
"v_pk_add_f32 v[140:141], v[140:141], v[72:73]\n"
"v_pk_add_f32 v[142:143], v[142:143], v[74:75]\n"
"v_cvt_pk_bf16_f32 v140, v140, v141\n"
"v_cvt_pk_bf16_f32 v141, v142, v143\n"
"ds_write_b64 v26, v[140:141] offset:0\n"
"v_pk_mul_f32 v[72:73], v[144:145], s[82:83]\n"
"v_pk_mul_f32 v[74:75], v[146:147], s[82:83]\n"
"v_max_f32 v144, 0, v144\n"
"v_max_f32 v145, 0, v145\n"
"v_max_f32 v146, 0, v146\n"
"v_max_f32 v147, 0, v147\n"
"v_exp_f32_e64 v72, v72 clamp\n"
"v_exp_f32_e64 v73, v73 clamp\n"
"v_exp_f32_e64 v74, v74 clamp\n"
"v_exp_f32_e64 v75, v75 clamp\n"
"v_pk_add_f32 v[144:145], v[144:145], s[88:89]\n"
"v_pk_add_f32 v[146:147], v[146:147], s[88:89]\n"
"v_pk_add_f32 v[144:145], v[144:145], v[72:73]\n"
"v_pk_add_f32 v[146:147], v[146:147], v[74:75]\n"
"v_cvt_pk_bf16_f32 v144, v144, v145\n"
"v_cvt_pk_bf16_f32 v145, v146, v147\n"
"ds_write_b64 v26, v[144:145] offset:2048\n"
"v_pk_mul_f32 v[72:73], v[148:149], s[82:83]\n"
"v_pk_mul_f32 v[74:75], v[150:151], s[82:83]\n"
"v_max_f32 v148, 0, v148\n"
"v_max_f32 v149, 0, v149\n"
"v_max_f32 v150, 0, v150\n"
"v_max_f32 v151, 0, v151\n"
"v_exp_f32_e64 v72, v72 clamp\n"
"v_exp_f32_e64 v73, v73 clamp\n"
"v_exp_f32_e64 v74, v74 clamp\n"
"v_exp_f32_e64 v75, v75 clamp\n"
"v_pk_add_f32 v[148:149], v[148:149], s[88:89]\n"
"v_pk_add_f32 v[150:151], v[150:151], s[88:89]\n"
"v_pk_add_f32 v[148:149], v[148:149], v[72:73]\n"
"v_pk_add_f32 v[150:151], v[150:151], v[74:75]\n"
"v_cvt_pk_bf16_f32 v148, v148, v149\n"
"v_cvt_pk_bf16_f32 v149, v150, v151\n"
"ds_write_b64 v26, v[148:149] offset:4096\n"
"v_pk_mul_f32 v[72:73], v[152:153], s[82:83]\n"
"v_pk_mul_f32 v[74:75], v[154:155], s[82:83]\n"
"v_max_f32 v152, 0, v152\n"
"v_max_f32 v153, 0, v153\n"
"v_max_f32 v154, 0, v154\n"
"v_max_f32 v155, 0, v155\n"
"v_exp_f32_e64 v72, v72 clamp\n"
"v_exp_f32_e64 v73, v73 clamp\n"
"v_exp_f32_e64 v74, v74 clamp\n"
"v_exp_f32_e64 v75, v75 clamp\n"
"v_pk_add_f32 v[152:153], v[152:153], s[88:89]\n"
"v_pk_add_f32 v[154:155], v[154:155], s[88:89]\n"
"v_pk_add_f32 v[152:153], v[152:153], v[72:73]\n"
"v_pk_add_f32 v[154:155], v[154:155], v[74:75]\n"
"v_cvt_pk_bf16_f32 v152, v152, v153\n"
"v_cvt_pk_bf16_f32 v153, v154, v155\n"
"ds_write_b64 v26, v[152:153] offset:6144\n"
"v_pk_mul_f32 v[72:73], v[156:157], s[82:83]\n"
"v_pk_mul_f32 v[74:75], v[158:159], s[82:83]\n"
"v_max_f32 v156, 0, v156\n"
"v_max_f32 v157, 0, v157\n"
"v_max_f32 v158, 0, v158\n"
"v_max_f32 v159, 0, v159\n"
"v_exp_f32_e64 v72, v72 clamp\n"
"v_exp_f32_e64 v73, v73 clamp\n"
"v_exp_f32_e64 v74, v74 clamp\n"
"v_exp_f32_e64 v75, v75 clamp\n"
"v_pk_add_f32 v[156:157], v[156:157], s[88:89]\n"
"v_pk_add_f32 v[158:159], v[158:159], s[88:89]\n"
"v_pk_add_f32 v[156:157], v[156:157], v[72:73]\n"
"v_pk_add_f32 v[158:159], v[158:159], v[74:75]\n"
"v_cvt_pk_bf16_f32 v156, v156, v157\n"
"v_cvt_pk_bf16_f32 v157, v158, v159\n"
"ds_write_b64 v26, v[156:157] offset:8192\n"
"s_add_u32 s76, s26, 18\n"
"s_mov_b32 s91, 73728\n"
"s_mul_i32 s90, s29, 144\n"
"s_add_u32 s90, s90, 0\n"
"s_sub_u32 s76, s76, 18\n"
"s_add_u32 s76, s76, s35\n"
"s_cmp_ge_u32 s76, 9\n"
"s_cselect_b32 s77, 9, 0\n"
"s_sub_u32 s76, s76, s77\n"
"s_mul_i32 s76, s76, 221184\n"
"s_add_u32 s90, s90, s76\n"
"s_mul_i32 s76, s26, 9216\n"
"s_add_u32 s91, s91, s76\n"
"s_waitcnt vmcnt(9) lgkmcnt(0)\n"
"s_barrier\n"
"ds_read_b128 v[72:75], v31 offset:0\n"
"ds_read_b128 v[80:83], v32 offset:8192\n"
"ds_read_b128 v[76:79], v31 offset:1024\n"
"ds_read_b128 v[84:87], v32 offset:9216\n"
"ds_read_b64_tr_b16 v[100:101], v4 offset:0\n"
"ds_read_b64_tr_b16 v[102:103], v4 offset:4608\n"
"ds_read_b64_tr_b16 v[104:105], v4 offset:40\n"
"ds_read_b64_tr_b16 v[106:107], v4 offset:4648\n"
"ds_read_b64_tr_b16 v[108:109], v4 offset:72\n"
"ds_read_b64_tr_b16 v[110:111], v4 offset:4680\n"
"ds_read_b64_tr_b16 v[112:113], v4 offset:112\n"
"ds_read_b64_tr_b16 v[114:115], v4 offset:4720\n"
"ds_read_b64_tr_b16 v[116:117], v6 offset:0\n"
"ds_read_b64_tr_b16 v[118:119], v6 offset:4608\n"
"s_waitcnt lgkmcnt(8)\n"
"v_mfma_f32_16x16x32_bf16 v[160:163], a[8:11], v[100:103], v[160:163]\n"
"s_waitcnt lgkmcnt(6)\n"
"ds_read_b64_tr_b16 v[120:121], v4 offset:144\n"
"ds_read_b64_tr_b16 v[122:123], v4 offset:4752\n"
"v_mfma_f32_16x16x32_bf16 v[164:167], a[8:11], v[104:107], v[164:167]\n"
"s_waitcnt lgkmcnt(6)\n"
"ds_read_b64_tr_b16 v[124:125], v4 offset:184\n"
"ds_read_b64_tr_b16 v[126:127], v4 offset:4792\n"
"v_mfma_f32_16x16x32_bf16 v[168:171], a[8:11], v[108:111], v[168:171]\n"
"s_waitcnt lgkmcnt(6)\n"
"s_add_u32 m0, s91, 0\n"
"s_nop 0\n"
"buffer_load_dwordx4 v12, s[16:19], s90 offen offset:0 lds\n"
"ds_read_b64_tr_b16 v[128:129], v4 offset:216\n"
"ds_read_b64_tr_b16 v[130:131], v4 offset:4824\n"
"v_mfma_f32_16x16x32_bf16 v[172:175], a[8:11], v[112:115], v[172:175]\n"
"s_waitcnt lgkmcnt(6)\n"
"ds_read_b64_tr_b16 v[132:133], v4 offset:256\n"
"ds_read_b64_tr_b16 v[134:135], v4 offset:4864\n"
"v_mfma_f32_16x16x32_bf16 v[176:179], a[8:11], v[116:119], v[176:179]\n"
"s_waitcnt lgkmcnt(6)\n"
"ds_read_b64_tr_b16 v[136:137], v6 offset:144\n"
"ds_read_b64_tr_b16 v[138:139], v6 offset:4752\n"
"v_mfma_f32_16x16x32_bf16 v[160:163], a[12:15], v[120:123], v[160:163]\n"
"s_waitcnt lgkmcnt(6)\n"
"buffer_load_dwordx4 v13, s[16:19], s90 offen offset:1024 lds\n"
"ds_read_b64_tr_b16 v[100:101], v4 offset:9216\n"
"ds_read_b64_tr_b16 v[102:103], v4 offset:13824\n"
"v_mfma_f32_16x16x32_bf16 v[164:167], a[12:15], v[124:127], v[164:167]\n"
"s_waitcnt lgkmcnt(6)\n"
"ds_read_b64_tr_b16 v[104:105], v4 offset:9256\n"
"ds_read_b64_tr_b16 v[106:107], v4 offset:13864\n"
"v_mfma_f32_16x16x32_bf16 v[168:171], a[12:15], v[128:131], v[168:171]\n"
"s_waitcnt lgkmcnt(6)\n"
"ds_read_b64_tr_b16 v[108:109], v4 offset:9288\n"
"ds_read_b64_tr_b16 v[110:111], v4 offset:13896\n"
"v_mfma_f32_16x16x32_bf16 v[172:175], a[12:15], v[132:135], v[172:175]\n"
"s_waitcnt lgkmcnt(6)\n"
"buffer_load_dwordx4 v14, s[16:19], s90 offen offset:2048 lds\n"
"ds_read_b64_tr_b16 v[112:113], v4 offset:9328\n"
"ds_read_b64_tr_b16 v[114:115], v4 offset:13936\n"
"v_mfma_f32_16x16x32_bf16 v[176:179], a[12:15], v[136:139], v[176:179]\n"
"s_waitcnt lgkmcnt(6)\n"
"ds_read_b64_tr_b16 v[116:117], v6 offset:9216\n"
"ds_read_b64_tr_b16 v[118:119], v6 offset:13824\n"
"v_mfma_f32_16x16x32_bf16 v[160:163], a[16:19], v[100:103], v[160:163]\n"
"s_waitcnt lgkmcnt(6)\n"
"ds_read_b64_tr_b16 v[120:121], v4 offset:9360\n"
"ds_read_b64_tr_b16 v[122:123], v4 offset:13968\n"
"v_mfma_f32_16x16x32_bf16 v[164:167], a[16:19], v[104:107], v[164:167]\n"
"s_waitcnt lgkmcnt(6)\n"
"buffer_load_dwordx4 v15, s[16:19], s90 offen offset:3072 lds\n"
"ds_read_b64_tr_b16 v[124:125], v4 offset:9400\n"
"ds_read_b64_tr_b16 v[126:127], v4 offset:14008\n"
"v_mfma_f32_16x16x32_bf16 v[168:171], a[16:19], v[108:111], v[168:171]\n"
"s_waitcnt lgkmcnt(6)\n"
"ds_read_b64_tr_b16 v[128:129], v4 offset:9432\n"
"ds_read_b64_tr_b16 v[130:131], v4 offset:14040\n"
"v_mfma_f32_16x16x32_bf16 v[172:175], a[16:19], v[112:115], v[172:175]\n"
"s_waitcnt lgkmcnt(6)\n"
"ds_read_b64_tr_b16 v[132:133], v4 offset:9472\n"
"ds_read_b64_tr_b16 v[134:135], v4 offset:14080\n"
"v_mfma_f32_16x16x32_bf16 v[176:179], a[16:19], v[116:119], v[176:179]\n"
"s_waitcnt lgkmcnt(6)\n"
"s_add_u32 m0, s91, 4096\n"
"s_nop 0\n"
"buffer_load_dwordx4 v16, s[16:19], s90 offen offset:0 lds\n"
"ds_read_b64_tr_b16 v[136:137], v6 offset:9360\n"
"ds_read_b64_tr_b16 v[138:139], v6 offset:13968\n"
"v_mfma_f32_16x16x32_bf16 v[160:163], a[20:23], v[120:123], v[160:163]\n"
"s_waitcnt lgkmcnt(6)\n"
"ds_read_b64_tr_b16 v[100:101], v4 offset:18432\n"
"ds_read_b64_tr_b16 v[102:103], v4 offset:23040\n"
"v_mfma_f32_16x16x32_bf16 v[164:167], a[20:23], v[124:127], v[164:167]\n"
"s_waitcnt lgkmcnt(6)\n"
"ds_read_b64_tr_b16 v[104:105], v4 offset:18472\n"
"ds_read_b64_tr_b16 v[106:107], v4 offset:23080\n"
"v_mfma_f32_16x16x32_bf16 v[168:171], a[20:23], v[128:131], v[168:171]\n"
"s_waitcnt lgkmcnt(6)\n"
"buffer_load_dwordx4 v17, s[16:19], s90 offen offset:1024 lds\n"
"ds_read_b64_tr_b16 v[108:109], v4 offset:18504\n"
"ds_read_b64_tr_b16 v[110:111], v4 offset:23112\n"
"v_mfma_f32_16x16x32_bf16 v[172:175], a[20:23], v[132:135], v[172:175]\n"
"s_waitcnt lgkmcnt(6)\n"
"ds_read_b64_tr_b16 v[112:113], v4 offset:18544\n"
"ds_read_b64_tr_b16 v[114:115], v4 offset:23152\n"
"v_mfma_f32_16x16x32_bf16 v[176:179], a[20:23], v[136:139], v[176:179]\n"
"s_waitcnt lgkmcnt(6)\n"
"ds_read_b64_tr_b16 v[116:117], v6 offset:18432\n"
"ds_read_b64_tr_b16 v[118:119], v6 offset:23040\n"
"v_mfma_f32_16x16x32_bf16 v[160:163], a[24:27], v[100:103], v[160:163]\n"
"s_waitcnt lgkmcnt(6)\n"
"buffer_load_dwordx4 v18, s[16:19], s90 offen offset:2048 lds\n"
"ds_read_b64_tr_b16 v[120:121], v4 offset:18576\n"
"ds_read_b64_tr_b16 v[122:123], v4 offset:23184\n"
"v_mfma_f32_16x16x32_bf16 v[164:167], a[24:27], v[104:107], v[164:167]\n"
"s_waitcnt lgkmcnt(6)\n"
"ds_read_b64_tr_b16 v[124:125], v4 offset:18616\n"
"ds_read_b64_tr_b16 v[126:127], v4 offset:23224\n"
"v_mfma_f32_16x16x32_bf16 v[168:171], a[24:27], v[108:111], v[168:171]\n"
"s_waitcnt lgkmcnt(6)\n"
"ds_read_b64_tr_b16 v[128:129], v4 offset:18648\n"
"ds_read_b64_tr_b16 v[130:131], v4 offset:23256\n"
"v_mfma_f32_16x16x32_bf16 v[172:175], a[24:27], v[112:115], v[172:175]\n"
"s_waitcnt lgkmcnt(6)\n"
"buffer_load_dwordx4 v19, s[16:19], s90 offen offset:3072 lds\n"
"ds_read_b64_tr_b16 v[132:133], v4 offset:18688\n"
"ds_read_b64_tr_b16 v[134:135], v4 offset:23296\n"
"v_mfma_f32_16x16x32_bf16 v[176:179], a[24:27], v[116:119], v[176:179]\n"
"s_waitcnt lgkmcnt(6)\n"
"ds_read_b64_tr_b16 v[136:137], v6 offset:18576\n"
"ds_read_b64_tr_b16 v[138:139], v6 offset:23184\n"
"v_mfma_f32_16x16x32_bf16 v[160:163], a[28:31], v[120:123], v[160:163]\n"
"s_waitcnt lgkmcnt(6)\n"
"ds_read_b64_tr_b16 v[100:101], v4 offset:27648\n"
"ds_read_b64_tr_b16 v[102:103], v4 offset:32256\n"
"v_mfma_f32_16x16x32_bf16 v[164:167], a[28:31], v[124:127], v[164:167]\n"
"s_waitcnt lgkmcnt(6)\n"
"s_add_u32 m0, s91, 8192\n"
"s_nop 0\n"
"buffer_load_dwordx4 v20, s[16:19], s90 offen offset:0 lds\n"
"ds_read_b64_tr_b16 v[104:105], v4 offset:27688\n"
"ds_read_b64_tr_b16 v[106:107], v4 offset:32296\n"
"v_mfma_f32_16x16x32_bf16 v[168:171], a[28:31], v[128:131], v[168:171]\n"
"s_waitcnt lgkmcnt(6)\n"
"ds_read_b64_tr_b16 v[108:109], v4 offset:27720\n"
"ds_read_b64_tr_b16 v[110:111], v4 offset:32328\n"
"v_mfma_f32_16x16x32_bf16 v[172:175], a[28:31], v[132:135], v[172:175]\n"
"s_waitcnt lgkmcnt(6)\n"
"ds_read_b64_tr_b16 v[112:113], v4 offset:27760\n"
"ds_read_b64_tr_b16 v[114:115], v4 offset:32368\n"
"v_mfma_f32_16x16x32_bf16 v[176:179], a[28:31], v[136:139], v[176:179]\n"
"s_waitcnt lgkmcnt(6)\n"
"ds_read_b64_tr_b16 v[116:117], v6 offset:27648\n"
"ds_read_b64_tr_b16 v[118:119], v6 offset:32256\n"
"v_mfma_f32_16x16x32_bf16 v[160:163], a[32:35], v[100:103], v[160:163]\n"
"s_waitcnt lgkmcnt(6)\n"
"ds_read_b64_tr_b16 v[120:121], v4 offset:27792\n"
"ds_read_b64_tr_b16 v[122:123], v4 offset:32400\n"
"v_mfma_f32_16x16x32_bf16 v[164:167], a[32:35], v[104:107], v[164:167]\n"
"s_waitcnt lgkmcnt(6)\n"
"ds_read_b64_tr_b16 v[124:125], v4 offset:27832\n"
"ds_read_b64_tr_b16 v[126:127], v4 offset:32440\n"
"v_mfma_f32_16x16x32_bf16 v[168:171], a[32:35], v[108:111], v[168:171]\n"
"s_waitcnt lgkmcnt(6)\n"
"ds_read_b64_tr_b16 v[128:129], v4 offset:27864\n"
"ds_read_b64_tr_b16 v[130:131], v4 offset:32472\n"
"v_mfma_f32_16x16x32_bf16 v[172:175], a[32:35], v[112:115], v[172:175]\n"
"s_waitcnt lgkmcnt(6)\n"
"ds_read_b64_tr_b16 v[132:133], v4 offset:27904\n"
"ds_read_b64_tr_b16 v[134:135], v4 offset:32512\n"
"v_mfma_f32_16x16x32_bf16 v[176:179], a[32:35], v[116:119], v[176:179]\n"
"s_waitcnt lgkmcnt(6)\n"
"ds_read_b64_tr_b16 v[136:137], v6 offset:27792\n"
"ds_read_b64_tr_b16 v[138:139], v6 offset:32400\n"
"v_mfma_f32_16x16x32_bf16 v[160:163], a[36:39], v[120:123], v[160:163]\n"
"s_waitcnt lgkmcnt(6)\n"
"v_mfma_f32_16x16x32_bf16 v[164:167], a[36:39], v[124:127], v[164:167]\n"
"s_waitcnt lgkmcnt(4)\n"
"v_mfma_f32_16x16x32_bf16 v[168:171], a[36:39], v[128:131], v[168:171]\n"
"s_waitcnt lgkmcnt(2)\n"
"v_mfma_f32_16x16x32_bf16 v[172:175], a[36:39], v[132:135], v[172:175]\n"
"s_waitcnt lgkmcnt(0)\n"
"v_mfma_f32_16x16x32_bf16 v[176:179], a[36:39], v[136:139], v[176:179]\n"
"v_mul_u32_u24 v34, 12, v0\n"
"s_mul_i32 s72, s26, 768\n"
"s_add_u32 s72, s72, 139520\n"
"v_add_u32 v35, s72, v34\n"
"v_cmp_gt_u32 vcc, 57, v0\n"
"s_nop 1\n"
"v_cndmask_b32 v34, v8, v34, vcc\n"
"v_add_u32 v36, 139520, v27\n"
"v_and_b32 v68, 3, v0\n"
"v_min_u32 v68, 2, v68\n"
"v_lshlrev_b32 v37, 5, v68\n"
"v_lshl_add_u32 v37, v3, 3, v37\n"
"v_add_u32 v37, 140032, v37\n"
"v_lshlrev_b32 v69, 2, v2\n"
"v_add_u32 v69, 140128, v69\n"
"v_lshlrev_b32 v68, 2, v68\n"
"v_add_u32 v68, 140192, v68\n"
"v_mov_b32 v70, 140224\n"
"v_cmp_eq_u32 vcc, 0, v3\n"
"s_nop 1\n"
"v_cndmask_b32 v38, v70, v69, vcc\n"
"v_cndmask_b32 v40, v70, v68, vcc\n"
"v_cmp_eq_u32 vcc, 1, v3\n"
"s_nop 1\n"
"v_cndmask_b32 v39, v70, v69, vcc\n"
"v_cndmask_b32 v41, v70, v68, vcc\n"
"v_add_u32 v42, s71, v1\n"
"v_lshlrev_b32 v68, 4, v2\n"
"v_add_u32 v43, s71, v68\n"
"v_lshl_add_u32 v44, v3, 2, v43\n"
"v_lshl_add_u32 v65, v3, 9, v68\n"
"s_lshl_b32 s72, s32, 4\n"
"s_lshr_b32 s73, s29, 2\n"
"s_add_u32 s73, s73, 1\n"
"s_sub_u32 s74, 0, s73\n"
"s_bitcmp1_b32 s29, 1\n"
"s_cselect_b32 s73, s74, s73\n"
"s_cselect_b32 s75, 31, 1\n"
"s_add_u32 s72, s72, s73\n"
"v_add_u32 v68, s72, v2\n"
"v_mov_b32 v69, 0x800\n"
"v_cmp_lt_u32 s[98:99], v68, v69\n"
"v_bfe_u32 v69, v68, 4, 7\n"
"v_mul_u32_u24 v69, 20480, v69\n"
"v_and_b32 v68, 15, v68\n"
"v_lshl_add_u32 v46, v68, 4, v69\n"
"v_lshl_add_u32 v45, v3, 8, v46\n"
"v_cmp_gt_u32 vcc, 16, v0\n"
"s_nop 1\n"
"v_cndmask_b32 v46, v8, v46, vcc\n"
"v_cndmask_b32 v43, v8, v43, vcc\n"
"s_add_u32 s75, s75, s28\n"
"s_and_b32 s75, s75, 31\n"
"s_lshl_b32 s75, s75, 14\n"
"s_add_u32 s75, s75, 64\n"
"s_lshl_b32 s74, s28, 14\n"
"v_mov_b32 v68, s75\n"
"v_mov_b32 v69, s74\n"
"v_cmp_gt_u32 s[76:77], 8, v0\n"
"s_nop 1\n"
"v_cndmask_b32 v68, v68, v69, s[76:77]\n"
"v_lshl_add_u32 v48, v49, 8, v68\n"
"v_cndmask_b32 v48, v8, v48, vcc\n"
"v_lshrrev_b32 v68, 3, v2\n"
"v_lshlrev_b32 v68, 5, v68\n"
"v_and_b32 v69, 7, v2\n"
"v_lshl_add_u32 v69, v3, 3, v69\n"
"v_add_lshl_u32 v53, v68, v69, 4\n"
"v_add_u32 v54, 0x10000, v53\n"
"v_add_u32 v55, 0x20000, v53\n"
"v_lshlrev_b32 v69, 4, v69\n"
"v_add_u32 v58, 84992, v69\n"
"v_add_u32 v69, 4096, v69\n"
"v_and_b32 v68, 1, v2\n"
"v_lshlrev_b32 v68, 6, v68\n"
"v_lshl_add_u32 v68, v3, 4, v68\n"
"v_add_u32 v68, 82944, v68\n"
"v_cmp_gt_u32 vcc, 8, v2\n"
"s_nop 1\n"
"v_cndmask_b32 v57, v68, v69, vcc\n"
"v_mov_b32 v68, 128\n"
"v_mov_b32 v69, 4608\n"
"v_cndmask_b32 v59, v68, v69, vcc\n"
"v_lshrrev_b32 v68, 3, v0\n"
"v_and_b32 v69, 7, v0\n"
"v_xor_b32 v69, v69, v68\n"
"v_mul_u32_u24 v68, 1152, v68\n"
"v_lshl_add_u32 v10, v69, 4, v68\n"
"s_mul_i32 s72, s29, 648\n"
"s_and_b32 s72, s72, 0x7f0\n"
"v_min_u32 v68, 41, v0\n"
"v_lshl_add_u32 v68, v68, 4, s72\n"
"v_and_b32 v66, 0x7f0, v68\n"
"s_add_u32 s76, s26, 22\n"
"s_mov_b32 s91, 0\n"
"s_mul_i32 s90, s29, 144\n"
"s_add_u32 s90, s90, 0\n"
"s_sub_u32 s76, s76, 18\n"
"s_add_u32 s76, s76, s35\n"
"s_cmp_ge_u32 s76, 9\n"
"s_cselect_b32 s77, 9, 0\n"
"s_sub_u32 s76, s76, s77\n"
"s_mul_i32 s76, s76, 221184\n"
"s_add_u32 s90, s90, s76\n"
"s_mul_i32 s76, s26, 9216\n"
"s_add_u32 s91, s91, s76\n"
"s_waitcnt vmcnt(9)\n"
"s_barrier\n"
"s_cmp_eq_u32 s26, 1\n"
"s_cbranch_scc0 .Lfused_block_kernel_s24\n"
"s_lshl_b32 s72, s33, 3\n"
"s_add_u32 s72, s72, 4096\n"
"buffer_store_dwordx2 v[22:23], v52, s[8:11], s72 offen offset:0 sc1\n"
".Lfused_block_kernel_s24:\n"
"v_cmp_gt_u32 vcc, 8, v2\n"
"s_and_b32 s72, s26, 1\n"
"s_cmp_eq_u32 s72, 1\n"
"s_cselect_b64 s[74:75], -1, 0\n"
"s_lshl_b32 s72, s29, 11\n"
"s_lshl_b32 s73, s28, 3\n"
"s_add_u32 s72, s72, s73\n"
"s_add_u32 s72, s72, 65536\n"
"ds_read_b64_tr_b16 v[100:101], v4 offset:36864\n"
"ds_read_b64_tr_b16 v[102:103], v4 offset:41472\n"
"ds_read_b64_tr_b16 v[104:105], v4 offset:36904\n"
"ds_read_b64_tr_b16 v[106:107], v4 offset:41512\n"
"ds_read_b64_tr_b16 v[108:109], v4 offset:36936\n"
"ds_read_b64_tr_b16 v[110:111], v4 offset:41544\n"
"ds_read_b64_tr_b16 v[112:113], v4 offset:36976\n"
"ds_read_b64_tr_b16 v[114:115], v4 offset:41584\n"
"ds_read_b64_tr_b16 v[116:117], v6 offset:36864\n"
"ds_read_b64_tr_b16 v[118:119], v6 offset:41472\n"
"s_waitcnt lgkmcnt(8)\n"
"v_mfma_f32_16x16x32_bf16 v[160:163], a[40:43], v[100:103], v[160:163]\n"
"s_waitcnt lgkmcnt(6)\n"
"ds_read_b64_tr_b16 v[120:121], v4 offset:37008\n"
"ds_read_b64_tr_b16 v[122:123], v4 offset:41616\n"
"v_mfma_f32_16x16x32_bf16 v[164:167], a[40:43], v[104:107], v[164:167]\n"
"s_waitcnt lgkmcnt(6)\n"
"ds_read_b64_tr_b16 v[124:125], v4 offset:37048\n"
"ds_read_b64_tr_b16 v[126:127], v4 offset:41656\n"
"v_mfma_f32_16x16x32_bf16 v[168:171], a[40:43], v[108:111], v[168:171]\n"
"s_waitcnt lgkmcnt(6)\n"
"ds_read_b64_tr_b16 v[128:129], v4 offset:37080\n"
"ds_read_b64_tr_b16 v[130:131], v4 offset:41688\n"
"v_mfma_f32_16x16x32_bf16 v[172:175], a[40:43], v[112:115], v[172:175]\n"
"s_waitcnt lgkmcnt(6)\n"
"ds_read_b64_tr_b16 v[132:133], v4 offset:37120\n"
"ds_read_b64_tr_b16 v[134:135], v4 offset:41728\n"
"v_mfma_f32_16x16x32_bf16 v[176:179], a[40:43], v[116:119], v[176:179]\n"
"s_waitcnt lgkmcnt(6)\n"
"ds_read_b64_tr_b16 v[136:137], v6 offset:37008\n"
"ds_read_b64_tr_b16 v[138:139], v6 offset:41616\n"
"v_mfma_f32_16x16x32_bf16 v[160:163], a[44:47], v[120:123], v[160:163]\n"
"s_waitcnt lgkmcnt(6)\n"
"ds_read_b64_tr_b16 v[100:101], v4 offset:46080\n"
"ds_read_b64_tr_b16 v[102:103], v4 offset:50688\n"
"v_mfma_f32_16x16x32_bf16 v[164:167], a[44:47], v[124:127], v[164:167]\n"
"s_waitcnt lgkmcnt(6)\n"
"ds_read_b64_tr_b16 v[104:105], v4 offset:46120\n"
"ds_read_b64_tr_b16 v[106:107], v4 offset:50728\n"
"v_mfma_f32_16x16x32_bf16 v[168:171], a[44:47], v[128:131], v[168:171]\n"
"s_waitcnt lgkmcnt(6)\n"
"ds_read_b64_tr_b16 v[108:109], v4 offset:46152\n"
"ds_read_b64_tr_b16 v[110:111], v4 offset:50760\n"
"v_mfma_f32_16x16x32_bf16 v[172:175], a[44:47], v[132:135], v[172:175]\n"
"s_waitcnt lgkmcnt(6)\n"
"ds_read_b64_tr_b16 v[112:113], v4 offset:46192\n"
"ds_read_b64_tr_b16 v[114:115], v4 offset:50800\n"
"v_mfma_f32_16x16x32_bf16 v[176:179], a[44:47], v[136:139], v[176:179]\n"
"s_waitcnt lgkmcnt(6)\n"
"ds_read_b64_tr_b16 v[116:117], v6 offset:46080\n"
"ds_read_b64_tr_b16 v[118:119], v6 offset:50688\n"
"v_mfma_f32_16x16x32_bf16 v[160:163], a[48:51], v[100:103], v[160:163]\n"
"s_waitcnt lgkmcnt(6)\n"
"ds_read_b64_tr_b16 v[120:121], v4 offset:46224\n"
"ds_read_b64_tr_b16 v[122:123], v4 offset:50832\n"
"v_mfma_f32_16x16x32_bf16 v[164:167], a[48:51], v[104:107], v[164:167]\n"
"s_waitcnt lgkmcnt(6)\n"
"ds_read_b64_tr_b16 v[124:125], v4 offset:46264\n"
"ds_read_b64_tr_b16 v[126:127], v4 offset:50872\n"
"v_mfma_f32_16x16x32_bf16 v[168:171], a[48:51], v[108:111], v[168:171]\n"
"s_waitcnt lgkmcnt(6)\n"
"ds_read_b64_tr_b16 v[128:129], v4 offset:46296\n"
"ds_read_b64_tr_b16 v[130:131], v4 offset:50904\n"
"v_mfma_f32_16x16x32_bf16 v[172:175], a[48:51], v[112:115], v[172:175]\n"
"s_waitcnt lgkmcnt(6)\n"
"ds_read_b64_tr_b16 v[132:133], v4 offset:46336\n"
"ds_read_b64_tr_b16 v[134:135], v4 offset:50944\n"
"v_mfma_f32_16x16x32_bf16 v[176:179], a[48:51], v[116:119], v[176:179]\n"
"s_waitcnt lgkmcnt(6)\n"
"ds_read_b64_tr_b16 v[136:137], v6 offset:46224\n"
"ds_read_b64_tr_b16 v[138:139], v6 offset:50832\n"
"v_mfma_f32_16x16x32_bf16 v[160:163], a[52:55], v[120:123], v[160:163]\n"
"s_waitcnt lgkmcnt(6)\n"
"ds_read_b64_tr_b16 v[100:101], v4 offset:55296\n"
"ds_read_b64_tr_b16 v[102:103], v4 offset:59904\n"
"v_mfma_f32_16x16x32_bf16 v[164:167], a[52:55], v[124:127], v[164:167]\n"
"s_waitcnt lgkmcnt(6)\n"
"ds_read_b64_tr_b16 v[104:105], v4 offset:55336\n"
"ds_read_b64_tr_b16 v[106:107], v4 offset:59944\n"
"v_mfma_f32_16x16x32_bf16 v[168:171], a[52:55], v[128:131], v[168:171]\n"
"s_waitcnt lgkmcnt(6)\n"
"ds_read_b64_tr_b16 v[108:109], v4 offset:55368\n"
"ds_read_b64_tr_b16 v[110:111], v4 offset:59976\n"
"v_mfma_f32_16x16x32_bf16 v[172:175], a[52:55], v[132:135], v[172:175]\n"
"s_waitcnt lgkmcnt(6)\n"
"ds_read_b64_tr_b16 v[112:113], v4 offset:55408\n"
"ds_read_b64_tr_b16 v[114:115], v4 offset:60016\n"
"v_mfma_f32_16x16x32_bf16 v[176:179], a[52:55], v[136:139], v[176:179]\n"
"s_waitcnt lgkmcnt(6)\n"
"ds_read_b64_tr_b16 v[116:117], v6 offset:55296\n"
"ds_read_b64_tr_b16 v[118:119], v6 offset:59904\n"
"v_mfma_f32_16x16x32_bf16 v[160:163], a[56:59], v[100:103], v[160:163]\n"
"s_waitcnt lgkmcnt(6)\n"
"ds_read_b64_tr_b16 v[120:121], v4 offset:55440\n"
"ds_read_b64_tr_b16 v[122:123], v4 offset:60048\n"
"v_mfma_f32_16x16x32_bf16 v[164:167], a[56:59], v[104:107], v[164:167]\n"
"s_waitcnt lgkmcnt(6)\n"
"ds_read_b64_tr_b16 v[124:125], v4 offset:55480\n"
"ds_read_b64_tr_b16 v[126:127], v4 offset:60088\n"
"v_mfma_f32_16x16x32_bf16 v[168:171], a[56:59], v[108:111], v[168:171]\n"
"s_waitcnt lgkmcnt(6)\n"
"ds_read_b64_tr_b16 v[128:129], v4 offset:55512\n"
"ds_read_b64_tr_b16 v[130:131], v4 offset:60120\n"
"v_mfma_f32_16x16x32_bf16 v[172:175], a[56:59], v[112:115], v[172:175]\n"
"s_waitcnt lgkmcnt(6)\n"
"ds_read_b64_tr_b16 v[132:133], v4 offset:55552\n"
"ds_read_b64_tr_b16 v[134:135], v4 offset:60160\n"
"v_mfma_f32_16x16x32_bf16 v[176:179], a[56:59], v[116:119], v[176:179]\n"
"s_waitcnt lgkmcnt(6)\n"
"ds_read_b64_tr_b16 v[136:137], v6 offset:55440\n"
"ds_read_b64_tr_b16 v[138:139], v6 offset:60048\n"
"v_mfma_f32_16x16x32_bf16 v[160:163], a[60:63], v[120:123], v[160:163]\n"
"s_waitcnt lgkmcnt(6)\n"
"ds_read_b64_tr_b16 v[100:101], v4 offset:64512\n"
"ds_read_b64_tr_b16 v[102:103], v5 offset:3584\n"
"v_mfma_f32_16x16x32_bf16 v[164:167], a[60:63], v[124:127], v[164:167]\n"
"s_waitcnt lgkmcnt(6)\n"
"ds_read_b64_tr_b16 v[104:105], v4 offset:64552\n"
"ds_read_b64_tr_b16 v[106:107], v5 offset:3624\n"
"v_mfma_f32_16x16x32_bf16 v[168:171], a[60:63], v[128:131], v[168:171]\n"
"s_waitcnt lgkmcnt(6)\n"
"ds_read_b64_tr_b16 v[108:109], v4 offset:64584\n"
"ds_read_b64_tr_b16 v[110:111], v5 offset:3656\n"
"v_mfma_f32_16x16x32_bf16 v[172:175], a[60:63], v[132:135], v[172:175]\n"
"s_waitcnt lgkmcnt(6)\n"
"ds_read_b64_tr_b16 v[112:113], v4 offset:64624\n"
"ds_read_b64_tr_b16 v[114:115], v5 offset:3696\n"
"v_mfma_f32_16x16x32_bf16 v[176:179], a[60:63], v[136:139], v[176:179]\n"
"s_waitcnt lgkmcnt(6)\n"
"ds_read_b64_tr_b16 v[116:117], v6 offset:64512\n"
"ds_read_b64_tr_b16 v[118:119], v7 offset:3584\n"
"v_mfma_f32_16x16x32_bf16 v[160:163], a[64:67], v[100:103], v[160:163]\n"
"s_waitcnt lgkmcnt(6)\n"
"ds_read_b64_tr_b16 v[120:121], v4 offset:64656\n"
"ds_read_b64_tr_b16 v[122:123], v5 offset:3728\n"
"v_mfma_f32_16x16x32_bf16 v[164:167], a[64:67], v[104:107], v[164:167]\n"
"s_waitcnt lgkmcnt(6)\n"
"ds_read_b64_tr_b16 v[124:125], v4 offset:64696\n"
"ds_read_b64_tr_b16 v[126:127], v5 offset:3768\n"
"v_mfma_f32_16x16x32_bf16 v[168:171], a[64:67], v[108:111], v[168:171]\n"
"s_waitcnt lgkmcnt(6)\n"
"ds_read_b64_tr_b16 v[128:129], v4 offset:64728\n"
"ds_read_b64_tr_b16 v[130:131], v5 offset:3800\n"
"v_mfma_f32_16x16x32_bf16 v[172:175], a[64:67], v[112:115], v[172:175]\n"
"s_waitcnt lgkmcnt(6)\n"
"ds_read_b64_tr_b16 v[132:133], v4 offset:64768\n"
"ds_read_b64_tr_b16 v[134:135], v5 offset:3840\n"
"v_mfma_f32_16x16x32_bf16 v[176:179], a[64:67], v[116:119], v[176:179]\n"
"s_waitcnt lgkmcnt(6)\n"
"ds_read_b64_tr_b16 v[136:137], v6 offset:64656\n"
"ds_read_b64_tr_b16 v[138:139], v7 offset:3728\n"
"v_mfma_f32_16x16x32_bf16 v[160:163], a[68:71], v[120:123], v[160:163]\n"
"s_waitcnt lgkmcnt(6)\n"
"v_mfma_f32_16x16x32_bf16 v[164:167], a[68:71], v[124:127], v[164:167]\n"
"s_waitcnt lgkmcnt(4)\n"
"v_mfma_f32_16x16x32_bf16 v[168:171], a[68:71], v[128:131], v[168:171]\n"
"s_waitcnt lgkmcnt(2)\n"
"v_mfma_f32_16x16x32_bf16 v[172:175], a[68:71], v[132:135], v[172:175]\n"
"s_waitcnt lgkmcnt(0)\n"
"v_mfma_f32_16x16x32_bf16 v[176:179], a[68:71], v[136:139], v[176:179]\n"
"v_cvt_pk_bf16_f32 v160, v160, v161\n"
"v_cvt_pk_bf16_f32 v161, v162, v163\n"
"ds_write_b64 v26, v[160:161] offset:10240\n"
"v_cvt_pk_bf16_f32 v164, v164, v165\n"
"v_cvt_pk_bf16_f32 v165, v166, v167\n"
"ds_write_b64 v26, v[164:165] offset:12288\n"
"v_cvt_pk_bf16_f32 v168, v168, v169\n"
"v_cvt_pk_bf16_f32 v169, v170, v171\n"
"ds_write_b64 v26, v[168:169] offset:14336\n"
"v_cvt_pk_bf16_f32 v172, v172, v173\n"
"v_cvt_pk_bf16_f32 v173, v174, v175\n"
"ds_write_b64 v26, v[172:173] offset:16384\n"
"v_cndmask_b32 v176, 1.0, v176, vcc\n"
"v_cndmask_b32 v177, 1.0, v177, vcc\n"
"v_cndmask_b32 v178, 1.0, v178, vcc\n"
"v_cndmask_b32 v179, 1.0, v179, vcc\n"
"v_cvt_pk_bf16_f32 v176, v176, v177\n"
"v_cvt_pk_bf16_f32 v177, v178, v179\n"
"ds_write_b64 v26, v[176:177] offset:18432\n"
"s_waitcnt lgkmcnt(0)\n"
"s_barrier\n"
"ds_read_b128 v[88:91], v31 offset:10240\n"
"ds_read_b128 v[92:95], v31 offset:11264\n"
"ds_read_b128 v[200:203], v32 offset:18432\n"
"ds_read_b128 v[204:207], v32 offset:19456\n"
"s_waitcnt lgkmcnt(3)\n"
"v_mfma_f32_16x16x32_bf16 v[140:143], v[72:75], v[88:91], 0\n"
"v_mfma_f32_16x16x32_bf16 v[144:147], v[80:83], v[88:91], 0\n"
"s_waitcnt lgkmcnt(2)\n"
"v_mfma_f32_16x16x32_bf16 v[140:143], v[76:79], v[92:95], v[140:143]\n"
"v_mfma_f32_16x16x32_bf16 v[144:147], v[84:87], v[92:95], v[144:147]\n"
"s_waitcnt lgkmcnt(1)\n"
"v_mfma_f32_16x16x32_bf16 v[148:151], v[72:75], v[200:203], 0\n"
"v_mfma_f32_16x16x32_bf16 v[152:155], v[80:83], v[200:203], 0\n"
"s_waitcnt lgkmcnt(0)\n"
"v_mfma_f32_16x16x32_bf16 v[148:151], v[76:79], v[204:207], v[148:151]\n"
"v_mfma_f32_16x16x32_bf16 v[152:155], v[84:87], v[204:207], v[152:155]\n"
"s_nop 0\n"
"v_cvt_pk_bf16_f32 v140, v140, v141\n"
"v_cvt_pk_bf16_f32 v141, v142, v143\n"
"buffer_store_dwordx2 v[140:141], v27, s[8:11], s101 offen sc1\n"
"v_cvt_pk_bf16_f32 v144, v144, v145\n"
"v_cvt_pk_bf16_f32 v145, v146, v147\n"
"v_cndmask_b32 v144, v144, v145, s[74:75]\n"
"buffer_store_dword v144, v29, s[8:11], s101 offen offset:608 sc1\n"
"v_cvt_pk_bf16_f32 v148, v148, v149\n"
"v_cvt_pk_bf16_f32 v149, v150, v151\n"
"buffer_store_dwordx2 v[148:149], v28, s[8:11], s101 offen offset:512 sc1\n"
"v_cvt_pk_bf16_f32 v152, v152, v153\n"
"v_cvt_pk_bf16_f32 v153, v154, v155\n"
"v_cndmask_b32 v152, v152, v153, s[74:75]\n"
"buffer_store_dword v152, v30, s[8:11], s101 offen offset:672 sc1\n"
"s_add_u32 m0, s91, 0\n"
"s_nop 0\n"
"buffer_load_dwordx4 v12, s[16:19], s90 offen offset:0 lds\n"
"buffer_load_dwordx4 v13, s[16:19], s90 offen offset:1024 lds\n"
"buffer_load_dwordx4 v14, s[16:19], s90 offen offset:2048 lds\n"
"buffer_load_dwordx4 v15, s[16:19], s90 offen offset:3072 lds\n"
"s_waitcnt vmcnt(4)\n"
"s_barrier\n"
"s_cmp_eq_u32 s26, 0\n"
"s_cbranch_scc0 .Lfused_block_kernel_s25\n"
"buffer_store_dwordx2 v[22:23], v33, s[8:11], s72 offen offset:0 sc1\n"
".Lfused_block_kernel_s25:\n"
"ds_read_b64_tr_b16 v[100:101], v5 offset:8192\n"
"ds_read_b64_tr_b16 v[102:103], v5 offset:12800\n"
"ds_read_b64_tr_b16 v[104:105], v5 offset:8232\n"
"ds_read_b64_tr_b16 v[106:107], v5 offset:12840\n"
"ds_read_b64_tr_b16 v[108:109], v5 offset:8264\n"
"ds_read_b64_tr_b16 v[110:111], v5 offset:12872\n"
"ds_read_b64_tr_b16 v[112:113], v5 offset:8304\n"
"ds_read_b64_tr_b16 v[114:115], v5 offset:12912\n"
"ds_read_b64_tr_b16 v[116:117], v7 offset:8192\n"
"ds_read_b64_tr_b16 v[118:119], v7 offset:12800\n"
"s_waitcnt lgkmcnt(8)\n"
"v_mfma_f32_16x16x32_bf16 v[180:183], v[100:103], a[0:3], 0\n"
"s_waitcnt lgkmcnt(6)\n"
"ds_read_b64_tr_b16 v[120:121], v5 offset:8336\n"
"ds_read_b64_tr_b16 v[122:123], v5 offset:12944\n"
"v_mfma_f32_16x16x32_bf16 v[184:187], v[104:107], a[0:3], 0\n"
"s_waitcnt lgkmcnt(6)\n"
"ds_read_b64_tr_b16 v[124:125], v5 offset:8376\n"
"ds_read_b64_tr_b16 v[126:127], v5 offset:12984\n"
"v_mfma_f32_16x16x32_bf16 v[188:191], v[108:111], a[0:3], 0\n"
"s_waitcnt lgkmcnt(6)\n"
"s_add_u32 m0, s91, 4096\n"
"s_nop 0\n"
"buffer_load_dwordx4 v16, s[16:19], s90 offen offset:0 lds\n"
"ds_read_b64_tr_b16 v[128:129], v5 offset:8408\n"
"ds_read_b64_tr_b16 v[130:131], v5 offset:13016\n"
"v_mfma_f32_16x16x32_bf16 v[192:195], v[112:115], a[0:3], 0\n"
"s_waitcnt lgkmcnt(6)\n"
"ds_read_b64_tr_b16 v[132:133], v5 offset:8448\n"
"ds_read_b64_tr_b16 v[134:135], v5 offset:13056\n"
"v_mfma_f32_16x16x32_bf16 v[196:199], v[116:119], a[0:3], 0\n"
"s_waitcnt lgkmcnt(6)\n"
"ds_read_b64_tr_b16 v[136:137], v7 offset:8336\n"
"ds_read_b64_tr_b16 v[138:139], v7 offset:12944\n"
"v_mfma_f32_16x16x32_bf16 v[180:183], v[120:123], a[4:7], v[180:183]\n"
"s_waitcnt lgkmcnt(6)\n"
"buffer_load_dwordx4 v17, s[16:19], s90 offen offset:1024 lds\n"
"ds_read_b64_tr_b16 v[100:101], v5 offset:17408\n"
"ds_read_b64_tr_b16 v[102:103], v5 offset:22016\n"
"v_mfma_f32_16x16x32_bf16 v[184:187], v[124:127], a[4:7], v[184:187]\n"
"s_waitcnt lgkmcnt(6)\n"
"ds_read_b64_tr_b16 v[104:105], v5 offset:17448\n"
"ds_read_b64_tr_b16 v[106:107], v5 offset:22056\n"
"v_mfma_f32_16x16x32_bf16 v[188:191], v[128:131], a[4:7], v[188:191]\n"
"s_waitcnt lgkmcnt(6)\n"
"ds_read_b64_tr_b16 v[108:109], v5 offset:17480\n"
"ds_read_b64_tr_b16 v[110:111], v5 offset:22088\n"
"v_mfma_f32_16x16x32_bf16 v[192:195], v[132:135], a[4:7], v[192:195]\n"
"s_waitcnt lgkmcnt(6)\n"
"buffer_load_dwordx4 v18, s[16:19], s90 offen offset:2048 lds\n"
"ds_read_b64_tr_b16 v[112:113], v5 offset:17520\n"
"ds_read_b64_tr_b16 v[114:115], v5 offset:22128\n"
"v_mfma_f32_16x16x32_bf16 v[196:199], v[136:139], a[4:7], v[196:199]\n"
"s_waitcnt lgkmcnt(6)\n"
"ds_read_b64_tr_b16 v[116:117], v7 offset:17408\n"
"ds_read_b64_tr_b16 v[118:119], v7 offset:22016\n"
"v_mfma_f32_16x16x32_bf16 v[180:183], v[100:103], a[8:11], v[180:183]\n"
"s_waitcnt lgkmcnt(6)\n"
"ds_read_b64_tr_b16 v[120:121], v5 offset:17552\n"
"ds_read_b64_tr_b16 v[122:123], v5 offset:22160\n"
"v_mfma_f32_16x16x32_bf16 v[184:187], v[104:107], a[8:11], v[184:187]\n"
"s_waitcnt lgkmcnt(6)\n"
"buffer_load_dwordx4 v19, s[16:19], s90 offen offset:3072 lds\n"
"ds_read_b64_tr_b16 v[124:125], v5 offset:17592\n"
"ds_read_b64_tr_b16 v[126:127], v5 offset:22200\n"
"v_mfma_f32_16x16x32_bf16 v[188:191], v[108:111], a[8:11], v[188:191]\n"
"s_waitcnt lgkmcnt(6)\n"
"ds_read_b64_tr_b16 v[128:129], v5 offset:17624\n"
"ds_read_b64_tr_b16 v[130:131], v5 offset:22232\n"
"v_mfma_f32_16x16x32_bf16 v[192:195], v[112:115], a[8:11], v[192:195]\n"
"s_waitcnt lgkmcnt(6)\n"
"ds_read_b64_tr_b16 v[132:133], v5 offset:17664\n"
"ds_read_b64_tr_b16 v[134:135], v5 offset:22272\n"
"v_mfma_f32_16x16x32_bf16 v[196:199], v[116:119], a[8:11], v[196:199]\n"
"s_waitcnt lgkmcnt(6)\n"
"s_add_u32 m0, s91, 8192\n"
"s_nop 0\n"
"buffer_load_dwordx4 v20, s[16:19], s90 offen offset:0 lds\n"
"ds_read_b64_tr_b16 v[136:137], v7 offset:17552\n"
"ds_read_b64_tr_b16 v[138:139], v7 offset:22160\n"
"v_mfma_f32_16x16x32_bf16 v[180:183], v[120:123], a[12:15], v[180:183]\n"
"s_waitcnt lgkmcnt(6)\n"
"ds_read_b64_tr_b16 v[100:101], v5 offset:26624\n"
"ds_read_b64_tr_b16 v[102:103], v5 offset:31232\n"
"v_mfma_f32_16x16x32_bf16 v[184:187], v[124:127], a[12:15], v[184:187]\n"
"s_waitcnt lgkmcnt(6)\n"
"ds_read_b64_tr_b16 v[104:105], v5 offset:26664\n"
"ds_read_b64_tr_b16 v[106:107], v5 offset:31272\n"
"v_mfma_f32_16x16x32_bf16 v[188:191], v[128:131], a[12:15], v[188:191]\n"
"s_waitcnt lgkmcnt(6)\n"
"ds_read_b64_tr_b16 v[108:109], v5 offset:26696\n"
"ds_read_b64_tr_b16 v[110:111], v5 offset:31304\n"
"v_mfma_f32_16x16x32_bf16 v[192:195], v[132:135], a[12:15], v[192:195]\n"
"s_waitcnt lgkmcnt(6)\n"
"ds_read_b64_tr_b16 v[112:113], v5 offset:26736\n"
"ds_read_b64_tr_b16 v[114:115], v5 offset:31344\n"
"v_mfma_f32_16x16x32_bf16 v[196:199], v[136:139], a[12:15], v[196:199]\n"
"s_waitcnt lgkmcnt(6)\n"
"ds_read_b64_tr_b16 v[116:117], v7 offset:26624\n"
"ds_read_b64_tr_b16 v[118:119], v7 offset:31232\n"
"v_mfma_f32_16x16x32_bf16 v[180:183], v[100:103], a[16:19], v[180:183]\n"
"s_waitcnt lgkmcnt(6)\n"
"ds_read_b64_tr_b16 v[120:121], v5 offset:26768\n"
"ds_read_b64_tr_b16 v[122:123], v5 offset:31376\n"
"v_mfma_f32_16x16x32_bf16 v[184:187], v[104:107], a[16:19], v[184:187]\n"
"s_waitcnt lgkmcnt(6)\n"
"ds_read_b64_tr_b16 v[124:125], v5 offset:26808\n"
"ds_read_b64_tr_b16 v[126:127], v5 offset:31416\n"
"v_mfma_f32_16x16x32_bf16 v[188:191], v[108:111], a[16:19], v[188:191]\n"
"s_waitcnt lgkmcnt(6)\n"
"ds_read_b64_tr_b16 v[128:129], v5 offset:26840\n"
"ds_read_b64_tr_b16 v[130:131], v5 offset:31448\n"
"v_mfma_f32_16x16x32_bf16 v[192:195], v[112:115], a[16:19], v[192:195]\n"
"s_waitcnt lgkmcnt(6)\n"
"ds_read_b64_tr_b16 v[132:133], v5 offset:26880\n"
"ds_read_b64_tr_b16 v[134:135], v5 offset:31488\n"
"v_mfma_f32_16x16x32_bf16 v[196:199], v[116:119], a[16:19], v[196:199]\n"
"s_waitcnt lgkmcnt(6)\n"
"ds_read_b64_tr_b16 v[136:137], v7 offset:26768\n"
"ds_read_b64_tr_b16 v[138:139], v7 offset:31376\n"
"v_mfma_f32_16x16x32_bf16 v[180:183], v[120:123], a[20:23], v[180:183]\n"
"s_waitcnt lgkmcnt(6)\n"
"ds_read_b64_tr_b16 v[100:101], v5 offset:35840\n"
"ds_read_b64_tr_b16 v[102:103], v5 offset:40448\n"
"v_mfma_f32_16x16x32_bf16 v[184:187], v[124:127], a[20:23], v[184:187]\n"
"s_waitcnt lgkmcnt(6)\n"
"ds_read_b64_tr_b16 v[104:105], v5 offset:35880\n"
"ds_read_b64_tr_b16 v[106:107], v5 offset:40488\n"
"v_mfma_f32_16x16x32_bf16 v[188:191], v[128:131], a[20:23], v[188:191]\n"
"s_waitcnt lgkmcnt(6)\n"
"ds_read_b64_tr_b16 v[108:109], v5 offset:35912\n"
"ds_read_b64_tr_b16 v[110:111], v5 offset:40520\n"
"v_mfma_f32_16x16x32_bf16 v[192:195], v[132:135], a[20:23], v[192:195]\n"
"s_waitcnt lgkmcnt(6)\n"
"ds_read_b64_tr_b16 v[112:113], v5 offset:35952\n"
"ds_read_b64_tr_b16 v[114:115], v5 offset:40560\n"
"v_mfma_f32_16x16x32_bf16 v[196:199], v[136:139], a[20:23], v[196:199]\n"
"s_waitcnt lgkmcnt(6)\n"
"ds_read_b64_tr_b16 v[116:117], v7 offset:35840\n"
"ds_read_b64_tr_b16 v[118:119], v7 offset:40448\n"
"v_mfma_f32_16x16x32_bf16 v[180:183], v[100:103], a[24:27], v[180:183]\n"
"s_waitcnt lgkmcnt(6)\n"
"ds_read_b64_tr_b16 v[120:121], v5 offset:35984\n"
"ds_read_b64_tr_b16 v[122:123], v5 offset:40592\n"
"v_mfma_f32_16x16x32_bf16 v[184:187], v[104:107], a[24:27], v[184:187]\n"
"s_waitcnt lgkmcnt(6)\n"
"ds_read_b64_tr_b16 v[124:125], v5 offset:36024\n"
"ds_read_b64_tr_b16 v[126:127], v5 offset:40632\n"
"v_mfma_f32_16x16x32_bf16 v[188:191], v[108:111], a[24:27], v[188:191]\n"
"s_waitcnt lgkmcnt(6)\n"
"ds_read_b64_tr_b16 v[128:129], v5 offset:36056\n"
"ds_read_b64_tr_b16 v[130:131], v5 offset:40664\n"
"v_mfma_f32_16x16x32_bf16 v[192:195], v[112:115], a[24:27], v[192:195]\n"
"s_waitcnt lgkmcnt(6)\n"
"ds_read_b64_tr_b16 v[132:133], v5 offset:36096\n"
"ds_read_b64_tr_b16 v[134:135], v5 offset:40704\n"
"v_mfma_f32_16x16x32_bf16 v[196:199], v[116:119], a[24:27], v[196:199]\n"
"s_waitcnt lgkmcnt(6)\n"
"ds_read_b64_tr_b16 v[136:137], v7 offset:35984\n"
"ds_read_b64_tr_b16 v[138:139], v7 offset:40592\n"
"v_mfma_f32_16x16x32_bf16 v[180:183], v[120:123], a[28:31], v[180:183]\n"
"s_waitcnt lgkmcnt(6)\n"
"v_mfma_f32_16x16x32_bf16 v[184:187], v[124:127], a[28:31], v[184:187]\n"
"s_waitcnt lgkmcnt(4)\n"
"v_mfma_f32_16x16x32_bf16 v[188:191], v[128:131], a[28:31], v[188:191]\n"
"s_waitcnt lgkmcnt(2)\n"
"v_mfma_f32_16x16x32_bf16 v[192:195], v[132:135], a[28:31], v[192:195]\n"
"s_waitcnt lgkmcnt(0)\n"
"v_mfma_f32_16x16x32_bf16 v[196:199], v[136:139], a[28:31], v[196:199]\n"
"s_cmp_eq_u32 s26, 3\n"
"s_cbranch_scc0 .Lfused_block_kernel_s26\n"
"s_add_u32 s76, s35, 8\n"
"s_cmp_ge_u32 s76, 9\n"
"s_cselect_b32 s77, 9, 0\n"
"s_sub_u32 s76, s76, s77\n"
"s_mul_i32 s76, s76, 221184\n"
"s_mul_i32 s90, s29, 144\n"
"s_add_u32 s90, s90, s76\n"
"s_mov_b32 s91, 36864\n"
"s_add_u32 m0, s91, 0\n"
"s_nop 0\n"
"buffer_load_dwordx4 v12, s[16:19], s90 offen offset:0 lds\n"
"buffer_load_dwordx4 v13, s[16:19], s90 offen offset:1024 lds\n"
"buffer_load_dwordx4 v14, s[16:19], s90 offen offset:2048 lds\n"
"buffer_load_dwordx4 v15, s[16:19], s90 offen offset:3072 lds\n"
"s_add_u32 m0, s91, 4096\n"
"s_nop 0\n"
"buffer_load_dwordx4 v16, s[16:19], s90 offen offset:0 lds\n"
"buffer_load_dwordx4 v17, s[16:19], s90 offen offset:1024 lds\n"
"buffer_load_dwordx4 v18, s[16:19], s90 offen offset:2048 lds\n"
"buffer_load_dwordx4 v19, s[16:19], s90 offen offset:3072 lds\n"
"s_add_u32 m0, s91, 8192\n"
"s_nop 0\n"
"buffer_load_dwordx4 v20, s[16:19], s90 offen offset:0 lds\n"
".Lfused_block_kernel_s26:\n"
"s_add_u32 s72, s100, 0\n"
"s_add_u32 s73, s100, 3840\n"
"s_add_u32 s74, s100, 7680\n"
"s_add_u32 s75, s100, 11520\n"
"s_add_u32 s76, s100, 15360\n"
"s_add_u32 s77, s100, 19200\n"
"s_add_u32 s78, s100, 23040\n"
"s_cmp_eq_u32 s26, 0\n"
"s_cbranch_scc0 .Lfused_block_kernel_s27\n"
"s_mov_b32 s87, 0x4000\n"
".Lfused_block_kernel_poll28:\n"
"buffer_load_dwordx2 v[72:73], v51, s[8:11], s31 offen offset:0 sc1\n"
"s_waitcnt vmcnt(0)\n"
"v_cmp_ne_u64 vcc, s[24:25], v[72:73]\n"
"s_nop 0\n"
"s_cmp_eq_u64 vcc, 0\n"
"s_cbranch_scc1 .Lfused_block_kernel_pok29\n"
"s_sub_u32 s87, s87, 1\n"
"s_cmp_lg_u32 s87, 0\n"
"s_cbranch_scc1 .Lfused_block_kernel_poll28\n"
".Lfused_block_kernel_pok29:\n"
".Lfused_block_kernel_s27:\n"
"s_cmp_eq_u32 s26, 1\n"
"s_cbranch_scc0 .Lfused_block_kernel_s30\n"
"s_mov_b32 s86, 4096\n"
"s_mov_b32 s87, 0x4000\n"
".Lfused_block_kernel_poll31:\n"
"buffer_load_dwordx4 v[72:75], v1, s[8:11], s86 offen offset:0 sc1\n"
"buffer_load_dwordx4 v[76:79], v1, s[8:11], s86 offen offset:1024 sc1\n"
"s_waitcnt vmcnt(0)\n"
"v_cmp_ne_u64 vcc, s[24:25], v[72:73]\n"
"v_cmp_ne_u64 s[84:85], s[24:25], v[74:75]\n"
"s_nop 0\n"
"s_or_b64 vcc, vcc, s[84:85]\n"
"v_cmp_ne_u64 s[84:85], s[24:25], v[76:77]\n"
"s_nop 0\n"
"s_or_b64 vcc, vcc, s[84:85]\n"
"v_cmp_ne_u64 s[84:85], s[24:25], v[78:79]\n"
"s_nop 0\n"
"s_or_b64 vcc, vcc, s[84:85]\n"
"s_cmp_eq_u64 vcc, 0\n"
"s_cbranch_scc1 .Lfused_block_kernel_pok32\n"
"s_sub_u32 s87, s87, 1\n"
"s_cmp_lg_u32 s87, 0\n"
"s_cbranch_scc1 .Lfused_block_kernel_poll31\n"
".Lfused_block_kernel_pok32:\n"
".Lfused_block_kernel_s30:\n"
"s_waitcnt vmcnt(0)\n"
"s_barrier\n"
"buffer_load_dwordx3 v[72:74], v34, s[8:11], s72 offen offset:0 sc1\n"
"buffer_load_dwordx3 v[76:78], v34, s[8:11], s72 offen offset:768 sc1\n"
"buffer_load_dwordx3 v[80:82], v34, s[8:11], s72 offen offset:1536 sc1\n"
"buffer_load_dwordx3 v[84:86], v34, s[8:11], s72 offen offset:2304 sc1\n"
"ds_read_b64_tr_b16 v[100:101], v4 offset:0\n"
"ds_read_b64_tr_b16 v[102:103], v4 offset:4608\n"
"ds_read_b64_tr_b16 v[104:105], v4 offset:40\n"
"ds_read_b64_tr_b16 v[106:107], v4 offset:4648\n"
"ds_read_b64_tr_b16 v[108:109], v4 offset:72\n"
"ds_read_b64_tr_b16 v[110:111], v4 offset:4680\n"
"ds_read_b64_tr_b16 v[112:113], v4 offset:112\n"
"ds_read_b64_tr_b16 v[114:115], v4 offset:4720\n"
"ds_read_b64_tr_b16 v[116:117], v6 offset:0\n"
"ds_read_b64_tr_b16 v[118:119], v6 offset:4608\n"
"s_waitcnt lgkmcnt(8)\n"
"v_mfma_f32_16x16x32_bf16 v[180:183], v[100:103], a[32:35], v[180:183]\n"
"s_waitcnt lgkmcnt(6)\n"
"ds_read_b64_tr_b16 v[120:121], v4 offset:144\n"
"ds_read_b64_tr_b16 v[122:123], v4 offset:4752\n"
"v_mfma_f32_16x16x32_bf16 v[184:187], v[104:107], a[32:35], v[184:187]\n"
"s_waitcnt lgkmcnt(6)\n"
"ds_read_b64_tr_b16 v[124:125], v4 offset:184\n"
"ds_read_b64_tr_b16 v[126:127], v4 offset:4792\n"
"v_mfma_f32_16x16x32_bf16 v[188:191], v[108:111], a[32:35], v[188:191]\n"
"s_waitcnt lgkmcnt(6)\n"
"buffer_load_dwordx3 v[88:90], v34, s[8:11], s72 offen offset:3072 sc1\n"
"ds_read_b64_tr_b16 v[128:129], v4 offset:216\n"
"ds_read_b64_tr_b16 v[130:131], v4 offset:4824\n"
"v_mfma_f32_16x16x32_bf16 v[192:195], v[112:115], a[32:35], v[192:195]\n"
"s_waitcnt lgkmcnt(6)\n"
"ds_read_b64_tr_b16 v[132:133], v4 offset:256\n"
"ds_read_b64_tr_b16 v[134:135], v4 offset:4864\n"
"v_mfma_f32_16x16x32_bf16 v[196:199], v[116:119], a[32:35], v[196:199]\n"
"s_waitcnt lgkmcnt(6)\n"
"ds_read_b64_tr_b16 v[136:137], v6 offset:144\n"
"ds_read_b64_tr_b16 v[138:139], v6 offset:4752\n"
"v_mfma_f32_16x16x32_bf16 v[180:183], v[120:123], a[36:39], v[180:183]\n"
"s_waitcnt lgkmcnt(6)\n"
"buffer_load_dwordx3 v[92:94], v34, s[8:11], s73 offen offset:0 sc1\n"
"ds_read_b64_tr_b16 v[100:101], v4 offset:9216\n"
"ds_read_b64_tr_b16 v[102:103], v4 offset:13824\n"
"v_mfma_f32_16x16x32_bf16 v[184:187], v[124:127], a[36:39], v[184:187]\n"
"s_waitcnt lgkmcnt(6)\n"
"ds_read_b64_tr_b16 v[104:105], v4 offset:9256\n"
"ds_read_b64_tr_b16 v[106:107], v4 offset:13864\n"
"v_mfma_f32_16x16x32_bf16 v[188:191], v[128:131], a[36:39], v[188:191]\n"
"s_waitcnt lgkmcnt(6)\n"
"ds_read_b64_tr_b16 v[108:109], v4 offset:9288\n"
"ds_read_b64_tr_b16 v[110:111], v4 offset:13896\n"
"v_mfma_f32_16x16x32_bf16 v[192:195], v[132:135], a[36:39], v[192:195]\n"
"s_waitcnt lgkmcnt(6)\n"
"buffer_load_dwordx3 v[96:98], v34, s[8:11], s73 offen offset:768 sc1\n"
"ds_read_b64_tr_b16 v[112:113], v4 offset:9328\n"
"ds_read_b64_tr_b16 v[114:115], v4 offset:13936\n"
"v_mfma_f32_16x16x32_bf16 v[196:199], v[136:139], a[36:39], v[196:199]\n"
"s_waitcnt lgkmcnt(6)\n"
"ds_read_b64_tr_b16 v[116:117], v6 offset:9216\n"
"ds_read_b64_tr_b16 v[118:119], v6 offset:13824\n"
"v_mfma_f32_16x16x32_bf16 v[180:183], v[100:103], a[40:43], v[180:183]\n"
"s_waitcnt lgkmcnt(6)\n"
"ds_read_b64_tr_b16 v[120:121], v4 offset:9360\n"
"ds_read_b64_tr_b16 v[122:123], v4 offset:13968\n"
"v_mfma_f32_16x16x32_bf16 v[184:187], v[104:107], a[40:43], v[184:187]\n"
"s_waitcnt lgkmcnt(6)\n"
"buffer_load_dwordx3 v[140:142], v34, s[8:11], s73 offen offset:1536 sc1\n"
"ds_read_b64_tr_b16 v[124:125], v4 offset:9400\n"
"ds_read_b64_tr_b16 v[126:127], v4 offset:14008\n"
"v_mfma_f32_16x16x32_bf16 v[188:191], v[108:111], a[40:43], v[188:191]\n"
"s_waitcnt lgkmcnt(6)\n"
"ds_read_b64_tr_b16 v[128:129], v4 offset:9432\n"
"ds_read_b64_tr_b16 v[130:131], v4 offset:14040\n"
"v_mfma_f32_16x16x32_bf16 v[192:195], v[112:115], a[40:43], v[192:195]\n"
"s_waitcnt lgkmcnt(6)\n"
"ds_read_b64_tr_b16 v[132:133], v4 offset:9472\n"
"ds_read_b64_tr_b16 v[134:135], v4 offset:14080\n"
"v_mfma_f32_16x16x32_bf16 v[196:199], v[116:119], a[40:43], v[196:199]\n"
"s_waitcnt lgkmcnt(6)\n"
"buffer_load_dwordx3 v[144:146], v34, s[8:11], s73 offen offset:2304 sc1\n"
"ds_read_b64_tr_b16 v[136:137], v6 offset:9360\n"
"ds_read_b64_tr_b16 v[138:139], v6 offset:13968\n"
"v_mfma_f32_16x16x32_bf16 v[180:183], v[120:123], a[44:47], v[180:183]\n"
"s_waitcnt lgkmcnt(6)\n"
"ds_read_b64_tr_b16 v[100:101], v4 offset:18432\n"
"ds_read_b64_tr_b16 v[102:103], v4 offset:23040\n"
"v_mfma_f32_16x16x32_bf16 v[184:187], v[124:127], a[44:47], v[184:187]\n"
"s_waitcnt lgkmcnt(6)\n"
"ds_read_b64_tr_b16 v[104:105], v4 offset:18472\n"
"ds_read_b64_tr_b16 v[106:107], v4 offset:23080\n"
"v_mfma_f32_16x16x32_bf16 v[188:191], v[128:131], a[44:47], v[188:191]\n"
"s_waitcnt lgkmcnt(6)\n"
"buffer_load_dwordx3 v[148:150], v34, s[8:11], s73 offen offset:3072 sc1\n"
"ds_read_b64_tr_b16 v[108:109], v4 offset:18504\n"
"ds_read_b64_tr_b16 v[110:111], v4 offset:23112\n"
"v_mfma_f32_16x16x32_bf16 v[192:195], v[132:135], a[44:47], v[192:195]\n"
"s_waitcnt lgkmcnt(6)\n"
"ds_read_b64_tr_b16 v[112:113], v4 offset:18544\n"
"ds_read_b64_tr_b16 v[114:115], v4 offset:23152\n"
"v_mfma_f32_16x16x32_bf16 v[196:199], v[136:139], a[44:47], v[196:199]\n"
"s_waitcnt lgkmcnt(6)\n"
"ds_read_b64_tr_b16 v[116:117], v6 offset:18432\n"
"ds_read_b64_tr_b16 v[118:119], v6 offset:23040\n"
"v_mfma_f32_16x16x32_bf16 v[180:183], v[100:103], a[48:51], v[180:183]\n"
"s_waitcnt lgkmcnt(6)\n"
"buffer_load_dwordx3 v[152:154], v34, s[8:11], s74 offen offset:0 sc1\n"
"ds_read_b64_tr_b16 v[120:121], v4 offset:18576\n"
"ds_read_b64_tr_b16 v[122:123], v4 offset:23184\n"
"v_mfma_f32_16x16x32_bf16 v[184:187], v[104:107], a[48:51], v[184:187]\n"
"s_waitcnt lgkmcnt(6)\n"
"ds_read_b64_tr_b16 v[124:125], v4 offset:18616\n"
"ds_read_b64_tr_b16 v[126:127], v4 offset:23224\n"
"v_mfma_f32_16x16x32_bf16 v[188:191], v[108:111], a[48:51], v[188:191]\n"
"s_waitcnt lgkmcnt(6)\n"
"ds_read_b64_tr_b16 v[128:129], v4 offset:18648\n"
"ds_read_b64_tr_b16 v[130:131], v4 offset:23256\n"
"v_mfma_f32_16x16x32_bf16 v[192:195], v[112:115], a[48:51], v[192:195]\n"
"s_waitcnt lgkmcnt(6)\n"
"buffer_load_dwordx3 v[156:158], v34, s[8:11], s74 offen offset:768 sc1\n"
"ds_read_b64_tr_b16 v[132:133], v4 offset:18688\n"
"ds_read_b64_tr_b16 v[134:135], v4 offset:23296\n"
"v_mfma_f32_16x16x32_bf16 v[196:199], v[116:119], a[48:51], v[196:199]\n"
"s_waitcnt lgkmcnt(6)\n"
"ds_read_b64_tr_b16 v[136:137], v6 offset:18576\n"
"ds_read_b64_tr_b16 v[138:139], v6 offset:23184\n"
"v_mfma_f32_16x16x32_bf16 v[180:183], v[120:123], a[52:55], v[180:183]\n"
"s_waitcnt lgkmcnt(6)\n"
"ds_read_b64_tr_b16 v[100:101], v4 offset:27648\n"
"ds_read_b64_tr_b16 v[102:103], v4 offset:32256\n"
"v_mfma_f32_16x16x32_bf16 v[184:187], v[124:127], a[52:55], v[184:187]\n"
"s_waitcnt lgkmcnt(6)\n"
"buffer_load_dwordx3 v[160:162], v34, s[8:11], s74 offen offset:1536 sc1\n"
"ds_read_b64_tr_b16 v[104:105], v4 offset:27688\n"
"ds_read_b64_tr_b16 v[106:107], v4 offset:32296\n"
"v_mfma_f32_16x16x32_bf16 v[188:191], v[128:131], a[52:55], v[188:191]\n"
"s_waitcnt lgkmcnt(6)\n"
"ds_read_b64_tr_b16 v[108:109], v4 offset:27720\n"
"ds_read_b64_tr_b16 v[110:111], v4 offset:32328\n"
"v_mfma_f32_16x16x32_bf16 v[192:195], v[132:135], a[52:55], v[192:195]\n"
"s_waitcnt lgkmcnt(6)\n"
"ds_read_b64_tr_b16 v[112:113], v4 offset:27760\n"
"ds_read_b64_tr_b16 v[114:115], v4 offset:32368\n"
"v_mfma_f32_16x16x32_bf16 v[196:199], v[136:139], a[52:55], v[196:199]\n"
"s_waitcnt lgkmcnt(6)\n"
"buffer_load_dwordx3 v[164:166], v34, s[8:11], s74 offen offset:2304 sc1\n"
"ds_read_b64_tr_b16 v[116:117], v6 offset:27648\n"
"ds_read_b64_tr_b16 v[118:119], v6 offset:32256\n"
"v_mfma_f32_16x16x32_bf16 v[180:183], v[100:103], a[56:59], v[180:183]\n"
"s_waitcnt lgkmcnt(6)\n"
"ds_read_b64_tr_b16 v[120:121], v4 offset:27792\n"
"ds_read_b64_tr_b16 v[122:123], v4 offset:32400\n"
"v_mfma_f32_16x16x32_bf16 v[184:187], v[104:107], a[56:59], v[184:187]\n"
"s_waitcnt lgkmcnt(6)\n"
"ds_read_b64_tr_b16 v[124:125], v4 offset:27832\n"
"ds_read_b64_tr_b16 v[126:127], v4 offset:32440\n"
"v_mfma_f32_16x16x32_bf16 v[188:191], v[108:111], a[56:59], v[188:191]\n"
"s_waitcnt lgkmcnt(6)\n"
"buffer_load_dwordx3 v[168:170], v34, s[8:11], s74 offen offset:3072 sc1\n"
"ds_read_b64_tr_b16 v[128:129], v4 offset:27864\n"
"ds_read_b64_tr_b16 v[130:131], v4 offset:32472\n"
"v_mfma_f32_16x16x32_bf16 v[192:195], v[112:115], a[56:59], v[192:195]\n"
"s_waitcnt lgkmcnt(6)\n"
"ds_read_b64_tr_b16 v[132:133], v4 offset:27904\n"
"ds_read_b64_tr_b16 v[134:135], v4 offset:32512\n"
"v_mfma_f32_16x16x32_bf16 v[196:199], v[116:119], a[56:59], v[196:199]\n"
"s_waitcnt lgkmcnt(6)\n"
"ds_read_b64_tr_b16 v[136:137], v6 offset:27792\n"
"ds_read_b64_tr_b16 v[138:139], v6 offset:32400\n"
"v_mfma_f32_16x16x32_bf16 v[180:183], v[120:123], a[60:63], v[180:183]\n"
"s_waitcnt lgkmcnt(6)\n"
"buffer_load_dwordx3 v[172:174], v34, s[8:11], s75 offen offset:0 sc1\n"
"ds_read_b64_tr_b16 v[100:101], v4 offset:36864\n"
"ds_read_b64_tr_b16 v[102:103], v4 offset:41472\n"
"v_mfma_f32_16x16x32_bf16 v[184:187], v[124:127], a[60:63], v[184:187]\n"
"s_waitcnt lgkmcnt(6)\n"
"ds_read_b64_tr_b16 v[104:105], v4 offset:36904\n"
"ds_read_b64_tr_b16 v[106:107], v4 offset:41512\n"
"v_mfma_f32_16x16x32_bf16 v[188:191], v[128:131], a[60:63], v[188:191]\n"
"s_waitcnt lgkmcnt(6)\n"
"ds_read_b64_tr_b16 v[108:109], v4 offset:36936\n"
"ds_read_b64_tr_b16 v[110:111], v4 offset:41544\n"
"v_mfma_f32_16x16x32_bf16 v[192:195], v[132:135], a[60:63], v[192:195]\n"
"s_waitcnt lgkmcnt(6)\n"
"buffer_load_dwordx3 v[176:178], v34, s[8:11], s75 offen offset:768 sc1\n"
"ds_read_b64_tr_b16 v[112:113], v4 offset:36976\n"
"ds_read_b64_tr_b16 v[114:115], v4 offset:41584\n"
"v_mfma_f32_16x16x32_bf16 v[196:199], v[136:139], a[60:63], v[196:199]\n"
"s_waitcnt lgkmcnt(6)\n"
"ds_read_b64_tr_b16 v[116:117], v6 offset:36864\n"
"ds_read_b64_tr_b16 v[118:119], v6 offset:41472\n"
"v_mfma_f32_16x16x32_bf16 v[180:183], v[100:103], a[64:67], v[180:183]\n"
"s_waitcnt lgkmcnt(6)\n"
"ds_read_b64_tr_b16 v[120:121], v4 offset:37008\n"
"ds_read_b64_tr_b16 v[122:123], v4 offset:41616\n"
"v_mfma_f32_16x16x32_bf16 v[184:187], v[104:107], a[64:67], v[184:187]\n"
"s_waitcnt lgkmcnt(6)\n"
"buffer_load_dwordx3 v[200:202], v34, s[8:11], s75 offen offset:1536 sc1\n"
"ds_read_b64_tr_b16 v[124:125], v4 offset:37048\n"
"ds_read_b64_tr_b16 v[126:127], v4 offset:41656\n"
"v_mfma_f32_16x16x32_bf16 v[188:191], v[108:111], a[64:67], v[188:191]\n"
"s_waitcnt lgkmcnt(6)\n"
"ds_read_b64_tr_b16 v[128:129], v4 offset:37080\n"
"ds_read_b64_tr_b16 v[130:131], v4 offset:41688\n"
"v_mfma_f32_16x16x32_bf16 v[192:195], v[112:115], a[64:67], v[192:195]\n"
"s_waitcnt lgkmcnt(6)\n"
"ds_read_b64_tr_b16 v[132:133], v4 offset:37120\n"
"ds_read_b64_tr_b16 v[134:135], v4 offset:41728\n"
"v_mfma_f32_16x16x32_bf16 v[196:199], v[116:119], a[64:67], v[196:199]\n"
"s_waitcnt lgkmcnt(6)\n"
"buffer_load_dwordx3 v[204:206], v34, s[8:11], s75 offen offset:2304 sc1\n"
"ds_read_b64_tr_b16 v[136:137], v6 offset:37008\n"
"ds_read_b64_tr_b16 v[138:139], v6 offset:41616\n"
"v_mfma_f32_16x16x32_bf16 v[180:183], v[120:123], a[68:71], v[180:183]\n"
"s_waitcnt lgkmcnt(6)\n"
"v_mfma_f32_16x16x32_bf16 v[184:187], v[124:127], a[68:71], v[184:187]\n"
"s_waitcnt lgkmcnt(4)\n"
"v_mfma_f32_16x16x32_bf16 v[188:191], v[128:131], a[68:71], v[188:191]\n"
"s_waitcnt lgkmcnt(2)\n"
"buffer_load_dwordx3 v[208:210], v34, s[8:11], s75 offen offset:3072 sc1\n"
"v_mfma_f32_16x16x32_bf16 v[192:195], v[132:135], a[68:71], v[192:195]\n"
"s_waitcnt lgkmcnt(0)\n"
"v_mfma_f32_16x16x32_bf16 v[196:199], v[136:139], a[68:71], v[196:199]\n"
"s_barrier\n"
"buffer_load_dwordx3 v[212:214], v34, s[8:11], s76 offen offset:0 sc1\n"
"v_pk_mul_f32 v[124:125], v[180:181], s[82:83]\n"
"v_pk_mul_f32 v[126:127], v[182:183], s[82:83]\n"
"v_max_f32 v180, 0, v180\n"
"v_max_f32 v181, 0, v181\n"
"v_max_f32 v182, 0, v182\n"
"v_max_f32 v183, 0, v183\n"
"v_exp_f32_e64 v124, v124 clamp\n"
"v_exp_f32_e64 v125, v125 clamp\n"
"buffer_load_dwordx3 v[216:218], v34, s[8:11], s76 offen offset:768 sc1\n"
"v_exp_f32_e64 v126, v126 clamp\n"
"v_exp_f32_e64 v127, v127 clamp\n"
"v_pk_add_f32 v[180:181], v[180:181], s[88:89]\n"
"v_pk_add_f32 v[182:183], v[182:183], s[88:89]\n"
"v_pk_add_f32 v[180:181], v[180:181], v[124:125]\n"
"v_pk_add_f32 v[182:183], v[182:183], v[126:127]\n"
"v_pk_mul_f32 v[124:125], v[184:185], s[82:83]\n"
"v_pk_mul_f32 v[126:127], v[186:187], s[82:83]\n"
"buffer_load_dwordx3 v[220:222], v34, s[8:11], s76 offen offset:1536 sc1\n"
"v_max_f32 v184, 0, v184\n"
"v_max_f32 v185, 0, v185\n"
"v_max_f32 v186, 0, v186\n"
"v_max_f32 v187, 0, v187\n"
"v_exp_f32_e64 v124, v124 clamp\n"
"v_exp_f32_e64 v125, v125 clamp\n"
"v_exp_f32_e64 v126, v126 clamp\n"
"buffer_load_dwordx3 v[224:226], v34, s[8:11], s76 offen offset:2304 sc1\n"
"v_exp_f32_e64 v127, v127 clamp\n"
"v_pk_add_f32 v[184:185], v[184:185], s[88:89]\n"
"v_pk_add_f32 v[186:187], v[186:187], s[88:89]\n"
"v_pk_add_f32 v[184:185], v[184:185], v[124:125]\n"
"v_pk_add_f32 v[186:187], v[186:187], v[126:127]\n"
"v_pk_mul_f32 v[124:125], v[188:189], s[82:83]\n"
"v_pk_mul_f32 v[126:127], v[190:191], s[82:83]\n"
"v_max_f32 v188, 0, v188\n"
"buffer_load_dwordx3 v[228:230], v34, s[8:11], s76 offen offset:3072 sc1\n"
"v_max_f32 v189, 0, v189\n"
"v_max_f32 v190, 0, v190\n"
"v_max_f32 v191, 0, v191\n"
"v_exp_f32_e64 v124, v124 clamp\n"
"v_exp_f32_e64 v125, v125 clamp\n"
"v_exp_f32_e64 v126, v126 clamp\n"
"v_exp_f32_e64 v127, v127 clamp\n"
"v_pk_add_f32 v[188:189], v[188:189], s[88:89]\n"
"buffer_load_dwordx3 v[232:234], v34, s[8:11], s77 offen offset:0 sc1\n"
"v_pk_add_f32 v[190:191], v[190:191], s[88:89]\n"
"v_pk_add_f32 v[188:189], v[188:189], v[124:125]\n"
"v_pk_add_f32 v[190:191], v[190:191], v[126:127]\n"
"v_pk_mul_f32 v[124:125], v[192:193], s[82:83]\n"
"v_pk_mul_f32 v[126:127], v[194:195], s[82:83]\n"
"v_max_f32 v192, 0, v192\n"
"v_max_f32 v193, 0, v193\n"
"buffer_load_dwordx3 v[236:238], v34, s[8:11], s77 offen offset:768 sc1\n"
"v_max_f32 v194, 0, v194\n"
"v_max_f32 v195, 0, v195\n"
"v_exp_f32_e64 v124, v124 clamp\n"
"v_exp_f32_e64 v125, v125 clamp\n"
"v_exp_f32_e64 v126, v126 clamp\n"
"v_exp_f32_e64 v127, v127 clamp\n"
"v_pk_add_f32 v[192:193], v[192:193], s[88:89]\n"
"v_pk_add_f32 v[194:195], v[194:195], s[88:89]\n"
"buffer_load_dwordx3 v[240:242], v34, s[8:11], s77 offen offset:1536 sc1\n"
"v_pk_add_f32 v[192:193], v[192:193], v[124:125]\n"
"v_pk_add_f32 v[194:195], v[194:195], v[126:127]\n"
"v_pk_mul_f32 v[124:125], v[196:197], s[82:83]\n"
"v_pk_mul_f32 v[126:127], v[198:199], s[82:83]\n"
"v_max_f32 v196, 0, v196\n"
"v_max_f32 v197, 0, v197\n"
"v_max_f32 v198, 0, v198\n"
"v_max_f32 v199, 0, v199\n"
"buffer_load_dwordx3 v[244:246], v34, s[8:11], s77 offen offset:2304 sc1\n"
"v_exp_f32_e64 v124, v124 clamp\n"
"v_exp_f32_e64 v125, v125 clamp\n"
"v_exp_f32_e64 v126, v126 clamp\n"
"v_exp_f32_e64 v127, v127 clamp\n"
"v_pk_add_f32 v[196:197], v[196:197], s[88:89]\n"
"v_pk_add_f32 v[198:199], v[198:199], s[88:89]\n"
"v_pk_add_f32 v[196:197], v[196:197], v[124:125]\n"
"buffer_load_dwordx3 v[248:250], v34, s[8:11], s77 offen offset:3072 sc1\n"
"v_pk_add_f32 v[198:199], v[198:199], v[126:127]\n"
"v_cvt_pk_bf16_f32 v100, v180, v181\n"
"v_cvt_pk_bf16_f32 v101, v182, v183\n"
"v_cvt_pk_bf16_f32 v102, v196, v197\n"
"v_cvt_pk_bf16_f32 v103, v198, v199\n"
"v_cvt_pk_bf16_f32 v104, v184, v185\n"
"v_cvt_pk_bf16_f32 v105, v186, v187\n"
"v_cvt_pk_bf16_f32 v106, v196, v197\n"
"buffer_load_dwordx3 v[252:254], v34, s[8:11], s78 offen offset:0 sc1\n"
"v_cvt_pk_bf16_f32 v107, v198, v199\n"
"v_cvt_pk_bf16_f32 v108, v188, v189\n"
"v_cvt_pk_bf16_f32 v109, v190, v191\n"
"v_cvt_pk_bf16_f32 v110, v196, v197\n"
"v_cvt_pk_bf16_f32 v111, v198, v199\n"
"v_cvt_pk_bf16_f32 v112, v192, v193\n"
"v_cvt_pk_bf16_f32 v113, v194, v195\n"
"v_cvt_pk_bf16_f32 v114, v196, v197\n"
"buffer_load_dwordx3 v[68:70], v34, s[8:11], s78 offen offset:768 sc1\n"
"v_cvt_pk_bf16_f32 v115, v198, v199\n"
"v_mov_b32 v116, 0\n"
"v_mov_b32 v117, 0\n"
"v_mov_b32 v118, 0\n"
"v_mov_b32 v119, 0\n"
"v_mov_b32 v120, 0\n"
"v_mov_b32 v121, 0\n"
"s_waitcnt vmcnt(30)\n"
"v_dot2c_f32_bf16 v116, s7, v72\n"
"v_dot2c_f32_bf16 v117, s34, v72\n"
"v_dot2c_f32_bf16 v118, s7, v73\n"
"v_dot2c_f32_bf16 v119, s34, v73\n"
"v_dot2c_f32_bf16 v120, s7, v74\n"
"v_dot2c_f32_bf16 v121, s34, v74\n"
"s_mul_i32 s72, s32, 18432\n"
"s_lshl_b32 s73, s29, 7\n"
"s_add_u32 s73, s73, s72\n"
"s_add_u32 s74, s73, 9216\n"
"s_add_u32 s75, s72, 1024\n"
"s_add_u32 s76, s72, 10240\n"
"s_lshl_b32 s77, s26, 12\n"
"v_dot2c_f32_bf16 v116, s7, v76\n"
"v_dot2c_f32_bf16 v117, s34, v76\n"
"v_dot2c_f32_bf16 v118, s7, v77\n"
"v_dot2c_f32_bf16 v119, s34, v77\n"
"v_dot2c_f32_bf16 v120, s7, v78\n"
"v_dot2c_f32_bf16 v121, s34, v78\n"
"s_waitcnt vmcnt(28)\n"
"v_dot2c_f32_bf16 v116, s7, v80\n"
"v_dot2c_f32_bf16 v117, s34, v80\n"
"v_dot2c_f32_bf16 v118, s7, v81\n"
"v_dot2c_f32_bf16 v119, s34, v81\n"
"v_dot2c_f32_bf16 v120, s7, v82\n"
"v_dot2c_f32_bf16 v121, s34, v82\n"
"s_add_u32 m0, s77, 90112\n"
"s_nop 0\n"
"buffer_load_dwordx4 v10, s[12:15], s73 offen lds\n"
"v_dot2c_f32_bf16 v116, s7, v84\n"
"v_dot2c_f32_bf16 v117, s34, v84\n"
"v_dot2c_f32_bf16 v118, s7, v85\n"
"v_dot2c_f32_bf16 v119, s34, v85\n"
"v_dot2c_f32_bf16 v120, s7, v86\n"
"v_dot2c_f32_bf16 v121, s34, v86\n"
"s_waitcnt vmcnt(27)\n"
"v_dot2c_f32_bf16 v116, s7, v88\n"
"v_dot2c_f32_bf16 v117, s34, v88\n"
"v_dot2c_f32_bf16 v118, s7, v89\n"
"v_dot2c_f32_bf16 v119, s34, v89\n"
"v_dot2c_f32_bf16 v120, s7, v90\n"
"v_dot2c_f32_bf16 v121, s34, v90\n"
"s_add_u32 m0, s77, 91136\n"
"s_nop 0\n"
"buffer_load_dwordx4 v10, s[12:15], s74 offen lds\n"
"v_dot2c_f32_bf16 v116, s7, v92\n"
"v_dot2c_f32_bf16 v117, s34, v92\n"
"v_dot2c_f32_bf16 v118, s7, v93\n"
"v_dot2c_f32_bf16 v119, s34, v93\n"
"v_dot2c_f32_bf16 v120, s7, v94\n"
"v_dot2c_f32_bf16 v121, s34, v94\n"
"s_waitcnt vmcnt(26)\n"
"v_dot2c_f32_bf16 v116, s7, v96\n"
"v_dot2c_f32_bf16 v117, s34, v96\n"
"v_dot2c_f32_bf16 v118, s7, v97\n"
"v_dot2c_f32_bf16 v119, s34, v97\n"
"v_dot2c_f32_bf16 v120, s7, v98\n"
"v_dot2c_f32_bf16 v121, s34, v98\n"
"s_add_u32 m0, s77, 92160\n"
"s_nop 0\n"
"buffer_load_dwordx4 v10, s[12:15], s75 offen lds\n"
"v_dot2c_f32_bf16 v116, s7, v140\n"
"v_dot2c_f32_bf16 v117, s34, v140\n"
"v_dot2c_f32_bf16 v118, s7, v141\n"
"v_dot2c_f32_bf16 v119, s34, v141\n"
"v_dot2c_f32_bf16 v120, s7, v142\n"
"v_dot2c_f32_bf16 v121, s34, v142\n"
"s_waitcnt vmcnt(25)\n"
"v_dot2c_f32_bf16 v116, s7, v144\n"
"v_dot2c_f32_bf16 v117, s34, v144\n"
"v_dot2c_f32_bf16 v118, s7, v145\n"
"v_dot2c_f32_bf16 v119, s34, v145\n"
"v_dot2c_f32_bf16 v120, s7, v146\n"
"v_dot2c_f32_bf16 v121, s34, v146\n"
"s_add_u32 m0, s77, 93184\n"
"s_nop 0\n"
"buffer_load_dwordx4 v10, s[12:15], s76 offen lds\n"
"v_dot2c_f32_bf16 v116, s7, v148\n"
"v_dot2c_f32_bf16 v117, s34, v148\n"
"v_dot2c_f32_bf16 v118, s7, v149\n"
"v_dot2c_f32_bf16 v119, s34, v149\n"
"v_dot2c_f32_bf16 v120, s7, v150\n"
"v_dot2c_f32_bf16 v121, s34, v150\n"
"s_waitcnt vmcnt(24)\n"
"v_dot2c_f32_bf16 v116, s7, v152\n"
"v_dot2c_f32_bf16 v117, s34, v152\n"
"v_dot2c_f32_bf16 v118, s7, v153\n"
"v_dot2c_f32_bf16 v119, s34, v153\n"
"v_dot2c_f32_bf16 v120, s7, v154\n"
"v_dot2c_f32_bf16 v121, s34, v154\n"
"s_sub_u32 s91, s26, 0\n"
"s_mul_i32 s91, s91, 10240\n"
"s_mul_i32 s90, s29, 82944\n"
"s_add_u32 s90, s90, s91\n"
"s_add_u32 s90, s90, 2097152\n"
"s_add_u32 s91, s91, 0\n"
"s_add_u32 m0, s91, 0\n"
"s_nop 0\n"
"buffer_load_dwordx4 v1, s[8:11], s90 offen offset:0 sc1 lds\n"
"v_dot2c_f32_bf16 v116, s7, v156\n"
"v_dot2c_f32_bf16 v117, s34, v156\n"
"v_dot2c_f32_bf16 v118, s7, v157\n"
"v_dot2c_f32_bf16 v119, s34, v157\n"
"v_dot2c_f32_bf16 v120, s7, v158\n"
"v_dot2c_f32_bf16 v121, s34, v158\n"
"s_waitcnt vmcnt(23)\n"
"v_dot2c_f32_bf16 v116, s7, v160\n"
"v_dot2c_f32_bf16 v117, s34, v160\n"
"v_dot2c_f32_bf16 v118, s7, v161\n"
"v_dot2c_f32_bf16 v119, s34, v161\n"
"v_dot2c_f32_bf16 v120, s7, v162\n"
"v_dot2c_f32_bf16 v121, s34, v162\n"
"buffer_load_dwordx4 v1, s[8:11], s90 offen offset:1024 sc1 lds\n"
"v_dot2c_f32_bf16 v116, s7, v164\n"
"v_dot2c_f32_bf16 v117, s34, v164\n"
"v_dot2c_f32_bf16 v118, s7, v165\n"
"v_dot2c_f32_bf16 v119, s34, v165\n"
"v_dot2c_f32_bf16 v120, s7, v166\n"
"v_dot2c_f32_bf16 v121, s34, v166\n"
"s_waitcnt vmcnt(22)\n"
"v_dot2c_f32_bf16 v116, s7, v168\n"
"v_dot2c_f32_bf16 v117, s34, v168\n"
"v_dot2c_f32_bf16 v118, s7, v169\n"
"v_dot2c_f32_bf16 v119, s34, v169\n"
"v_dot2c_f32_bf16 v120, s7, v170\n"
"v_dot2c_f32_bf16 v121, s34, v170\n"
"buffer_load_dwordx4 v1, s[8:11], s90 offen offset:2048 sc1 lds\n"
"v_dot2c_f32_bf16 v116, s7, v172\n"
"v_dot2c_f32_bf16 v117, s34, v172\n"
"v_dot2c_f32_bf16 v118, s7, v173\n"
"v_dot2c_f32_bf16 v119, s34, v173\n"
"v_dot2c_f32_bf16 v120, s7, v174\n"
"v_dot2c_f32_bf16 v121, s34, v174\n"
"s_waitcnt vmcnt(21)\n"
"v_dot2c_f32_bf16 v116, s7, v176\n"
"v_dot2c_f32_bf16 v117, s34, v176\n"
"v_dot2c_f32_bf16 v118, s7, v177\n"
"v_dot2c_f32_bf16 v119, s34, v177\n"
"v_dot2c_f32_bf16 v120, s7, v178\n"
"v_dot2c_f32_bf16 v121, s34, v178\n"
"v_dot2c_f32_bf16 v116, s7, v200\n"
"v_dot2c_f32_bf16 v117, s34, v200\n"
"v_dot2c_f32_bf16 v118, s7, v201\n"
"v_dot2c_f32_bf16 v119, s34, v201\n"
"v_dot2c_f32_bf16 v120, s7, v202\n"
"v_dot2c_f32_bf16 v121, s34, v202\n"
"buffer_load_dwordx4 v1, s[8:11], s90 offen offset:3072 sc1 lds\n"
"s_waitcnt vmcnt(20)\n"
"v_dot2c_f32_bf16 v116, s7, v204\n"
"v_dot2c_f32_bf16 v117, s34, v204\n"
"v_dot2c_f32_bf16 v118, s7, v205\n"
"v_dot2c_f32_bf16 v119, s34, v205\n"
"v_dot2c_f32_bf16 v120, s7, v206\n"
"v_dot2c_f32_bf16 v121, s34, v206\n"
"v_dot2c_f32_bf16 v116, s7, v208\n"
"v_dot2c_f32_bf16 v117, s34, v208\n"
"v_dot2c_f32_bf16 v118, s7, v209\n"
"v_dot2c_f32_bf16 v119, s34, v209\n"
"v_dot2c_f32_bf16 v120, s7, v210\n"
"v_dot2c_f32_bf16 v121, s34, v210\n"
"s_add_u32 s90, s90, 4096\n"
"s_add_u32 m0, s91, 4096\n"
"s_nop 0\n"
"buffer_load_dwordx4 v1, s[8:11], s90 offen offset:0 sc1 lds\n"
"s_waitcnt vmcnt(19)\n"
"v_dot2c_f32_bf16 v116, s7, v212\n"
"v_dot2c_f32_bf16 v117, s34, v212\n"
"v_dot2c_f32_bf16 v118, s7, v213\n"
"v_dot2c_f32_bf16 v119, s34, v213\n"
"v_dot2c_f32_bf16 v120, s7, v214\n"
"v_dot2c_f32_bf16 v121, s34, v214\n"
"v_dot2c_f32_bf16 v116, s7, v216\n"
"v_dot2c_f32_bf16 v117, s34, v216\n"
"v_dot2c_f32_bf16 v118, s7, v217\n"
"v_dot2c_f32_bf16 v119, s34, v217\n"
"v_dot2c_f32_bf16 v120, s7, v218\n"
"v_dot2c_f32_bf16 v121, s34, v218\n"
"buffer_load_dwordx4 v1, s[8:11], s90 offen offset:1024 sc1 lds\n"
"s_waitcnt vmcnt(18)\n"
"v_dot2c_f32_bf16 v116, s7, v220\n"
"v_dot2c_f32_bf16 v117, s34, v220\n"
"v_dot2c_f32_bf16 v118, s7, v221\n"
"v_dot2c_f32_bf16 v119, s34, v221\n"
"v_dot2c_f32_bf16 v120, s7, v222\n"
"v_dot2c_f32_bf16 v121, s34, v222\n"
"v_dot2c_f32_bf16 v116, s7, v224\n"
"v_dot2c_f32_bf16 v117, s34, v224\n"
"v_dot2c_f32_bf16 v118, s7, v225\n"
"v_dot2c_f32_bf16 v119, s34, v225\n"
"v_dot2c_f32_bf16 v120, s7, v226\n"
"v_dot2c_f32_bf16 v121, s34, v226\n"
"buffer_load_dwordx4 v1, s[8:11], s90 offen offset:2048 sc1 lds\n"
"s_waitcnt vmcnt(17)\n"
"v_dot2c_f32_bf16 v116, s7, v228\n"
"v_dot2c_f32_bf16 v117, s34, v228\n"
"v_dot2c_f32_bf16 v118, s7, v229\n"
"v_dot2c_f32_bf16 v119, s34, v229\n"
"v_dot2c_f32_bf16 v120, s7, v230\n"
"v_dot2c_f32_bf16 v121, s34, v230\n"
"v_dot2c_f32_bf16 v116, s7, v232\n"
"v_dot2c_f32_bf16 v117, s34, v232\n"
"v_dot2c_f32_bf16 v118, s7, v233\n"
"v_dot2c_f32_bf16 v119, s34, v233\n"
"v_dot2c_f32_bf16 v120, s7, v234\n"
"v_dot2c_f32_bf16 v121, s34, v234\n"
"buffer_load_dwordx4 v1, s[8:11], s90 offen offset:3072 sc1 lds\n"
"s_waitcnt vmcnt(16)\n"
"v_dot2c_f32_bf16 v116, s7, v236\n"
"v_dot2c_f32_bf16 v117, s34, v236\n"
"v_dot2c_f32_bf16 v118, s7, v237\n"
"v_dot2c_f32_bf16 v119, s34, v237\n"
"v_dot2c_f32_bf16 v120, s7, v238\n"
"v_dot2c_f32_bf16 v121, s34, v238\n"
"v_dot2c_f32_bf16 v116, s7, v240\n"
"v_dot2c_f32_bf16 v117, s34, v240\n"
"v_dot2c_f32_bf16 v118, s7, v241\n"
"v_dot2c_f32_bf16 v119, s34, v241\n"
"v_dot2c_f32_bf16 v120, s7, v242\n"
"v_dot2c_f32_bf16 v121, s34, v242\n"
"s_add_u32 s90, s90, 4096\n"
"s_add_u32 m0, s91, 8192\n"
"s_nop 0\n"
"buffer_load_dwordx4 v1, s[8:11], s90 offen offset:0 sc1 lds\n"
"s_waitcnt vmcnt(15)\n"
"v_dot2c_f32_bf16 v116, s7, v244\n"
"v_dot2c_f32_bf16 v117, s34, v244\n"
"v_dot2c_f32_bf16 v118, s7, v245\n"
"v_dot2c_f32_bf16 v119, s34, v245\n"
"v_dot2c_f32_bf16 v120, s7, v246\n"
"v_dot2c_f32_bf16 v121, s34, v246\n"
"v_dot2c_f32_bf16 v116, s7, v248\n"
"v_dot2c_f32_bf16 v117, s34, v248\n"
"v_dot2c_f32_bf16 v118, s7, v249\n"
"v_dot2c_f32_bf16 v119, s34, v249\n"
"v_dot2c_f32_bf16 v120, s7, v250\n"
"v_dot2c_f32_bf16 v121, s34, v250\n"
"buffer_load_dwordx4 v1, s[8:11], s90 offen offset:1024 sc1 lds\n"
"s_waitcnt vmcnt(14)\n"
"v_dot2c_f32_bf16 v116, s7, v252\n"
"v_dot2c_f32_bf16 v117, s34, v252\n"
"v_dot2c_f32_bf16 v118, s7, v253\n"
"v_dot2c_f32_bf16 v119, s34, v253\n"
"v_dot2c_f32_bf16 v120, s7, v254\n"
"v_dot2c_f32_bf16 v121, s34, v254\n"
"v_dot2c_f32_bf16 v116, s7, v68\n"
"v_dot2c_f32_bf16 v117, s34, v68\n"
"v_dot2c_f32_bf16 v118, s7, v69\n"
"v_dot2c_f32_bf16 v119, s34, v69\n"
"v_dot2c_f32_bf16 v120, s7, v70\n"
"v_dot2c_f32_bf16 v121, s34, v70\n"
"v_cvt_pk_bf16_f32 v116, v116, v117\n"
"v_cvt_pk_bf16_f32 v117, v118, v119\n"
"s_nop 0\n"
"v_cvt_pk_bf16_f32 v118, v120, v121\n"
"ds_write2_b32 v35, v116, v117 offset1:1\n"
"ds_write_b32 v35, v118 offset:8\n"
"v_mov_b32 v143, 0\n"
"v_mov_b32 v147, 0\n"
"v_mov_b32 v150, 0\n"
"v_mov_b32 v154, 0\n"
"v_mov_b32 v159, 0\n"
"v_mov_b32 v163, 0\n"
"v_mov_b32 v166, 0\n"
"v_mov_b32 v170, 0\n"
"s_lshl_b32 s92, s29, 11\n"
"s_add_u32 s92, s92, 5242880\n"
"s_lshl_b32 s93, s29, 8\n"
"s_add_u32 s93, s93, 5259264\n"
"s_waitcnt lgkmcnt(0)\n"
"s_barrier\n"
"ds_read_b64 v[144:145], v37 offset:0\n"
"ds_read_b32 v146, v40 offset:0\n"
"ds_read_b64 v[152:153], v37 offset:768\n"
"ds_read_b32 v155, v40 offset:768\n"
"ds_read_b64 v[160:161], v37 offset:1536\n"
"ds_read_b32 v162, v41 offset:1536\n"
"ds_read_b64 v[168:169], v37 offset:2304\n"
"ds_read_b32 v171, v41 offset:2304\n"
"ds_read_b64 v[140:141], v36 offset:0\n"
"ds_read_b32 v142, v38 offset:0\n"
"ds_read_b64 v[148:149], v36 offset:768\n"
"ds_read_b32 v151, v38 offset:768\n"
"ds_read_b64 v[156:157], v36 offset:1536\n"
"ds_read_b32 v158, v39 offset:1536\n"
"s_waitcnt lgkmcnt(12)\n"
"ds_read_b64 v[164:165], v36 offset:2304\n"
"ds_read_b32 v167, v39 offset:2304\n"
"s_waitcnt lgkmcnt(14)\n"
"v_mfma_f32_16x16x32_bf16 v[76:79], v[144:147], v[100:103], 0\n"
"s_waitcnt lgkmcnt(12)\n"
"v_mfma_f32_16x16x32_bf16 v[84:87], v[152:155], v[104:107], 0\n"
"s_waitcnt lgkmcnt(10)\n"
"v_mfma_f32_16x16x32_bf16 v[92:95], v[160:163], v[108:111], 0\n"
"s_waitcnt lgkmcnt(8)\n"
"v_mfma_f32_16x16x32_bf16 v[128:131], v[168:171], v[112:115], 0\n"
"s_waitcnt lgkmcnt(6)\n"
"v_mfma_f32_16x16x32_bf16 v[72:75], v[140:143], v[100:103], 0\n"
"s_waitcnt lgkmcnt(4)\n"
"v_mfma_f32_16x16x32_bf16 v[80:83], v[148:151], v[104:107], 0\n"
"s_waitcnt lgkmcnt(2)\n"
"v_mfma_f32_16x16x32_bf16 v[88:91], v[156:159], v[108:111], 0\n"
"s_waitcnt lgkmcnt(0)\n"
"v_mfma_f32_16x16x32_bf16 v[124:127], v[164:167], v[112:115], 0\n"
"v_add_f32 v78, 0x358637bd, v78\n"
"v_add_f32 v86, 0x358637bd, v86\n"
"v_add_f32 v94, 0x358637bd, v94\n"
"v_add_f32 v130, 0x358637bd, v130\n"
"v_rcp_f32 v78, v78\n"
"v_rcp_f32 v86, v86\n"
"v_rcp_f32 v94, v94\n"
"v_rcp_f32 v130, v130\n"
"v_pk_mul_f32 v[72:73], v[72:73], v[78:79] op_sel_hi:[1,0]\n"
"v_pk_mul_f32 v[74:75], v[74:75], v[78:79] op_sel_hi:[1,0]\n"
"v_cvt_pk_bf16_f32 v172, v72, v73\n"
"v_cvt_pk_bf16_f32 v173, v74, v75\n"
"v_pk_mul_f32 v[80:81], v[80:81], v[86:87] op_sel_hi:[1,0]\n"
"v_pk_mul_f32 v[82:83], v[82:83], v[86:87] op_sel_hi:[1,0]\n"
"v_cvt_pk_bf16_f32 v174, v80, v81\n"
"v_cvt_pk_bf16_f32 v175, v82, v83\n"
"buffer_store_dwordx4 v[172:175], v42, s[8:11], s92 offen sc1\n"
"v_pk_mul_f32 v[88:89], v[88:89], v[94:95] op_sel_hi:[1,0]\n"
"v_pk_mul_f32 v[90:91], v[90:91], v[94:95] op_sel_hi:[1,0]\n"
"v_cvt_pk_bf16_f32 v176, v88, v89\n"
"v_cvt_pk_bf16_f32 v177, v90, v91\n"
"v_pk_mul_f32 v[124:125], v[124:125], v[130:131] op_sel_hi:[1,0]\n"
"v_pk_mul_f32 v[126:127], v[126:127], v[130:131] op_sel_hi:[1,0]\n"
"v_cvt_pk_bf16_f32 v178, v124, v125\n"
"v_cvt_pk_bf16_f32 v179, v126, v127\n"
"buffer_store_dwordx4 v[176:179], v42, s[8:11], s92 offen offset:1024 sc1\n"
"v_pk_mul_f32 v[76:77], v[76:77], v[78:79] op_sel_hi:[1,0]\n"
"v_pk_mul_f32 v[84:85], v[84:85], v[86:87] op_sel_hi:[1,0]\n"
"v_pk_mul_f32 v[92:93], v[92:93], v[94:95] op_sel_hi:[1,0]\n"
"v_pk_mul_f32 v[128:129], v[128:129], v[130:131] op_sel_hi:[1,0]\n"
"v_cvt_pk_bf16_f32 v180, v76, v77\n"
"v_cvt_pk_bf16_f32 v181, v84, v85\n"
"v_cvt_pk_bf16_f32 v182, v92, v93\n"
"v_cvt_pk_bf16_f32 v183, v128, v129\n"
"buffer_store_dwordx4 v[180:183], v43, s[8:11], s93 offen sc1\n"
"s_cmp_lg_u32 s26, 0\n"
"s_cbranch_scc0 .Lfused_block_kernel_s33\n"
"s_sub_u32 s91, s26, 1\n"
"s_mul_i32 s91, s91, 14336\n"
"s_mul_i32 s90, s29, 82944\n"
"s_add_u32 s90, s90, s91\n"
"s_add_u32 s90, s90, 2137088\n"
"s_add_u32 s91, s91, 39936\n"
"s_add_u32 m0, s91, 0\n"
"s_nop 0\n"
"buffer_load_dwordx4 v1, s[8:11], s90 offen offset:0 sc1 lds\n"
"buffer_load_dwordx4 v1, s[8:11], s90 offen offset:1024 sc1 lds\n"
"buffer_load_dwordx4 v1, s[8:11], s90 offen offset:2048 sc1 lds\n"
"buffer_load_dwordx4 v1, s[8:11], s90 offen offset:3072 sc1 lds\n"
"s_add_u32 s90, s90, 4096\n"
"s_add_u32 m0, s91, 4096\n"
"s_nop 0\n"
"buffer_load_dwordx4 v1, s[8:11], s90 offen offset:0 sc1 lds\n"
"buffer_load_dwordx4 v1, s[8:11], s90 offen offset:1024 sc1 lds\n"
".Lfused_block_kernel_s33:\n"
"s_cmp_lg_u32 s26, 0\n"
"s_cbranch_scc0 .Lfused_block_kernel_s34\n"
"s_waitcnt vmcnt(6)\n"
".Lfused_block_kernel_s34:\n"
"s_cmp_eq_u32 s26, 0\n"
"s_cbranch_scc0 .Lfused_block_kernel_s35\n"
"s_waitcnt vmcnt(0)\n"
".Lfused_block_kernel_s35:\n"
"s_barrier\n"
"s_cmp_eq_u32 s26, 0\n"
"s_cbranch_scc0 .Lfused_block_kernel_s36\n"
"buffer_store_dwordx2 v[22:23], v47, s[8:11], s30 offen offset:256 sc1\n"
".Lfused_block_kernel_s36:\n"
"s_cmp_lg_u32 s26, 0\n"
"s_cbranch_scc0 .Lfused_block_kernel_s37\n"
"buffer_load_dwordx4 v1, s[8:11], s90 offen offset:2048 sc1 lds\n"
"buffer_load_dwordx4 v1, s[8:11], s90 offen offset:3072 sc1 lds\n"
"s_add_u32 s90, s90, 4096\n"
"s_add_u32 m0, s91, 8192\n"
"s_nop 0\n"
"buffer_load_dwordx4 v1, s[8:11], s90 offen offset:0 sc1 lds\n"
"buffer_load_dwordx4 v1, s[8:11], s90 offen offset:1024 sc1 lds\n"
"buffer_load_dwordx4 v1, s[8:11], s90 offen offset:2048 sc1 lds\n"
"buffer_load_dwordx4 v1, s[8:11], s90 offen offset:3072 sc1 lds\n"
"s_add_u32 s90, s90, 4096\n"
"s_add_u32 m0, s91, 12288\n"
"s_nop 0\n"
"buffer_load_dwordx4 v1, s[8:11], s90 offen offset:0 sc1 lds\n"
"buffer_load_dwordx4 v1, s[8:11], s90 offen offset:1024 sc1 lds\n"
"s_sub_u32 s90, s26, 1\n"
"s_lshl_b32 s90, s90, 10\n"
"s_add_u32 m0, s90, 82944\n"
"s_add_u32 s90, s90, 720896\n"
"buffer_load_dwordx4 v1, s[8:11], s90 offen sc1 lds\n"
"s_sub_u32 s92, 3, s26\n"
"s_lshl_b32 s92, s92, 8\n"
"s_add_u32 s92, s92, s33\n"
"s_mov_b32 s70, 0\n"
"s_cmp_lt_u32 s92, 648\n"
"s_cbranch_scc0 .Lfused_block_kernel_s38\n"
"s_mov_b32 s20, s46\n"
"s_and_b32 s21, s47, 0xffff\n"
"s_mov_b32 s22, 5308416\n"
"s_mov_b32 s23, 0x20000\n"
"s_mov_b32 s70, 2\n"
"s_mul_i32 s81, s92, 7282\n"
"s_lshr_b32 s81, s81, 16\n"
"s_mul_i32 s89, s81, 9\n"
"s_sub_u32 s89, s92, s89\n"
"s_mov_b32 s88, 1\n"
"s_cmp_eq_u32 s89, 8\n"
"s_cselect_b32 s79, 4, 0\n"
"s_add_u32 s79, s79, s88\n"
"s_cmp_eq_u32 s79, 1\n"
"s_cbranch_scc0 .Lfused_block_kernel_s39\n"
"s_mul_i32 s72, s89, 64\n"
"s_mov_b32 s73, 32\n"
"s_mov_b32 s74, 1\n"
"s_mov_b32 s75, 4\n"
"s_mov_b32 s76, 8\n"
"s_mov_b32 s77, 16\n"
"s_mov_b32 s78, 2\n"
".Lfused_block_kernel_s39:\n"
"s_cmp_eq_u32 s79, 5\n"
"s_cbranch_scc0 .Lfused_block_kernel_s40\n"
"s_mov_b32 s72, 512\n"
"s_mov_b32 s73, 1\n"
"s_mov_b32 s74, 2\n"
"s_mov_b32 s75, 4\n"
"s_mov_b32 s76, 8\n"
"s_mov_b32 s77, 16\n"
"s_mov_b32 s78, 32\n"
".Lfused_block_kernel_s40:\n"
"s_lshl_b32 s83, s89, 11\n"
"s_mul_i32 s84, s81, 7282\n"
"s_lshr_b32 s84, s84, 16\n"
"s_mul_i32 s85, s84, 9\n"
"s_sub_u32 s85, s81, s85\n"
"s_mul_i32 s85, s85, 18432\n"
"s_mov_b32 s80, 9216\n"
"s_mul_i32 s84, s84, 1327104\n"
"s_add_u32 s83, s83, s84\n"
"s_add_u32 s83, s83, s85\n"
"s_add_u32 s83, s83, 18874368\n"
"s_lshl_b32 s81, s81, 7\n"
"s_mov_b32 s82, 16\n"
"s_mov_b32 s84, 16\n"
"s_mov_b32 s85, 128\n"
"s_mov_b32 s86, 1024\n"
"s_mov_b32 s87, 512\n"
"s_mov_b32 s88, 331776\n"
"s_mov_b32 s89, 663552\n"
"s_mul_i32 s72, s72, s80\n"
"s_mul_i32 s73, s73, s80\n"
"s_mul_i32 s74, s74, s80\n"
"s_mul_i32 s75, s75, s80\n"
"s_mul_i32 s76, s76, s80\n"
"s_mul_i32 s77, s77, s80\n"
"s_mul_i32 s78, s78, s80\n"
"s_add_u32 s54, s72, s81\n"
"s_mov_b32 s62, s83\n"
"s_add_u32 s55, s54, s76\n"
"s_add_u32 s63, s62, s87\n"
"s_add_u32 s56, s54, s77\n"
"s_add_u32 s64, s62, s88\n"
"s_add_u32 s57, s55, s77\n"
"s_add_u32 s65, s63, s88\n"
"s_add_u32 s58, s54, s78\n"
"s_add_u32 s66, s62, s89\n"
"s_add_u32 s59, s55, s78\n"
"s_add_u32 s67, s63, s89\n"
"s_add_u32 s60, s56, s78\n"
"s_add_u32 s68, s64, s89\n"
"s_add_u32 s61, s57, s78\n"
"s_add_u32 s69, s65, s89\n"
"v_and_b32 v68, 7, v0\n"
"v_mul_u32_u24 v60, s82, v68\n"
"v_mul_u32_u24 v61, s84, v68\n"
"v_bfe_u32 v68, v0, 3, 1\n"
"v_mad_u32_u24 v60, v68, s73, v60\n"
"v_bfe_u32 v68, v0, 4, 1\n"
"v_mad_u32_u24 v60, v68, s74, v60\n"
"v_bfe_u32 v68, v0, 3, 2\n"
"v_mad_u32_u24 v61, v68, s85, v61\n"
"v_lshrrev_b32 v68, 5, v0\n"
"v_mad_u32_u24 v60, v68, s75, v60\n"
"v_mad_u32_u24 v61, v68, s86, v61\n"
"buffer_load_dwordx4 v[224:227], v60, s[20:23], s54 offen\n"
"buffer_load_dwordx4 v[228:231], v60, s[20:23], s55 offen\n"
"buffer_load_dwordx4 v[232:235], v60, s[20:23], s56 offen\n"
"buffer_load_dwordx4 v[236:239], v60, s[20:23], s57 offen\n"
"buffer_load_dwordx4 v[240:243], v60, s[20:23], s58 offen\n"
"buffer_load_dwordx4 v[244:247], v60, s[20:23], s59 offen\n"
"buffer_load_dwordx4 v[248:251], v60, s[20:23], s60 offen\n"
"buffer_load_dwordx4 v[252:255], v60, s[20:23], s61 offen\n"
".Lfused_block_kernel_s38:\n"
".Lfused_block_kernel_s37:\n"
"s_lshl_b32 s72, s26, 12\n"
"s_add_u32 s72, s72, 90112\n"
"v_and_b32 v72, 1, v3\n"
"v_lshlrev_b32 v72, 2, v72\n"
"v_and_b32 v73, 7, v2\n"
"v_lshl_add_u32 v74, v2, 7, s72\n"
"s_lshl_b32 s73, s29, 1\n"
"s_waitcnt vmcnt(14)\n"
"v_or_b32 v75, 0, v72\n"
"v_xor_b32 v75, v75, v73\n"
"v_lshl_add_u32 v76, v75, 4, v74\n"
"ds_read_b128 v[84:87], v76\n"
"v_add_u32 v80, s73, v76\n"
"ds_read_u16 v100, v80 offset:2048\n"
"v_or_b32 v75, 1, v72\n"
"v_xor_b32 v75, v75, v73\n"
"v_lshl_add_u32 v77, v75, 4, v74\n"
"ds_read_b128 v[88:91], v77\n"
"v_add_u32 v81, s73, v77\n"
"ds_read_u16 v101, v81 offset:2048\n"
"v_or_b32 v75, 2, v72\n"
"v_xor_b32 v75, v75, v73\n"
"v_lshl_add_u32 v78, v75, 4, v74\n"
"ds_read_b128 v[92:95], v78\n"
"v_add_u32 v82, s73, v78\n"
"ds_read_u16 v102, v82 offset:2048\n"
"v_or_b32 v75, 3, v72\n"
"v_xor_b32 v75, v75, v73\n"
"v_lshl_add_u32 v79, v75, 4, v74\n"
"ds_read_b128 v[96:99], v79\n"
"v_add_u32 v83, s73, v79\n"
"ds_read_u16 v103, v83 offset:2048\n"
"v_mov_b32 v72, 0x01000c0c\n"
"v_mov_b32 v73, 0x03020c0c\n"
"v_cmp_gt_u32 vcc, 32, v0\n"
"s_nop 1\n"
"v_cndmask_b32 v72, v73, v72, vcc\n"
"s_waitcnt lgkmcnt(0)\n"
"v_perm_b32 v112, v84, v84, v72\n"
"v_perm_b32 v113, v88, v88, v72\n"
"v_perm_b32 v114, v92, v92, v72\n"
"v_perm_b32 v115, v96, v96, v72\n"
"v_perm_b32 v116, v85, v85, v72\n"
"v_perm_b32 v117, v89, v89, v72\n"
"v_perm_b32 v118, v93, v93, v72\n"
"v_perm_b32 v119, v97, v97, v72\n"
"v_perm_b32 v120, v86, v86, v72\n"
"v_perm_b32 v121, v90, v90, v72\n"
"v_perm_b32 v122, v94, v94, v72\n"
"v_perm_b32 v123, v98, v98, v72\n"
"v_perm_b32 v124, v87, v87, v72\n"
"v_perm_b32 v125, v91, v91, v72\n"
"v_perm_b32 v126, v95, v95, v72\n"
"v_perm_b32 v127, v99, v99, v72\n"
"v_lshlrev_b32 v100, 16, v100\n"
"v_cndmask_b32 v128, 0, v100, vcc\n"
"v_lshlrev_b32 v101, 16, v101\n"
"v_cndmask_b32 v129, 0, v101, vcc\n"
"v_lshlrev_b32 v102, 16, v102\n"
"v_cndmask_b32 v130, 0, v102, vcc\n"
"v_lshlrev_b32 v103, 16, v103\n"
"v_cndmask_b32 v131, 0, v103, vcc\n"
"s_cmp_eq_u32 s26, 0\n"
"s_cbranch_scc0 .Lfused_block_kernel_s41\n"
"s_mov_b32 s87, 0x4000\n"
".Lfused_block_kernel_poll42:\n"
"buffer_load_dwordx2 v[72:73], v49, s[8:11], s31 offen offset:256 sc1\n"
"s_waitcnt vmcnt(0)\n"
"v_cmp_ne_u64 vcc, s[24:25], v[72:73]\n"
"s_nop 0\n"
"s_cmp_eq_u64 vcc, 0\n"
"s_cbranch_scc1 .Lfused_block_kernel_pok43\n"
"s_sub_u32 s87, s87, 1\n"
"s_cmp_lg_u32 s87, 0\n"
"s_cbranch_scc1 .Lfused_block_kernel_poll42\n"
".Lfused_block_kernel_pok43:\n"
".Lfused_block_kernel_s41:\n"
"s_waitcnt vmcnt(0)\n"
"s_barrier\n"
"s_add_u32 s72, s71, 5242880\n"
"v_mov_b32 v56, v57\n"
"buffer_load_dwordx4 a[0:3], v1, s[8:11], s72 offen offset:0 sc1\n"
"ds_read_b128 a[72:75], v53 offset:0\n"
"ds_read_b128 a[76:79], v53 offset:1024\n"
"ds_read_b128 a[80:83], v53 offset:2048\n"
"ds_read_b128 a[84:87], v53 offset:3072\n"
"ds_read_b128 a[88:91], v56\n"
"v_add_u32 v56, v56, v59\n"
"buffer_load_dwordx4 a[4:7], v1, s[8:11], s72 offen offset:1024 sc1\n"
"ds_read_b128 a[92:95], v53 offset:4608\n"
"ds_read_b128 a[96:99], v53 offset:5632\n"
"ds_read_b128 a[100:103], v53 offset:6656\n"
"ds_read_b128 a[104:107], v53 offset:7680\n"
"buffer_load_dwordx4 a[8:11], v1, s[8:11], s72 offen offset:2048 sc1\n"
"ds_read_b128 a[108:111], v56\n"
"v_add_u32 v56, v56, v59\n"
"s_waitcnt lgkmcnt(9)\n"
"ds_read_b128 a[112:115], v53 offset:9216\n"
"ds_read_b128 a[116:119], v53 offset:10240\n"
"ds_read_b128 a[120:123], v53 offset:11264\n"
"ds_read_b128 a[124:127], v53 offset:12288\n"
"buffer_load_dwordx4 a[12:15], v1, s[8:11], s72 offen offset:3072 sc1\n"
"ds_read_b128 a[128:131], v56\n"
"v_add_u32 v56, v56, v59\n"
"s_waitcnt lgkmcnt(9)\n"
"ds_read_b128 a[132:135], v53 offset:13824\n"
"ds_read_b128 a[136:139], v53 offset:14848\n"
"ds_read_b128 a[140:143], v53 offset:15872\n"
"s_add_u32 s72, s72, 4096\n"
"buffer_load_dwordx4 a[16:19], v1, s[8:11], s72 offen offset:0 sc1\n"
"ds_read_b128 a[144:147], v53 offset:16896\n"
"ds_read_b128 a[148:151], v56\n"
"v_add_u32 v56, v56, v59\n"
"s_waitcnt lgkmcnt(9)\n"
"ds_read_b128 a[152:155], v53 offset:18432\n"
"ds_read_b128 a[156:159], v53 offset:19456\n"
"ds_read_b128 a[160:163], v53 offset:20480\n"
"buffer_load_dwordx4 a[20:23], v1, s[8:11], s72 offen offset:1024 sc1\n"
"ds_read_b128 a[164:167], v53 offset:21504\n"
"ds_read_b128 a[168:171], v56\n"
"v_add_u32 v56, v56, v59\n"
"s_waitcnt lgkmcnt(9)\n"
"ds_read_b128 a[172:175], v53 offset:23040\n"
"ds_read_b128 a[176:179], v53 offset:24064\n"
"buffer_load_dwordx4 a[24:27], v1, s[8:11], s72 offen offset:2048 sc1\n"
"ds_read_b128 a[180:183], v53 offset:25088\n"
"ds_read_b128 a[184:187], v53 offset:26112\n"
"ds_read_b128 a[188:191], v56\n"
"v_add_u32 v56, v56, v59\n"
"s_waitcnt lgkmcnt(9)\n"
"ds_read_b128 a[192:195], v53 offset:27648\n"
"ds_read_b128 a[196:199], v53 offset:28672\n"
"buffer_load_dwordx4 a[28:31], v1, s[8:11], s72 offen offset:3072 sc1\n"
"ds_read_b128 a[200:203], v53 offset:29696\n"
"ds_read_b128 a[204:207], v53 offset:30720\n"
"ds_read_b128 a[208:211], v56\n"
"v_add_u32 v56, v56, v59\n"
"s_waitcnt lgkmcnt(9)\n"
"ds_read_b128 a[212:215], v53 offset:32256\n"
"s_add_u32 s72, s72, 4096\n"
"buffer_load_dwordx4 a[32:35], v1, s[8:11], s72 offen offset:0 sc1\n"
"ds_read_b128 a[216:219], v53 offset:33280\n"
"ds_read_b128 a[220:223], v53 offset:34304\n"
"ds_read_b128 a[224:227], v53 offset:35328\n"
"ds_read_b128 a[228:231], v56\n"
"v_add_u32 v56, v56, v59\n"
"s_waitcnt lgkmcnt(9)\n"
"ds_read_b128 a[232:235], v53 offset:36864\n"
"buffer_load_dwordx4 a[36:39], v1, s[8:11], s72 offen offset:1024 sc1\n"
"ds_read_b128 a[236:239], v53 offset:37888\n"
"ds_read_b128 a[240:243], v53 offset:38912\n"
"ds_read_b128 a[244:247], v53 offset:39936\n"
"ds_read_b128 a[248:251], v56\n"
"v_add_u32 v56, v56, v59\n"
"s_waitcnt vmcnt(9) lgkmcnt(15)\n"
"v_mfma_f32_16x16x32_bf16 v[112:115], a[72:75], a[0:3], v[112:115]\n"
"ds_read_b128 a[252:255], v53 offset:41472\n"
"v_mfma_f32_16x16x32_bf16 v[116:119], a[76:79], a[0:3], v[116:119]\n"
"buffer_load_dwordx4 a[40:43], v1, s[8:11], s72 offen offset:2048 sc1\n"
"ds_read_b128 a[72:75], v53 offset:42496\n"
"v_mfma_f32_16x16x32_bf16 v[120:123], a[80:83], a[0:3], v[120:123]\n"
"ds_read_b128 a[76:79], v53 offset:43520\n"
"v_mfma_f32_16x16x32_bf16 v[124:127], a[84:87], a[0:3], v[124:127]\n"
"ds_read_b128 a[80:83], v53 offset:44544\n"
"v_mfma_f32_16x16x32_bf16 v[128:131], a[88:91], a[0:3], v[128:131]\n"
"s_waitcnt vmcnt(9) lgkmcnt(9)\n"
"ds_read_b128 a[84:87], v56\n"
"v_add_u32 v56, v56, v59\n"
"v_mfma_f32_16x16x32_bf16 v[112:115], a[92:95], a[4:7], v[112:115]\n"
"ds_read_b128 a[88:91], v53 offset:46080\n"
"v_mfma_f32_16x16x32_bf16 v[116:119], a[96:99], a[4:7], v[116:119]\n"
"buffer_load_dwordx4 a[44:47], v1, s[8:11], s72 offen offset:3072 sc1\n"
"ds_read_b128 a[92:95], v53 offset:47104\n"
"v_mfma_f32_16x16x32_bf16 v[120:123], a[100:103], a[4:7], v[120:123]\n"
"ds_read_b128 a[96:99], v53 offset:48128\n"
"v_mfma_f32_16x16x32_bf16 v[124:127], a[104:107], a[4:7], v[124:127]\n"
"ds_read_b128 a[100:103], v53 offset:49152\n"
"v_mfma_f32_16x16x32_bf16 v[128:131], a[108:111], a[4:7], v[128:131]\n"
"s_waitcnt vmcnt(9) lgkmcnt(9)\n"
"ds_read_b128 a[104:107], v56\n"
"v_add_u32 v56, v56, v59\n"
"v_mfma_f32_16x16x32_bf16 v[112:115], a[112:115], a[8:11], v[112:115]\n"
"ds_read_b128 a[108:111], v53 offset:50688\n"
"v_mfma_f32_16x16x32_bf16 v[116:119], a[116:119], a[8:11], v[116:119]\n"
"s_add_u32 s72, s72, 4096\n"
"buffer_load_dwordx4 a[48:51], v1, s[8:11], s72 offen offset:0 sc1\n"
"ds_read_b128 a[112:115], v53 offset:51712\n"
"v_mfma_f32_16x16x32_bf16 v[120:123], a[120:123], a[8:11], v[120:123]\n"
"ds_read_b128 a[116:119], v53 offset:52736\n"
"v_mfma_f32_16x16x32_bf16 v[124:127], a[124:127], a[8:11], v[124:127]\n"
"ds_read_b128 a[120:123], v53 offset:53760\n"
"v_mfma_f32_16x16x32_bf16 v[128:131], a[128:131], a[8:11], v[128:131]\n"
"s_waitcnt vmcnt(9) lgkmcnt(9)\n"
"ds_read_b128 a[124:127], v56\n"
"v_add_u32 v56, v56, v59\n"
"v_mfma_f32_16x16x32_bf16 v[112:115], a[132:135], a[12:15], v[112:115]\n"
"ds_read_b128 a[128:131], v53 offset:55296\n"
"v_mfma_f32_16x16x32_bf16 v[116:119], a[136:139], a[12:15], v[116:119]\n"
"buffer_load_dwordx4 a[52:55], v1, s[8:11], s72 offen offset:1024 sc1\n"
"ds_read_b128 a[132:135], v53 offset:56320\n"
"v_mfma_f32_16x16x32_bf16 v[120:123], a[140:143], a[12:15], v[120:123]\n"
"ds_read_b128 a[136:139], v53 offset:57344\n"
"v_mfma_f32_16x16x32_bf16 v[124:127], a[144:147], a[12:15], v[124:127]\n"
"ds_read_b128 a[140:143], v53 offset:58368\n"
"v_mfma_f32_16x16x32_bf16 v[128:131], a[148:151], a[12:15], v[128:131]\n"
"s_waitcnt vmcnt(9) lgkmcnt(9)\n"
"ds_read_b128 a[144:147], v56\n"
"v_add_u32 v56, v56, v59\n"
"v_mfma_f32_16x16x32_bf16 v[112:115], a[152:155], a[16:19], v[112:115]\n"
"ds_read_b128 a[148:151], v53 offset:59904\n"
"v_mfma_f32_16x16x32_bf16 v[116:119], a[156:159], a[16:19], v[116:119]\n"
"buffer_load_dwordx4 a[56:59], v1, s[8:11], s72 offen offset:2048 sc1\n"
"ds_read_b128 a[152:155], v53 offset:60928\n"
"v_mfma_f32_16x16x32_bf16 v[120:123], a[160:163], a[16:19], v[120:123]\n"
"ds_read_b128 a[156:159], v53 offset:61952\n"
"v_mfma_f32_16x16x32_bf16 v[124:127], a[164:167], a[16:19], v[124:127]\n"
"ds_read_b128 a[160:163], v53 offset:62976\n"
"v_mfma_f32_16x16x32_bf16 v[128:131], a[168:171], a[16:19], v[128:131]\n"
"s_waitcnt vmcnt(9) lgkmcnt(9)\n"
"ds_read_b128 a[164:167], v56\n"
"v_add_u32 v56, v56, v59\n"
"v_mfma_f32_16x16x32_bf16 v[112:115], a[172:175], a[20:23], v[112:115]\n"
"ds_read_b128 a[168:171], v53 offset:64512\n"
"v_mfma_f32_16x16x32_bf16 v[116:119], a[176:179], a[20:23], v[116:119]\n"
"buffer_load_dwordx4 a[60:63], v1, s[8:11], s72 offen offset:3072 sc1\n"
"ds_read_b128 a[172:175], v54 offset:0\n"
"v_mfma_f32_16x16x32_bf16 v[120:123], a[180:183], a[20:23], v[120:123]\n"
"ds_read_b128 a[176:179], v54 offset:1024\n"
"v_mfma_f32_16x16x32_bf16 v[124:127], a[184:187], a[20:23], v[124:127]\n"
"ds_read_b128 a[180:183], v54 offset:2048\n"
"v_mfma_f32_16x16x32_bf16 v[128:131], a[188:191], a[20:23], v[128:131]\n"
"s_waitcnt vmcnt(9) lgkmcnt(9)\n"
"ds_read_b128 a[184:187], v56\n"
"v_add_u32 v56, v56, v59\n"
"v_mfma_f32_16x16x32_bf16 v[112:115], a[192:195], a[24:27], v[112:115]\n"
"ds_read_b128 a[188:191], v54 offset:3584\n"
"v_mfma_f32_16x16x32_bf16 v[116:119], a[196:199], a[24:27], v[116:119]\n"
"s_add_u32 s72, s72, 4096\n"
"buffer_load_dwordx4 a[64:67], v1, s[8:11], s72 offen offset:0 sc1\n"
"ds_read_b128 a[192:195], v54 offset:4608\n"
"v_mfma_f32_16x16x32_bf16 v[120:123], a[200:203], a[24:27], v[120:123]\n"
"ds_read_b128 a[196:199], v54 offset:5632\n"
"v_mfma_f32_16x16x32_bf16 v[124:127], a[204:207], a[24:27], v[124:127]\n"
"ds_read_b128 a[200:203], v54 offset:6656\n"
"v_mfma_f32_16x16x32_bf16 v[128:131], a[208:211], a[24:27], v[128:131]\n"
"s_waitcnt vmcnt(9) lgkmcnt(9)\n"
"ds_read_b128 a[204:207], v56\n"
"v_add_u32 v56, v56, v59\n"
"v_mfma_f32_16x16x32_bf16 v[112:115], a[212:215], a[28:31], v[112:115]\n"
"ds_read_b128 a[208:211], v54 offset:8192\n"
"v_mfma_f32_16x16x32_bf16 v[116:119], a[216:219], a[28:31], v[116:119]\n"
"buffer_load_dwordx4 a[68:71], v1, s[8:11], s72 offen offset:1024 sc1\n"
"ds_read_b128 a[212:215], v54 offset:9216\n"
"v_mfma_f32_16x16x32_bf16 v[120:123], a[220:223], a[28:31], v[120:123]\n"
"ds_read_b128 a[216:219], v54 offset:10240\n"
"v_mfma_f32_16x16x32_bf16 v[124:127], a[224:227], a[28:31], v[124:127]\n"
"ds_read_b128 a[220:223], v54 offset:11264\n"
"v_mfma_f32_16x16x32_bf16 v[128:131], a[228:231], a[28:31], v[128:131]\n"
"s_waitcnt vmcnt(9) lgkmcnt(9)\n"
"ds_read_b128 a[224:227], v56\n"
"v_add_u32 v56, v56, v59\n"
"v_mfma_f32_16x16x32_bf16 v[112:115], a[232:235], a[32:35], v[112:115]\n"
"ds_read_b128 a[228:231], v54 offset:12800\n"
"v_mfma_f32_16x16x32_bf16 v[116:119], a[236:239], a[32:35], v[116:119]\n"
"s_sub_u32 s91, s26, 0\n"
"s_mul_i32 s91, s91, 10240\n"
"s_mul_i32 s90, s29, 82944\n"
"s_add_u32 s90, s90, s91\n"
"s_add_u32 s90, s90, 2860032\n"
"s_add_u32 s91, s91, 122880\n"
"s_add_u32 m0, s91, 0\n"
"s_nop 0\n"
"buffer_load_dwordx4 v1, s[8:11], s90 offen offset:0 sc1 lds\n"
"ds_read_b128 a[232:235], v54 offset:13824\n"
"v_mfma_f32_16x16x32_bf16 v[120:123], a[240:243], a[32:35], v[120:123]\n"
"ds_read_b128 a[236:239], v54 offset:14848\n"
"v_mfma_f32_16x16x32_bf16 v[124:127], a[244:247], a[32:35], v[124:127]\n"
"ds_read_b128 a[240:243], v54 offset:15872\n"
"v_mfma_f32_16x16x32_bf16 v[128:131], a[248:251], a[32:35], v[128:131]\n"
"s_waitcnt vmcnt(9) lgkmcnt(9)\n"
"ds_read_b128 a[244:247], v56\n"
"v_add_u32 v56, v56, v59\n"
"v_mfma_f32_16x16x32_bf16 v[112:115], a[252:255], a[36:39], v[112:115]\n"
"v_mfma_f32_16x16x32_bf16 v[116:119], a[72:75], a[36:39], v[116:119]\n"
"buffer_load_dwordx4 v1, s[8:11], s90 offen offset:1024 sc1 lds\n"
"v_mfma_f32_16x16x32_bf16 v[120:123], a[76:79], a[36:39], v[120:123]\n"
"v_mfma_f32_16x16x32_bf16 v[124:127], a[80:83], a[36:39], v[124:127]\n"
"v_mfma_f32_16x16x32_bf16 v[128:131], a[84:87], a[36:39], v[128:131]\n"
"s_waitcnt vmcnt(9) lgkmcnt(15)\n"
"v_mfma_f32_16x16x32_bf16 v[112:115], a[88:91], a[40:43], v[112:115]\n"
"v_mfma_f32_16x16x32_bf16 v[116:119], a[92:95], a[40:43], v[116:119]\n"
"buffer_load_dwordx4 v1, s[8:11], s90 offen offset:2048 sc1 lds\n"
"v_mfma_f32_16x16x32_bf16 v[120:123], a[96:99], a[40:43], v[120:123]\n"
"v_mfma_f32_16x16x32_bf16 v[124:127], a[100:103], a[40:43], v[124:127]\n"
"v_mfma_f32_16x16x32_bf16 v[128:131], a[104:107], a[40:43], v[128:131]\n"
"s_waitcnt vmcnt(9) lgkmcnt(15)\n"
"v_mfma_f32_16x16x32_bf16 v[112:115], a[108:111], a[44:47], v[112:115]\n"
"v_mfma_f32_16x16x32_bf16 v[116:119], a[112:115], a[44:47], v[116:119]\n"
"buffer_load_dwordx4 v1, s[8:11], s90 offen offset:3072 sc1 lds\n"
"v_mfma_f32_16x16x32_bf16 v[120:123], a[116:119], a[44:47], v[120:123]\n"
"v_mfma_f32_16x16x32_bf16 v[124:127], a[120:123], a[44:47], v[124:127]\n"
"v_mfma_f32_16x16x32_bf16 v[128:131], a[124:127], a[44:47], v[128:131]\n"
"s_waitcnt vmcnt(9) lgkmcnt(15)\n"
"v_mfma_f32_16x16x32_bf16 v[112:115], a[128:131], a[48:51], v[112:115]\n"
"v_mfma_f32_16x16x32_bf16 v[116:119], a[132:135], a[48:51], v[116:119]\n"
"s_add_u32 s90, s90, 4096\n"
"s_add_u32 m0, s91, 4096\n"
"s_nop 0\n"
"buffer_load_dwordx4 v1, s[8:11], s90 offen offset:0 sc1 lds\n"
"v_mfma_f32_16x16x32_bf16 v[120:123], a[136:139], a[48:51], v[120:123]\n"
"v_mfma_f32_16x16x32_bf16 v[124:127], a[140:143], a[48:51], v[124:127]\n"
"v_mfma_f32_16x16x32_bf16 v[128:131], a[144:147], a[48:51], v[128:131]\n"
"s_waitcnt vmcnt(9) lgkmcnt(15)\n"
"v_mfma_f32_16x16x32_bf16 v[112:115], a[148:151], a[52:55], v[112:115]\n"
"v_mfma_f32_16x16x32_bf16 v[116:119], a[152:155], a[52:55], v[116:119]\n"
"buffer_load_dwordx4 v1, s[8:11], s90 offen offset:1024 sc1 lds\n"
"v_mfma_f32_16x16x32_bf16 v[120:123], a[156:159], a[52:55], v[120:123]\n"
"v_mfma_f32_16x16x32_bf16 v[124:127], a[160:163], a[52:55], v[124:127]\n"
"v_mfma_f32_16x16x32_bf16 v[128:131], a[164:167], a[52:55], v[128:131]\n"
"s_waitcnt vmcnt(9) lgkmcnt(15)\n"
"v_mfma_f32_16x16x32_bf16 v[112:115], a[168:171], a[56:59], v[112:115]\n"
"v_mfma_f32_16x16x32_bf16 v[116:119], a[172:175], a[56:59], v[116:119]\n"
"buffer_load_dwordx4 v1, s[8:11], s90 offen offset:2048 sc1 lds\n"
"v_mfma_f32_16x16x32_bf16 v[120:123], a[176:179], a[56:59], v[120:123]\n"
"v_mfma_f32_16x16x32_bf16 v[124:127], a[180:183], a[56:59], v[124:127]\n"
"v_mfma_f32_16x16x32_bf16 v[128:131], a[184:187], a[56:59], v[128:131]\n"
"s_waitcnt vmcnt(9) lgkmcnt(10)\n"
"v_mfma_f32_16x16x32_bf16 v[112:115], a[188:191], a[60:63], v[112:115]\n"
"v_mfma_f32_16x16x32_bf16 v[116:119], a[192:195], a[60:63], v[116:119]\n"
"buffer_load_dwordx4 v1, s[8:11], s90 offen offset:3072 sc1 lds\n"
"v_mfma_f32_16x16x32_bf16 v[120:123], a[196:199], a[60:63], v[120:123]\n"
"v_mfma_f32_16x16x32_bf16 v[124:127], a[200:203], a[60:63], v[124:127]\n"
"v_mfma_f32_16x16x32_bf16 v[128:131], a[204:207], a[60:63], v[128:131]\n"
"s_waitcnt vmcnt(9) lgkmcnt(5)\n"
"v_mfma_f32_16x16x32_bf16 v[112:115], a[208:211], a[64:67], v[112:115]\n"
"v_mfma_f32_16x16x32_bf16 v[116:119], a[212:215], a[64:67], v[116:119]\n"
"s_add_u32 s90, s90, 4096\n"
"s_add_u32 m0, s91, 8192\n"
"s_nop 0\n"
"buffer_load_dwordx4 v1, s[8:11], s90 offen offset:0 sc1 lds\n"
"v_mfma_f32_16x16x32_bf16 v[120:123], a[216:219], a[64:67], v[120:123]\n"
"v_mfma_f32_16x16x32_bf16 v[124:127], a[220:223], a[64:67], v[124:127]\n"
"v_mfma_f32_16x16x32_bf16 v[128:131], a[224:227], a[64:67], v[128:131]\n"
"s_waitcnt vmcnt(9) lgkmcnt(0)\n"
"v_mfma_f32_16x16x32_bf16 v[112:115], a[228:231], a[68:71], v[112:115]\n"
"v_mfma_f32_16x16x32_bf16 v[116:119], a[232:235], a[68:71], v[116:119]\n"
"buffer_load_dwordx4 v1, s[8:11], s90 offen offset:1024 sc1 lds\n"
"v_mfma_f32_16x16x32_bf16 v[120:123], a[236:239], a[68:71], v[120:123]\n"
"v_mfma_f32_16x16x32_bf16 v[124:127], a[240:243], a[68:71], v[124:127]\n"
"v_mfma_f32_16x16x32_bf16 v[128:131], a[244:247], a[68:71], v[128:131]\n"
"s_barrier\n"
"s_lshl_b32 s92, s29, 11\n"
"s_add_u32 s92, s92, 7864320\n"
"s_lshl_b32 s93, s29, 8\n"
"s_add_u32 s93, s93, 7880704\n"
"v_cmp_gt_u32 vcc, 32, v0\n"
"s_nop 1\n"
"v_add_f32 v184, v128, v129\n"
"v_mov_b32 v185, v184\n"
"s_nop 1\n"
"v_permlane32_swap_b32 v185, v184\n"
"v_add_f32 v62, v62, v184\n"
"v_pk_add_f32 v[152:153], v[112:113], v[62:63] op_sel_hi:[1,0] neg_lo:[0,1] neg_hi:[0,1]\n"
"v_pk_add_f32 v[154:155], v[114:115], v[62:63] op_sel_hi:[1,0] neg_lo:[0,1] neg_hi:[0,1]\n"
"v_cvt_pk_bf16_f32 v172, v152, v153\n"
"v_cvt_pk_bf16_f32 v173, v154, v155\n"
"v_pk_add_f32 v[156:157], v[116:117], v[62:63] op_sel_hi:[1,0] neg_lo:[0,1] neg_hi:[0,1]\n"
"v_pk_add_f32 v[158:159], v[118:119], v[62:63] op_sel_hi:[1,0] neg_lo:[0,1] neg_hi:[0,1]\n"
"v_cvt_pk_bf16_f32 v174, v156, v157\n"
"v_cvt_pk_bf16_f32 v175, v158, v159\n"
"buffer_store_dwordx4 v[172:175], v42, s[8:11], s92 offen sc1\n"
"v_pk_add_f32 v[160:161], v[120:121], v[62:63] op_sel_hi:[1,0] neg_lo:[0,1] neg_hi:[0,1]\n"
"v_pk_add_f32 v[162:163], v[122:123], v[62:63] op_sel_hi:[1,0] neg_lo:[0,1] neg_hi:[0,1]\n"
"v_cvt_pk_bf16_f32 v176, v160, v161\n"
"v_cvt_pk_bf16_f32 v177, v162, v163\n"
"v_pk_add_f32 v[164:165], v[124:125], v[62:63] op_sel_hi:[1,0] neg_lo:[0,1] neg_hi:[0,1]\n"
"v_pk_add_f32 v[166:167], v[126:127], v[62:63] op_sel_hi:[1,0] neg_lo:[0,1] neg_hi:[0,1]\n"
"v_cvt_pk_bf16_f32 v178, v164, v165\n"
"v_cvt_pk_bf16_f32 v179, v166, v167\n"
"buffer_store_dwordx4 v[176:179], v42, s[8:11], s92 offen offset:1024 sc1\n"
"v_pk_add_f32 v[168:169], v[128:129], v[62:63] op_sel_hi:[1,0] neg_lo:[0,1] neg_hi:[0,1]\n"
"v_pk_add_f32 v[170:171], v[130:131], v[62:63] op_sel_hi:[1,0] neg_lo:[0,1] neg_hi:[0,1]\n"
"v_cvt_pk_bf16_f32 v180, v168, v169\n"
"v_cvt_pk_bf16_f32 v181, v170, v171\n"
"v_cndmask_b32 v168, 0, v168, vcc\n"
"v_cndmask_b32 v169, 0, v169, vcc\n"
"v_permlane16_swap_b32 v180, v182\n"
"v_permlane16_swap_b32 v181, v183\n"
"buffer_store_dwordx4 v[180:183], v43, s[8:11], s93 offen sc1\n"
"v_cndmask_b32 v170, 0, v170, vcc\n"
"v_cndmask_b32 v171, 0, v171, vcc\n"
"v_pk_mul_f32 v[184:185], v[152:153], v[152:153]\n"
"v_pk_fma_f32 v[184:185], v[154:155], v[154:155], v[184:185]\n"
"v_pk_fma_f32 v[184:185], v[156:157], v[156:157], v[184:185]\n"
"v_pk_fma_f32 v[184:185], v[158:159], v[158:159], v[184:185]\n"
"v_pk_fma_f32 v[184:185], v[160:161], v[160:161], v[184:185]\n"
"v_pk_fma_f32 v[184:185], v[162:163], v[162:163], v[184:185]\n"
"v_pk_fma_f32 v[184:185], v[164:165], v[164:165], v[184:185]\n"
"v_pk_fma_f32 v[184:185], v[166:167], v[166:167], v[184:185]\n"
"v_pk_fma_f32 v[184:185], v[168:169], v[168:169], v[184:185]\n"
"v_pk_fma_f32 v[184:185], v[170:171], v[170:171], v[184:185]\n"
"v_add_f32 v63, v184, v185\n"
"buffer_store_dword v63, v44, s[8:11], s93 offen offset:2048 sc1\n"
"s_cmp_lg_u32 s26, 0\n"
"s_cbranch_scc0 .Lfused_block_kernel_s44\n"
"s_sub_u32 s91, s26, 1\n"
"s_mul_i32 s91, s91, 14336\n"
"s_mul_i32 s90, s29, 82944\n"
"s_add_u32 s90, s90, s91\n"
"s_add_u32 s90, s90, 2818048\n"
"s_add_u32 s91, s91, 80896\n"
"s_add_u32 m0, s91, 0\n"
"s_nop 0\n"
"buffer_load_dwordx4 v1, s[8:11], s90 offen offset:0 sc1 lds\n"
"buffer_load_dwordx4 v1, s[8:11], s90 offen offset:1024 sc1 lds\n"
"buffer_load_dwordx4 v1, s[8:11], s90 offen offset:2048 sc1 lds\n"
"buffer_load_dwordx4 v1, s[8:11], s90 offen offset:3072 sc1 lds\n"
"s_add_u32 s90, s90, 4096\n"
"s_add_u32 m0, s91, 4096\n"
"s_nop 0\n"
"buffer_load_dwordx4 v1, s[8:11], s90 offen offset:0 sc1 lds\n"
"buffer_load_dwordx4 v1, s[8:11], s90 offen offset:1024 sc1 lds\n"
".Lfused_block_kernel_s44:\n"
"s_cmp_lg_u32 s26, 0\n"
"s_cbranch_scc0 .Lfused_block_kernel_s45\n"
"s_waitcnt vmcnt(6)\n"
".Lfused_block_kernel_s45:\n"
"s_cmp_eq_u32 s26, 0\n"
"s_cbranch_scc0 .Lfused_block_kernel_s46\n"
"s_waitcnt vmcnt(0)\n"
".Lfused_block_kernel_s46:\n"
"s_barrier\n"
"s_cmp_eq_u32 s26, 0\n"
"s_cbranch_scc0 .Lfused_block_kernel_s47\n"
"buffer_store_dwordx2 v[22:23], v47, s[8:11], s30 offen offset:384 sc1\n"
".Lfused_block_kernel_s47:\n"
"s_cmp_lg_u32 s26, 0\n"
"s_cbranch_scc0 .Lfused_block_kernel_s48\n"
"buffer_load_dwordx4 v1, s[8:11], s90 offen offset:2048 sc1 lds\n"
"buffer_load_dwordx4 v1, s[8:11], s90 offen offset:3072 sc1 lds\n"
"s_add_u32 s90, s90, 4096\n"
"s_add_u32 m0, s91, 8192\n"
"s_nop 0\n"
"buffer_load_dwordx4 v1, s[8:11], s90 offen offset:0 sc1 lds\n"
"buffer_load_dwordx4 v1, s[8:11], s90 offen offset:1024 sc1 lds\n"
"buffer_load_dwordx4 v1, s[8:11], s90 offen offset:2048 sc1 lds\n"
"buffer_load_dwordx4 v1, s[8:11], s90 offen offset:3072 sc1 lds\n"
"s_add_u32 s90, s90, 4096\n"
"s_add_u32 m0, s91, 12288\n"
"s_nop 0\n"
"buffer_load_dwordx4 v1, s[8:11], s90 offen offset:0 sc1 lds\n"
"buffer_load_dwordx4 v1, s[8:11], s90 offen offset:1024 sc1 lds\n"
"s_cmp_eq_u32 s70, 2\n"
"s_cbranch_scc0 .Lfused_block_kernel_s49\n"
"s_waitcnt vmcnt(46)\n"
"v_perm_b32 v184, v228, v224, s4\n"
"v_perm_b32 v185, v236, v232, s4\n"
"v_perm_b32 v186, v244, v240, s4\n"
"v_perm_b32 v187, v252, v248, s4\n"
"buffer_store_dwordx4 v[184:187], v61, s[8:11], s62 offen sc1\n"
"v_perm_b32 v188, v228, v224, s5\n"
"v_perm_b32 v189, v236, v232, s5\n"
"v_perm_b32 v190, v244, v240, s5\n"
"v_perm_b32 v191, v252, v248, s5\n"
"buffer_store_dwordx4 v[188:191], v61, s[8:11], s63 offen sc1\n"
"v_perm_b32 v184, v229, v225, s4\n"
"v_perm_b32 v185, v237, v233, s4\n"
"v_perm_b32 v186, v245, v241, s4\n"
"v_perm_b32 v187, v253, v249, s4\n"
"buffer_store_dwordx4 v[184:187], v61, s[8:11], s64 offen sc1\n"
"v_perm_b32 v188, v229, v225, s5\n"
"v_perm_b32 v189, v237, v233, s5\n"
"v_perm_b32 v190, v245, v241, s5\n"
"v_perm_b32 v191, v253, v249, s5\n"
"buffer_store_dwordx4 v[188:191], v61, s[8:11], s65 offen sc1\n"
"v_perm_b32 v184, v230, v226, s4\n"
"v_perm_b32 v185, v238, v234, s4\n"
"v_perm_b32 v186, v246, v242, s4\n"
"v_perm_b32 v187, v254, v250, s4\n"
"buffer_store_dwordx4 v[184:187], v61, s[8:11], s66 offen sc1\n"
"v_perm_b32 v188, v230, v226, s5\n"
"v_perm_b32 v189, v238, v234, s5\n"
"v_perm_b32 v190, v246, v242, s5\n"
"v_perm_b32 v191, v254, v250, s5\n"
"buffer_store_dwordx4 v[188:191], v61, s[8:11], s67 offen sc1\n"
"v_perm_b32 v184, v231, v227, s4\n"
"v_perm_b32 v185, v239, v235, s4\n"
"v_perm_b32 v186, v247, v243, s4\n"
"v_perm_b32 v187, v255, v251, s4\n"
"buffer_store_dwordx4 v[184:187], v61, s[8:11], s68 offen sc1\n"
"v_perm_b32 v188, v231, v227, s5\n"
"v_perm_b32 v189, v239, v235, s5\n"
"v_perm_b32 v190, v247, v243, s5\n"
"v_perm_b32 v191, v255, v251, s5\n"
"buffer_store_dwordx4 v[188:191], v61, s[8:11], s69 offen sc1\n"
".Lfused_block_kernel_s49:\n"
"s_sub_u32 s92, 3, s26\n"
"s_lshl_b32 s92, s92, 8\n"
"s_add_u32 s92, s92, s33\n"
"s_mov_b32 s70, 0\n"
"s_cmp_lt_u32 s92, 648\n"
"s_cbranch_scc0 .Lfused_block_kernel_s50\n"
"s_mov_b32 s20, s48\n"
"s_and_b32 s21, s49, 0xffff\n"
"s_mov_b32 s22, 5308416\n"
"s_mov_b32 s23, 0x20000\n"
"s_mov_b32 s70, 2\n"
"s_mul_i32 s89, s92, 7282\n"
"s_lshr_b32 s89, s89, 16\n"
"s_mul_i32 s81, s89, 9\n"
"s_sub_u32 s81, s92, s81\n"
"s_mul_i32 s84, s89, 7282\n"
"s_lshr_b32 s84, s84, 16\n"
"s_mul_i32 s83, s84, 9\n"
"s_sub_u32 s83, s89, s83\n"
"s_lshl_b32 s72, s89, 6\n"
"s_lshl_b32 s83, s83, 11\n"
"s_mov_b32 s73, 32\n"
"s_mov_b32 s74, 1\n"
"s_mov_b32 s75, 4\n"
"s_mov_b32 s76, 8\n"
"s_mov_b32 s77, 16\n"
"s_mov_b32 s78, 2\n"
"s_sub_u32 s85, s81, s84\n"
"s_sub_u32 s85, s85, 1\n"
"s_and_b32 s85, s85, 7\n"
"s_add_u32 s85, s85, 10\n"
"s_cmp_eq_u32 s81, 8\n"
"s_cselect_b32 s85, 9, s85\n"
"s_mul_i32 s85, s85, 18432\n"
"s_mov_b32 s80, 1152\n"
"s_mul_i32 s84, s84, 1327104\n"
"s_add_u32 s83, s83, s84\n"
"s_add_u32 s83, s83, s85\n"
"s_add_u32 s83, s83, 18874368\n"
"s_lshl_b32 s81, s81, 7\n"
"s_mov_b32 s82, 16\n"
"s_mov_b32 s84, 16\n"
"s_mov_b32 s85, 128\n"
"s_mov_b32 s86, 1024\n"
"s_mov_b32 s87, 512\n"
"s_mov_b32 s88, 331776\n"
"s_mov_b32 s89, 663552\n"
"s_mul_i32 s72, s72, s80\n"
"s_mul_i32 s73, s73, s80\n"
"s_mul_i32 s74, s74, s80\n"
"s_mul_i32 s75, s75, s80\n"
"s_mul_i32 s76, s76, s80\n"
"s_mul_i32 s77, s77, s80\n"
"s_mul_i32 s78, s78, s80\n"
"s_add_u32 s54, s72, s81\n"
"s_mov_b32 s62, s83\n"
"s_add_u32 s55, s54, s76\n"
"s_add_u32 s63, s62, s87\n"
"s_add_u32 s56, s54, s77\n"
"s_add_u32 s64, s62, s88\n"
"s_add_u32 s57, s55, s77\n"
"s_add_u32 s65, s63, s88\n"
"s_add_u32 s58, s54, s78\n"
"s_add_u32 s66, s62, s89\n"
"s_add_u32 s59, s55, s78\n"
"s_add_u32 s67, s63, s89\n"
"s_add_u32 s60, s56, s78\n"
"s_add_u32 s68, s64, s89\n"
"s_add_u32 s61, s57, s78\n"
"s_add_u32 s69, s65, s89\n"
"v_and_b32 v68, 7, v0\n"
"v_mul_u32_u24 v60, s82, v68\n"
"v_mul_u32_u24 v61, s84, v68\n"
"v_bfe_u32 v68, v0, 3, 1\n"
"v_mad_u32_u24 v60, v68, s73, v60\n"
"v_bfe_u32 v68, v0, 4, 1\n"
"v_mad_u32_u24 v60, v68, s74, v60\n"
"v_bfe_u32 v68, v0, 3, 2\n"
"v_mad_u32_u24 v61, v68, s85, v61\n"
"v_lshrrev_b32 v68, 5, v0\n"
"v_mad_u32_u24 v60, v68, s75, v60\n"
"v_mad_u32_u24 v61, v68, s86, v61\n"
"buffer_load_dwordx4 v[224:227], v60, s[20:23], s54 offen\n"
"buffer_load_dwordx4 v[228:231], v60, s[20:23], s55 offen\n"
"buffer_load_dwordx4 v[232:235], v60, s[20:23], s56 offen\n"
"buffer_load_dwordx4 v[236:239], v60, s[20:23], s57 offen\n"
"buffer_load_dwordx4 v[240:243], v60, s[20:23], s58 offen\n"
"buffer_load_dwordx4 v[244:247], v60, s[20:23], s59 offen\n"
"buffer_load_dwordx4 v[248:251], v60, s[20:23], s60 offen\n"
"buffer_load_dwordx4 v[252:255], v60, s[20:23], s61 offen\n"
".Lfused_block_kernel_s50:\n"
".Lfused_block_kernel_s48:\n"
"v_pk_mov_b32 v[132:133], v[112:113], v[112:113] op_sel:[0,1]\n"
"v_pk_mov_b32 v[134:135], v[114:115], v[114:115] op_sel:[0,1]\n"
"v_pk_mov_b32 v[136:137], v[116:117], v[116:117] op_sel:[0,1]\n"
"v_pk_mov_b32 v[138:139], v[118:119], v[118:119] op_sel:[0,1]\n"
"v_pk_mov_b32 v[140:141], v[120:121], v[120:121] op_sel:[0,1]\n"
"v_pk_mov_b32 v[142:143], v[122:123], v[122:123] op_sel:[0,1]\n"
"v_pk_mov_b32 v[144:145], v[124:125], v[124:125] op_sel:[0,1]\n"
"v_pk_mov_b32 v[146:147], v[126:127], v[126:127] op_sel:[0,1]\n"
"v_pk_mov_b32 v[148:149], v[128:129], v[128:129] op_sel:[0,1]\n"
"v_pk_mov_b32 v[150:151], v[130:131], v[130:131] op_sel:[0,1]\n"
"s_cmp_eq_u32 s26, 0\n"
"s_cbranch_scc0 .Lfused_block_kernel_s51\n"
"s_mov_b32 s87, 0x4000\n"
".Lfused_block_kernel_poll52:\n"
"buffer_load_dwordx2 v[72:73], v49, s[8:11], s31 offen offset:384 sc1\n"
"s_waitcnt vmcnt(0)\n"
"v_cmp_ne_u64 vcc, s[24:25], v[72:73]\n"
"s_nop 0\n"
"s_cmp_eq_u64 vcc, 0\n"
"s_cbranch_scc1 .Lfused_block_kernel_pok53\n"
"s_sub_u32 s87, s87, 1\n"
"s_cmp_lg_u32 s87, 0\n"
"s_cbranch_scc1 .Lfused_block_kernel_poll52\n"
".Lfused_block_kernel_pok53:\n"
".Lfused_block_kernel_s51:\n"
"s_waitcnt vmcnt(0)\n"
"s_barrier\n"
"s_cmp_eq_u32 s26, 1\n"
"s_cbranch_scc0 .Lfused_block_kernel_s54\n"
"s_lshl_b32 s72, s33, 3\n"
"s_add_u32 s72, s72, 6144\n"
"buffer_store_dwordx2 v[22:23], v52, s[8:11], s72 offen offset:0 sc1\n"
".Lfused_block_kernel_s54:\n"
"s_add_u32 s72, s71, 7864320\n"
"s_add_u32 s73, s72, 16384\n"
"v_mov_b32 v56, v58\n"
"buffer_load_dwordx4 a[0:3], v1, s[8:11], s72 offen offset:0 sc1\n"
"ds_read_b128 a[72:75], v54 offset:15360\n"
"ds_read_b128 a[76:79], v54 offset:16384\n"
"ds_read_b128 a[80:83], v54 offset:17408\n"
"ds_read_b128 a[84:87], v54 offset:18432\n"
"ds_read_b128 a[88:91], v56\n"
"v_add_u32 v56, 4608, v56\n"
"buffer_load_dwordx4 a[4:7], v1, s[8:11], s72 offen offset:1024 sc1\n"
"ds_read_b128 a[92:95], v54 offset:19968\n"
"ds_read_b128 a[96:99], v54 offset:20992\n"
"ds_read_b128 a[100:103], v54 offset:22016\n"
"ds_read_b128 a[104:107], v54 offset:23040\n"
"buffer_load_dwordx4 a[8:11], v1, s[8:11], s72 offen offset:2048 sc1\n"
"ds_read_b128 a[108:111], v56\n"
"v_add_u32 v56, 4608, v56\n"
"s_waitcnt lgkmcnt(9)\n"
"ds_read_b128 a[112:115], v54 offset:24576\n"
"ds_read_b128 a[116:119], v54 offset:25600\n"
"ds_read_b128 a[120:123], v54 offset:26624\n"
"ds_read_b128 a[124:127], v54 offset:27648\n"
"buffer_load_dwordx4 a[12:15], v1, s[8:11], s72 offen offset:3072 sc1\n"
"ds_read_b128 a[128:131], v56\n"
"v_add_u32 v56, 4608, v56\n"
"s_waitcnt lgkmcnt(9)\n"
"ds_read_b128 a[132:135], v54 offset:29184\n"
"ds_read_b128 a[136:139], v54 offset:30208\n"
"ds_read_b128 a[140:143], v54 offset:31232\n"
"s_add_u32 s72, s72, 4096\n"
"buffer_load_dwordx4 a[16:19], v1, s[8:11], s72 offen offset:0 sc1\n"
"ds_read_b128 a[144:147], v54 offset:32256\n"
"ds_read_b128 a[148:151], v56\n"
"v_add_u32 v56, 4608, v56\n"
"s_waitcnt lgkmcnt(9)\n"
"ds_read_b128 a[152:155], v54 offset:33792\n"
"ds_read_b128 a[156:159], v54 offset:34816\n"
"ds_read_b128 a[160:163], v54 offset:35840\n"
"buffer_load_dwordx4 a[20:23], v1, s[8:11], s72 offen offset:1024 sc1\n"
"ds_read_b128 a[164:167], v54 offset:36864\n"
"ds_read_b128 a[168:171], v56\n"
"v_add_u32 v56, 4608, v56\n"
"s_waitcnt lgkmcnt(9)\n"
"ds_read_b128 a[172:175], v54 offset:38400\n"
"ds_read_b128 a[176:179], v54 offset:39424\n"
"buffer_load_dwordx4 a[24:27], v1, s[8:11], s72 offen offset:2048 sc1\n"
"ds_read_b128 a[180:183], v54 offset:40448\n"
"ds_read_b128 a[184:187], v54 offset:41472\n"
"ds_read_b128 a[188:191], v56\n"
"v_add_u32 v56, 4608, v56\n"
"s_waitcnt lgkmcnt(9)\n"
"ds_read_b128 a[192:195], v54 offset:43008\n"
"ds_read_b128 a[196:199], v54 offset:44032\n"
"buffer_load_dwordx4 a[28:31], v1, s[8:11], s72 offen offset:3072 sc1\n"
"ds_read_b128 a[200:203], v54 offset:45056\n"
"ds_read_b128 a[204:207], v54 offset:46080\n"
"ds_read_b128 a[208:211], v56\n"
"v_add_u32 v56, 4608, v56\n"
"s_waitcnt lgkmcnt(9)\n"
"ds_read_b128 a[212:215], v54 offset:47616\n"
"s_add_u32 s72, s72, 4096\n"
"buffer_load_dwordx4 a[32:35], v1, s[8:11], s72 offen offset:0 sc1\n"
"ds_read_b128 a[216:219], v54 offset:48640\n"
"ds_read_b128 a[220:223], v54 offset:49664\n"
"ds_read_b128 a[224:227], v54 offset:50688\n"
"ds_read_b128 a[228:231], v56\n"
"v_add_u32 v56, 4608, v56\n"
"s_waitcnt lgkmcnt(9)\n"
"ds_read_b128 a[232:235], v54 offset:52224\n"
"buffer_load_dwordx4 a[36:39], v1, s[8:11], s72 offen offset:1024 sc1\n"
"buffer_load_dwordx4 v[184:187], v65, s[8:11], s73 offen offset:2048 sc1\n"
"buffer_load_dwordx4 v[188:191], v65, s[8:11], s73 offen offset:2304 sc1\n"
"ds_read_b128 a[236:239], v54 offset:53248\n"
"ds_read_b128 a[240:243], v54 offset:54272\n"
"ds_read_b128 a[244:247], v54 offset:55296\n"
"ds_read_b128 a[248:251], v56\n"
"v_add_u32 v56, 4608, v56\n"
"s_waitcnt vmcnt(11) lgkmcnt(15)\n"
"v_mfma_f32_16x16x32_bf16 v[112:115], a[72:75], a[0:3], 0\n"
"ds_read_b128 a[252:255], v54 offset:56832\n"
"v_mfma_f32_16x16x32_bf16 v[116:119], a[76:79], a[0:3], 0\n"
"buffer_load_dwordx4 a[40:43], v1, s[8:11], s72 offen offset:2048 sc1\n"
"ds_read_b128 a[72:75], v54 offset:57856\n"
"v_mfma_f32_16x16x32_bf16 v[120:123], a[80:83], a[0:3], 0\n"
"ds_read_b128 a[76:79], v54 offset:58880\n"
"v_mfma_f32_16x16x32_bf16 v[124:127], a[84:87], a[0:3], 0\n"
"ds_read_b128 a[80:83], v54 offset:59904\n"
"v_mfma_f32_16x16x32_bf16 v[128:131], a[88:91], a[0:3], 0\n"
"s_waitcnt vmcnt(11) lgkmcnt(9)\n"
"ds_read_b128 a[84:87], v56\n"
"v_add_u32 v56, 4608, v56\n"
"v_mfma_f32_16x16x32_bf16 v[112:115], a[92:95], a[4:7], v[112:115]\n"
"ds_read_b128 a[88:91], v54 offset:61440\n"
"v_mfma_f32_16x16x32_bf16 v[116:119], a[96:99], a[4:7], v[116:119]\n"
"buffer_load_dwordx4 a[44:47], v1, s[8:11], s72 offen offset:3072 sc1\n"
"ds_read_b128 a[92:95], v54 offset:62464\n"
"v_mfma_f32_16x16x32_bf16 v[120:123], a[100:103], a[4:7], v[120:123]\n"
"ds_read_b128 a[96:99], v54 offset:63488\n"
"v_mfma_f32_16x16x32_bf16 v[124:127], a[104:107], a[4:7], v[124:127]\n"
"ds_read_b128 a[100:103], v54 offset:64512\n"
"v_mfma_f32_16x16x32_bf16 v[128:131], a[108:111], a[4:7], v[128:131]\n"
"s_waitcnt vmcnt(11) lgkmcnt(9)\n"
"ds_read_b128 a[104:107], v56\n"
"v_add_u32 v56, 4608, v56\n"
"v_mfma_f32_16x16x32_bf16 v[112:115], a[112:115], a[8:11], v[112:115]\n"
"ds_read_b128 a[108:111], v55 offset:512\n"
"v_mfma_f32_16x16x32_bf16 v[116:119], a[116:119], a[8:11], v[116:119]\n"
"s_add_u32 s72, s72, 4096\n"
"buffer_load_dwordx4 a[48:51], v1, s[8:11], s72 offen offset:0 sc1\n"
"ds_read_b128 a[112:115], v55 offset:1536\n"
"v_mfma_f32_16x16x32_bf16 v[120:123], a[120:123], a[8:11], v[120:123]\n"
"ds_read_b128 a[116:119], v55 offset:2560\n"
"v_mfma_f32_16x16x32_bf16 v[124:127], a[124:127], a[8:11], v[124:127]\n"
"ds_read_b128 a[120:123], v55 offset:3584\n"
"v_mfma_f32_16x16x32_bf16 v[128:131], a[128:131], a[8:11], v[128:131]\n"
"s_waitcnt vmcnt(11) lgkmcnt(9)\n"
"ds_read_b128 a[124:127], v56\n"
"v_add_u32 v56, 4608, v56\n"
"v_mfma_f32_16x16x32_bf16 v[112:115], a[132:135], a[12:15], v[112:115]\n"
"ds_read_b128 a[128:131], v55 offset:5120\n"
"v_mfma_f32_16x16x32_bf16 v[116:119], a[136:139], a[12:15], v[116:119]\n"
"buffer_load_dwordx4 a[52:55], v1, s[8:11], s72 offen offset:1024 sc1\n"
"ds_read_b128 a[132:135], v55 offset:6144\n"
"v_mfma_f32_16x16x32_bf16 v[120:123], a[140:143], a[12:15], v[120:123]\n"
"ds_read_b128 a[136:139], v55 offset:7168\n"
"v_mfma_f32_16x16x32_bf16 v[124:127], a[144:147], a[12:15], v[124:127]\n"
"ds_read_b128 a[140:143], v55 offset:8192\n"
"v_mfma_f32_16x16x32_bf16 v[128:131], a[148:151], a[12:15], v[128:131]\n"
"s_waitcnt vmcnt(11) lgkmcnt(9)\n"
"ds_read_b128 a[144:147], v56\n"
"v_add_u32 v56, 4608, v56\n"
"v_mfma_f32_16x16x32_bf16 v[112:115], a[152:155], a[16:19], v[112:115]\n"
"ds_read_b128 a[148:151], v55 offset:9728\n"
"v_mfma_f32_16x16x32_bf16 v[116:119], a[156:159], a[16:19], v[116:119]\n"
"buffer_load_dwordx4 a[56:59], v1, s[8:11], s72 offen offset:2048 sc1\n"
"ds_read_b128 a[152:155], v55 offset:10752\n"
"v_mfma_f32_16x16x32_bf16 v[120:123], a[160:163], a[16:19], v[120:123]\n"
"ds_read_b128 a[156:159], v55 offset:11776\n"
"v_mfma_f32_16x16x32_bf16 v[124:127], a[164:167], a[16:19], v[124:127]\n"
"ds_read_b128 a[160:163], v55 offset:12800\n"
"v_mfma_f32_16x16x32_bf16 v[128:131], a[168:171], a[16:19], v[128:131]\n"
"s_waitcnt vmcnt(11) lgkmcnt(9)\n"
"ds_read_b128 a[164:167], v56\n"
"v_add_u32 v56, 4608, v56\n"
"v_mfma_f32_16x16x32_bf16 v[112:115], a[172:175], a[20:23], v[112:115]\n"
"ds_read_b128 a[168:171], v55 offset:14336\n"
"v_mfma_f32_16x16x32_bf16 v[116:119], a[176:179], a[20:23], v[116:119]\n"
"buffer_load_dwordx4 a[60:63], v1, s[8:11], s72 offen offset:3072 sc1\n"
"ds_read_b128 a[172:175], v55 offset:15360\n"
"v_mfma_f32_16x16x32_bf16 v[120:123], a[180:183], a[20:23], v[120:123]\n"
"ds_read_b128 a[176:179], v55 offset:16384\n"
"v_mfma_f32_16x16x32_bf16 v[124:127], a[184:187], a[20:23], v[124:127]\n"
"ds_read_b128 a[180:183], v55 offset:17408\n"
"v_mfma_f32_16x16x32_bf16 v[128:131], a[188:191], a[20:23], v[128:131]\n"
"s_waitcnt vmcnt(11) lgkmcnt(9)\n"
"ds_read_b128 a[184:187], v56\n"
"v_add_u32 v56, 4608, v56\n"
"v_mfma_f32_16x16x32_bf16 v[112:115], a[192:195], a[24:27], v[112:115]\n"
"ds_read_b128 a[188:191], v55 offset:18944\n"
"v_mfma_f32_16x16x32_bf16 v[116:119], a[196:199], a[24:27], v[116:119]\n"
"s_add_u32 s72, s72, 4096\n"
"buffer_load_dwordx4 a[64:67], v1, s[8:11], s72 offen offset:0 sc1\n"
"ds_read_b128 a[192:195], v55 offset:19968\n"
"v_mfma_f32_16x16x32_bf16 v[120:123], a[200:203], a[24:27], v[120:123]\n"
"ds_read_b128 a[196:199], v55 offset:20992\n"
"v_mfma_f32_16x16x32_bf16 v[124:127], a[204:207], a[24:27], v[124:127]\n"
"ds_read_b128 a[200:203], v55 offset:22016\n"
"v_mfma_f32_16x16x32_bf16 v[128:131], a[208:211], a[24:27], v[128:131]\n"
"s_waitcnt vmcnt(11) lgkmcnt(9)\n"
"ds_read_b128 a[204:207], v56\n"
"v_add_u32 v56, 4608, v56\n"
"v_mfma_f32_16x16x32_bf16 v[112:115], a[212:215], a[28:31], v[112:115]\n"
"ds_read_b128 a[208:211], v55 offset:23552\n"
"v_mfma_f32_16x16x32_bf16 v[116:119], a[216:219], a[28:31], v[116:119]\n"
"buffer_load_dwordx4 a[68:71], v1, s[8:11], s72 offen offset:1024 sc1\n"
"ds_read_b128 a[212:215], v55 offset:24576\n"
"v_mfma_f32_16x16x32_bf16 v[120:123], a[220:223], a[28:31], v[120:123]\n"
"ds_read_b128 a[216:219], v55 offset:25600\n"
"v_mfma_f32_16x16x32_bf16 v[124:127], a[224:227], a[28:31], v[124:127]\n"
"ds_read_b128 a[220:223], v55 offset:26624\n"
"v_mfma_f32_16x16x32_bf16 v[128:131], a[228:231], a[28:31], v[128:131]\n"
"s_waitcnt vmcnt(11) lgkmcnt(9)\n"
"ds_read_b128 a[224:227], v56\n"
"v_add_u32 v56, 4608, v56\n"
"v_mfma_f32_16x16x32_bf16 v[112:115], a[232:235], a[32:35], v[112:115]\n"
"ds_read_b128 a[228:231], v55 offset:28160\n"
"v_mfma_f32_16x16x32_bf16 v[116:119], a[236:239], a[32:35], v[116:119]\n"
"s_sub_u32 s91, s26, 0\n"
"s_mul_i32 s91, s91, 10240\n"
"s_mul_i32 s90, s29, 82944\n"
"s_add_u32 s90, s90, s91\n"
"s_add_u32 s90, s90, 3538944\n"
"s_add_u32 s91, s91, 0\n"
"s_add_u32 m0, s91, 0\n"
"s_nop 0\n"
"buffer_load_dwordx4 v1, s[8:11], s90 offen offset:0 sc1 lds\n"
"ds_read_b128 a[232:235], v55 offset:29184\n"
"v_mfma_f32_16x16x32_bf16 v[120:123], a[240:243], a[32:35], v[120:123]\n"
"ds_read_b128 a[236:239], v55 offset:30208\n"
"v_mfma_f32_16x16x32_bf16 v[124:127], a[244:247], a[32:35], v[124:127]\n"
"ds_read_b128 a[240:243], v55 offset:31232\n"
"v_mfma_f32_16x16x32_bf16 v[128:131], a[248:251], a[32:35], v[128:131]\n"
"s_waitcnt vmcnt(11) lgkmcnt(9)\n"
"ds_read_b128 a[244:247], v56\n"
"v_add_u32 v56, 4608, v56\n"
"v_mfma_f32_16x16x32_bf16 v[112:115], a[252:255], a[36:39], v[112:115]\n"
"v_mfma_f32_16x16x32_bf16 v[116:119], a[72:75], a[36:39], v[116:119]\n"
"buffer_load_dwordx4 v1, s[8:11], s90 offen offset:1024 sc1 lds\n"
"v_mfma_f32_16x16x32_bf16 v[120:123], a[76:79], a[36:39], v[120:123]\n"
"v_mfma_f32_16x16x32_bf16 v[124:127], a[80:83], a[36:39], v[124:127]\n"
"v_mfma_f32_16x16x32_bf16 v[128:131], a[84:87], a[36:39], v[128:131]\n"
"s_waitcnt vmcnt(9) lgkmcnt(15)\n"
"v_mfma_f32_16x16x32_bf16 v[112:115], a[88:91], a[40:43], v[112:115]\n"
"v_mfma_f32_16x16x32_bf16 v[116:119], a[92:95], a[40:43], v[116:119]\n"
"buffer_load_dwordx4 v1, s[8:11], s90 offen offset:2048 sc1 lds\n"
"v_mfma_f32_16x16x32_bf16 v[120:123], a[96:99], a[40:43], v[120:123]\n"
"v_mfma_f32_16x16x32_bf16 v[124:127], a[100:103], a[40:43], v[124:127]\n"
"v_mfma_f32_16x16x32_bf16 v[128:131], a[104:107], a[40:43], v[128:131]\n"
"s_waitcnt vmcnt(9) lgkmcnt(15)\n"
"v_mfma_f32_16x16x32_bf16 v[112:115], a[108:111], a[44:47], v[112:115]\n"
"s_waitcnt vmcnt(11)\n"
"v_mfma_f32_16x16x32_bf16 v[116:119], a[112:115], a[44:47], v[116:119]\n"
"buffer_load_dwordx4 v1, s[8:11], s90 offen offset:3072 sc1 lds\n"
"v_add_f32 v184, v184, v188\n"
"v_mfma_f32_16x16x32_bf16 v[120:123], a[116:119], a[44:47], v[120:123]\n"
"v_add_f32 v185, v185, v189\n"
"v_mfma_f32_16x16x32_bf16 v[124:127], a[120:123], a[44:47], v[124:127]\n"
"v_add_f32 v186, v186, v190\n"
"v_mfma_f32_16x16x32_bf16 v[128:131], a[124:127], a[44:47], v[128:131]\n"
"s_waitcnt vmcnt(9) lgkmcnt(15)\n"
"v_add_f32 v187, v187, v191\n"
"v_mfma_f32_16x16x32_bf16 v[112:115], a[128:131], a[48:51], v[112:115]\n"
"v_add_f32 v184, v184, v185\n"
"v_mfma_f32_16x16x32_bf16 v[116:119], a[132:135], a[48:51], v[116:119]\n"
"s_add_u32 s90, s90, 4096\n"
"s_add_u32 m0, s91, 4096\n"
"s_nop 0\n"
"buffer_load_dwordx4 v1, s[8:11], s90 offen offset:0 sc1 lds\n"
"v_add_f32 v186, v186, v187\n"
"v_mfma_f32_16x16x32_bf16 v[120:123], a[136:139], a[48:51], v[120:123]\n"
"v_add_f32 v184, v184, v186\n"
"v_mfma_f32_16x16x32_bf16 v[124:127], a[140:143], a[48:51], v[124:127]\n"
"v_mov_b32 v185, v184\n"
"v_mfma_f32_16x16x32_bf16 v[128:131], a[144:147], a[48:51], v[128:131]\n"
"s_waitcnt vmcnt(9) lgkmcnt(15)\n"
"v_permlane32_swap_b32 v185, v184\n"
"v_mfma_f32_16x16x32_bf16 v[112:115], a[148:151], a[52:55], v[112:115]\n"
"v_add_f32 v184, v184, v185\n"
"v_mfma_f32_16x16x32_bf16 v[116:119], a[152:155], a[52:55], v[116:119]\n"
"buffer_load_dwordx4 v1, s[8:11], s90 offen offset:1024 sc1 lds\n"
"v_mov_b32 v185, v184\n"
"v_mfma_f32_16x16x32_bf16 v[120:123], a[156:159], a[52:55], v[120:123]\n"
"s_nop 0\n"
"v_permlane16_swap_b32 v185, v184\n"
"v_mfma_f32_16x16x32_bf16 v[124:127], a[160:163], a[52:55], v[124:127]\n"
"v_add_f32 v184, v184, v185\n"
"v_mfma_f32_16x16x32_bf16 v[128:131], a[164:167], a[52:55], v[128:131]\n"
"s_waitcnt vmcnt(9) lgkmcnt(15)\n"
"v_mul_f32 v184, 0x3ae38e39, v184\n"
"v_mfma_f32_16x16x32_bf16 v[112:115], a[168:171], a[56:59], v[112:115]\n"
"v_add_f32 v185, 0x3727c5ac, v184\n"
"v_mfma_f32_16x16x32_bf16 v[116:119], a[172:175], a[56:59], v[116:119]\n"
"buffer_load_dwordx4 v1, s[8:11], s90 offen offset:2048 sc1 lds\n"
"v_rsq_f32 v64, v185\n"
"v_mfma_f32_16x16x32_bf16 v[120:123], a[176:179], a[56:59], v[120:123]\n"
"v_mul_f32 v184, v184, v64\n"
"v_mfma_f32_16x16x32_bf16 v[124:127], a[180:183], a[56:59], v[124:127]\n"
"v_fmaak_f32 v184, v184, v64, 0x3727c5ac\n"
"v_mfma_f32_16x16x32_bf16 v[128:131], a[184:187], a[56:59], v[128:131]\n"
"s_waitcnt vmcnt(9) lgkmcnt(10)\n"
"v_rsq_f32 v184, v184\n"
"v_mfma_f32_16x16x32_bf16 v[112:115], a[188:191], a[60:63], v[112:115]\n"
"v_mul_f32 v192, v184, v64\n"
"v_mfma_f32_16x16x32_bf16 v[116:119], a[192:195], a[60:63], v[116:119]\n"
"buffer_load_dwordx4 v1, s[8:11], s90 offen offset:3072 sc1 lds\n"
"v_cndmask_b32 v192, 0, v192, s[98:99]\n"
"v_mfma_f32_16x16x32_bf16 v[120:123], a[196:199], a[60:63], v[120:123]\n"
"v_mul_f32 v194, 0xc0135761, v192\n"
"v_mfma_f32_16x16x32_bf16 v[124:127], a[200:203], a[60:63], v[124:127]\n"
"v_mul_f32 v184, v192, v192\n"
"v_mfma_f32_16x16x32_bf16 v[128:131], a[204:207], a[60:63], v[128:131]\n"
"s_waitcnt vmcnt(9) lgkmcnt(5)\n"
"v_mul_f32 v184, 0x3d372713, v184\n"
"v_mfma_f32_16x16x32_bf16 v[112:115], a[208:211], a[64:67], v[112:115]\n"
"v_mul_f32 v196, v184, v194\n"
"v_mfma_f32_16x16x32_bf16 v[116:119], a[212:215], a[64:67], v[116:119]\n"
"s_add_u32 s90, s90, 4096\n"
"s_add_u32 m0, s91, 8192\n"
"s_nop 0\n"
"buffer_load_dwordx4 v1, s[8:11], s90 offen offset:0 sc1 lds\n"
"v_mfma_f32_16x16x32_bf16 v[120:123], a[216:219], a[64:67], v[120:123]\n"
"v_mfma_f32_16x16x32_bf16 v[124:127], a[220:223], a[64:67], v[124:127]\n"
"v_mfma_f32_16x16x32_bf16 v[128:131], a[224:227], a[64:67], v[128:131]\n"
"s_waitcnt vmcnt(9) lgkmcnt(0)\n"
"v_mfma_f32_16x16x32_bf16 v[112:115], a[228:231], a[68:71], v[112:115]\n"
"v_mfma_f32_16x16x32_bf16 v[116:119], a[232:235], a[68:71], v[116:119]\n"
"buffer_load_dwordx4 v1, s[8:11], s90 offen offset:1024 sc1 lds\n"
"v_mfma_f32_16x16x32_bf16 v[120:123], a[236:239], a[68:71], v[120:123]\n"
"v_mfma_f32_16x16x32_bf16 v[124:127], a[240:243], a[68:71], v[124:127]\n"
"v_mfma_f32_16x16x32_bf16 v[128:131], a[244:247], a[68:71], v[128:131]\n"
"s_barrier\n"
"s_lshl_b32 s92, s29, 11\n"
"s_add_u32 s92, s92, 10485760\n"
"s_lshl_b32 s93, s29, 8\n"
"s_add_u32 s93, s93, 10502144\n"
"v_pk_mul_f32 v[200:201], v[112:113], v[112:113]\n"
"v_pk_mul_f32 v[202:203], v[114:115], v[114:115]\n"
"v_pk_fma_f32 v[200:201], v[200:201], v[196:197], v[194:195] op_sel_hi:[1,0,0]\n"
"v_pk_fma_f32 v[202:203], v[202:203], v[196:197], v[194:195] op_sel_hi:[1,0,0]\n"
"v_pk_mul_f32 v[200:201], v[200:201], v[112:113]\n"
"v_pk_mul_f32 v[202:203], v[202:203], v[114:115]\n"
"v_exp_f32 v200, v200\n"
"v_exp_f32 v201, v201\n"
"v_exp_f32 v202, v202\n"
"v_exp_f32 v203, v203\n"
"v_pk_mul_f32 v[112:113], v[112:113], v[192:193] op_sel_hi:[1,0]\n"
"v_pk_mul_f32 v[114:115], v[114:115], v[192:193] op_sel_hi:[1,0]\n"
"v_pk_add_f32 v[200:201], v[200:201], 1.0 op_sel_hi:[1,0]\n"
"v_pk_add_f32 v[202:203], v[202:203], 1.0 op_sel_hi:[1,0]\n"
"v_rcp_f32 v200, v200\n"
"v_rcp_f32 v201, v201\n"
"v_rcp_f32 v202, v202\n"
"v_rcp_f32 v203, v203\n"
"v_pk_mul_f32 v[112:113], v[112:113], v[200:201]\n"
"v_pk_mul_f32 v[114:115], v[114:115], v[202:203]\n"
"v_cvt_pk_bf16_f32 v172, v112, v113\n"
"v_cvt_pk_bf16_f32 v173, v114, v115\n"
"v_pk_mul_f32 v[200:201], v[116:117], v[116:117]\n"
"v_pk_mul_f32 v[202:203], v[118:119], v[118:119]\n"
"v_pk_fma_f32 v[200:201], v[200:201], v[196:197], v[194:195] op_sel_hi:[1,0,0]\n"
"v_pk_fma_f32 v[202:203], v[202:203], v[196:197], v[194:195] op_sel_hi:[1,0,0]\n"
"v_pk_mul_f32 v[200:201], v[200:201], v[116:117]\n"
"v_pk_mul_f32 v[202:203], v[202:203], v[118:119]\n"
"v_exp_f32 v200, v200\n"
"v_exp_f32 v201, v201\n"
"v_exp_f32 v202, v202\n"
"v_exp_f32 v203, v203\n"
"v_pk_mul_f32 v[116:117], v[116:117], v[192:193] op_sel_hi:[1,0]\n"
"v_pk_mul_f32 v[118:119], v[118:119], v[192:193] op_sel_hi:[1,0]\n"
"v_pk_add_f32 v[200:201], v[200:201], 1.0 op_sel_hi:[1,0]\n"
"v_pk_add_f32 v[202:203], v[202:203], 1.0 op_sel_hi:[1,0]\n"
"v_rcp_f32 v200, v200\n"
"v_rcp_f32 v201, v201\n"
"v_rcp_f32 v202, v202\n"
"v_rcp_f32 v203, v203\n"
"v_pk_mul_f32 v[116:117], v[116:117], v[200:201]\n"
"v_pk_mul_f32 v[118:119], v[118:119], v[202:203]\n"
"v_cvt_pk_bf16_f32 v174, v116, v117\n"
"v_cvt_pk_bf16_f32 v175, v118, v119\n"
"buffer_store_dwordx4 v[172:175], v45, s[8:11], s92 offen sc1\n"
"v_pk_mul_f32 v[200:201], v[120:121], v[120:121]\n"
"v_pk_mul_f32 v[202:203], v[122:123], v[122:123]\n"
"v_pk_fma_f32 v[200:201], v[200:201], v[196:197], v[194:195] op_sel_hi:[1,0,0]\n"
"v_pk_fma_f32 v[202:203], v[202:203], v[196:197], v[194:195] op_sel_hi:[1,0,0]\n"
"v_pk_mul_f32 v[200:201], v[200:201], v[120:121]\n"
"v_pk_mul_f32 v[202:203], v[202:203], v[122:123]\n"
"v_exp_f32 v200, v200\n"
"v_exp_f32 v201, v201\n"
"v_exp_f32 v202, v202\n"
"v_exp_f32 v203, v203\n"
"v_pk_mul_f32 v[120:121], v[120:121], v[192:193] op_sel_hi:[1,0]\n"
"v_pk_mul_f32 v[122:123], v[122:123], v[192:193] op_sel_hi:[1,0]\n"
"v_pk_add_f32 v[200:201], v[200:201], 1.0 op_sel_hi:[1,0]\n"
"v_pk_add_f32 v[202:203], v[202:203], 1.0 op_sel_hi:[1,0]\n"
"v_rcp_f32 v200, v200\n"
"v_rcp_f32 v201, v201\n"
"v_rcp_f32 v202, v202\n"
"v_rcp_f32 v203, v203\n"
"v_pk_mul_f32 v[120:121], v[120:121], v[200:201]\n"
"v_pk_mul_f32 v[122:123], v[122:123], v[202:203]\n"
"v_cvt_pk_bf16_f32 v176, v120, v121\n"
"v_cvt_pk_bf16_f32 v177, v122, v123\n"
"v_pk_mul_f32 v[200:201], v[124:125], v[124:125]\n"
"v_pk_mul_f32 v[202:203], v[126:127], v[126:127]\n"
"v_pk_fma_f32 v[200:201], v[200:201], v[196:197], v[194:195] op_sel_hi:[1,0,0]\n"
"v_pk_fma_f32 v[202:203], v[202:203], v[196:197], v[194:195] op_sel_hi:[1,0,0]\n"
"v_pk_mul_f32 v[200:201], v[200:201], v[124:125]\n"
"v_pk_mul_f32 v[202:203], v[202:203], v[126:127]\n"
"v_exp_f32 v200, v200\n"
"v_exp_f32 v201, v201\n"
"v_exp_f32 v202, v202\n"
"v_exp_f32 v203, v203\n"
"v_pk_mul_f32 v[124:125], v[124:125], v[192:193] op_sel_hi:[1,0]\n"
"v_pk_mul_f32 v[126:127], v[126:127], v[192:193] op_sel_hi:[1,0]\n"
"v_pk_add_f32 v[200:201], v[200:201], 1.0 op_sel_hi:[1,0]\n"
"v_pk_add_f32 v[202:203], v[202:203], 1.0 op_sel_hi:[1,0]\n"
"v_rcp_f32 v200, v200\n"
"v_rcp_f32 v201, v201\n"
"v_rcp_f32 v202, v202\n"
"v_rcp_f32 v203, v203\n"
"v_pk_mul_f32 v[124:125], v[124:125], v[200:201]\n"
"v_pk_mul_f32 v[126:127], v[126:127], v[202:203]\n"
"v_cvt_pk_bf16_f32 v178, v124, v125\n"
"v_cvt_pk_bf16_f32 v179, v126, v127\n"
"buffer_store_dwordx4 v[176:179], v45, s[8:11], s92 offen offset:1024 sc1\n"
"v_pk_mul_f32 v[200:201], v[128:129], v[128:129]\n"
"v_pk_mul_f32 v[202:203], v[130:131], v[130:131]\n"
"v_pk_fma_f32 v[200:201], v[200:201], v[196:197], v[194:195] op_sel_hi:[1,0,0]\n"
"v_pk_fma_f32 v[202:203], v[202:203], v[196:197], v[194:195] op_sel_hi:[1,0,0]\n"
"v_pk_mul_f32 v[200:201], v[200:201], v[128:129]\n"
"v_pk_mul_f32 v[202:203], v[202:203], v[130:131]\n"
"v_exp_f32 v200, v200\n"
"v_exp_f32 v201, v201\n"
"v_exp_f32 v202, v202\n"
"v_exp_f32 v203, v203\n"
"v_pk_mul_f32 v[128:129], v[128:129], v[192:193] op_sel_hi:[1,0]\n"
"v_pk_mul_f32 v[130:131], v[130:131], v[192:193] op_sel_hi:[1,0]\n"
"v_pk_add_f32 v[200:201], v[200:201], 1.0 op_sel_hi:[1,0]\n"
"v_pk_add_f32 v[202:203], v[202:203], 1.0 op_sel_hi:[1,0]\n"
"v_rcp_f32 v200, v200\n"
"v_rcp_f32 v201, v201\n"
"v_rcp_f32 v202, v202\n"
"v_rcp_f32 v203, v203\n"
"v_pk_mul_f32 v[128:129], v[128:129], v[200:201]\n"
"v_pk_mul_f32 v[130:131], v[130:131], v[202:203]\n"
"v_cvt_pk_bf16_f32 v180, v128, v129\n"
"v_cvt_pk_bf16_f32 v181, v130, v131\n"
"s_nop 0\n"
"v_permlane16_swap_b32 v180, v182\n"
"v_permlane16_swap_b32 v181, v183\n"
"buffer_store_dwordx4 v[180:183], v46, s[8:11], s93 offen sc1\n"
"s_lshl_b32 s80, s29, 3\n"
"s_add_u32 s80, s80, 65536\n"
"s_cmp_lg_u32 s26, 0\n"
"s_cbranch_scc0 .Lfused_block_kernel_s55\n"
"s_sub_u32 s91, s26, 1\n"
"s_mul_i32 s91, s91, 14336\n"
"s_mul_i32 s90, s29, 82944\n"
"s_add_u32 s90, s90, s91\n"
"s_add_u32 s90, s90, 3578880\n"
"s_add_u32 s91, s91, 39936\n"
"s_add_u32 m0, s91, 0\n"
"s_nop 0\n"
"buffer_load_dwordx4 v1, s[8:11], s90 offen offset:0 sc1 lds\n"
"buffer_load_dwordx4 v1, s[8:11], s90 offen offset:1024 sc1 lds\n"
"buffer_load_dwordx4 v1, s[8:11], s90 offen offset:2048 sc1 lds\n"
"buffer_load_dwordx4 v1, s[8:11], s90 offen offset:3072 sc1 lds\n"
"s_add_u32 s90, s90, 4096\n"
"s_add_u32 m0, s91, 4096\n"
"s_nop 0\n"
"buffer_load_dwordx4 v1, s[8:11], s90 offen offset:0 sc1 lds\n"
"buffer_load_dwordx4 v1, s[8:11], s90 offen offset:1024 sc1 lds\n"
".Lfused_block_kernel_s55:\n"
"s_cmp_lg_u32 s26, 0\n"
"s_cbranch_scc0 .Lfused_block_kernel_s56\n"
"s_waitcnt vmcnt(6)\n"
".Lfused_block_kernel_s56:\n"
"s_cmp_eq_u32 s26, 0\n"
"s_cbranch_scc0 .Lfused_block_kernel_s57\n"
"s_waitcnt vmcnt(0)\n"
".Lfused_block_kernel_s57:\n"
"s_barrier\n"
"s_cmp_eq_u32 s26, 0\n"
"s_cbranch_scc0 .Lfused_block_kernel_s58\n"
"buffer_store_dwordx2 v[22:23], v48, s[8:11], s80 offen offset:512 sc1\n"
".Lfused_block_kernel_s58:\n"
"s_cmp_lg_u32 s26, 0\n"
"s_cbranch_scc0 .Lfused_block_kernel_s59\n"
"buffer_load_dwordx4 v1, s[8:11], s90 offen offset:2048 sc1 lds\n"
"buffer_load_dwordx4 v1, s[8:11], s90 offen offset:3072 sc1 lds\n"
"s_add_u32 s90, s90, 4096\n"
"s_add_u32 m0, s91, 8192\n"
"s_nop 0\n"
"buffer_load_dwordx4 v1, s[8:11], s90 offen offset:0 sc1 lds\n"
"buffer_load_dwordx4 v1, s[8:11], s90 offen offset:1024 sc1 lds\n"
"buffer_load_dwordx4 v1, s[8:11], s90 offen offset:2048 sc1 lds\n"
"buffer_load_dwordx4 v1, s[8:11], s90 offen offset:3072 sc1 lds\n"
"s_add_u32 s90, s90, 4096\n"
"s_add_u32 m0, s91, 12288\n"
"s_nop 0\n"
"buffer_load_dwordx4 v1, s[8:11], s90 offen offset:0 sc1 lds\n"
"buffer_load_dwordx4 v1, s[8:11], s90 offen offset:1024 sc1 lds\n"
"s_sub_u32 s90, s26, 1\n"
"s_lshl_b32 s90, s90, 10\n"
"s_add_u32 m0, s90, 82944\n"
"s_add_u32 s90, s90, 724992\n"
"buffer_load_dwordx4 v1, s[8:11], s90 offen sc1 lds\n"
"s_cmp_eq_u32 s70, 2\n"
"s_cbranch_scc0 .Lfused_block_kernel_s60\n"
"s_waitcnt vmcnt(48)\n"
"v_perm_b32 v184, v228, v224, s4\n"
"v_perm_b32 v185, v236, v232, s4\n"
"v_perm_b32 v186, v244, v240, s4\n"
"v_perm_b32 v187, v252, v248, s4\n"
"buffer_store_dwordx4 v[184:187], v61, s[8:11], s62 offen sc1\n"
"v_perm_b32 v188, v228, v224, s5\n"
"v_perm_b32 v189, v236, v232, s5\n"
"v_perm_b32 v190, v244, v240, s5\n"
"v_perm_b32 v191, v252, v248, s5\n"
"buffer_store_dwordx4 v[188:191], v61, s[8:11], s63 offen sc1\n"
"v_perm_b32 v184, v229, v225, s4\n"
"v_perm_b32 v185, v237, v233, s4\n"
"v_perm_b32 v186, v245, v241, s4\n"
"v_perm_b32 v187, v253, v249, s4\n"
"buffer_store_dwordx4 v[184:187], v61, s[8:11], s64 offen sc1\n"
"v_perm_b32 v188, v229, v225, s5\n"
"v_perm_b32 v189, v237, v233, s5\n"
"v_perm_b32 v190, v245, v241, s5\n"
"v_perm_b32 v191, v253, v249, s5\n"
"buffer_store_dwordx4 v[188:191], v61, s[8:11], s65 offen sc1\n"
"v_perm_b32 v184, v230, v226, s4\n"
"v_perm_b32 v185, v238, v234, s4\n"
"v_perm_b32 v186, v246, v242, s4\n"
"v_perm_b32 v187, v254, v250, s4\n"
"buffer_store_dwordx4 v[184:187], v61, s[8:11], s66 offen sc1\n"
"v_perm_b32 v188, v230, v226, s5\n"
"v_perm_b32 v189, v238, v234, s5\n"
"v_perm_b32 v190, v246, v242, s5\n"
"v_perm_b32 v191, v254, v250, s5\n"
"buffer_store_dwordx4 v[188:191], v61, s[8:11], s67 offen sc1\n"
"v_perm_b32 v184, v231, v227, s4\n"
"v_perm_b32 v185, v239, v235, s4\n"
"v_perm_b32 v186, v247, v243, s4\n"
"v_perm_b32 v187, v255, v251, s4\n"
"buffer_store_dwordx4 v[184:187], v61, s[8:11], s68 offen sc1\n"
"v_perm_b32 v188, v231, v227, s5\n"
"v_perm_b32 v189, v239, v235, s5\n"
"v_perm_b32 v190, v247, v243, s5\n"
"v_perm_b32 v191, v255, v251, s5\n"
"buffer_store_dwordx4 v[188:191], v61, s[8:11], s69 offen sc1\n"
".Lfused_block_kernel_s60:\n"
".Lfused_block_kernel_s59:\n"
"v_pk_mul_f32 v[112:113], v[152:153], v[64:65] op_sel_hi:[1,0]\n"
"v_pk_mul_f32 v[114:115], v[154:155], v[64:65] op_sel_hi:[1,0]\n"
"v_pk_mul_f32 v[116:117], v[156:157], v[64:65] op_sel_hi:[1,0]\n"
"v_pk_mul_f32 v[118:119], v[158:159], v[64:65] op_sel_hi:[1,0]\n"
"v_pk_mul_f32 v[120:121], v[160:161], v[64:65] op_sel_hi:[1,0]\n"
"v_pk_mul_f32 v[122:123], v[162:163], v[64:65] op_sel_hi:[1,0]\n"
"v_pk_mul_f32 v[124:125], v[164:165], v[64:65] op_sel_hi:[1,0]\n"
"v_pk_mul_f32 v[126:127], v[166:167], v[64:65] op_sel_hi:[1,0]\n"
"v_pk_mul_f32 v[128:129], v[168:169], v[64:65] op_sel_hi:[1,0]\n"
"v_pk_mul_f32 v[130:131], v[170:171], v[64:65] op_sel_hi:[1,0]\n"
"v_mov_b32 v62, 0\n"
"v_mov_b32 v4, v53\n"
"v_add_u32 v5, 8192, v4\n"
"v_add_u32 v6, 16384, v4\n"
"s_lshl_b32 s72, s29, 2\n"
"s_add_u32 s72, s72, s26\n"
"s_mul_i32 s72, s72, 331776\n"
"s_add_u32 s72, s72, 18878464\n"
"s_add_u32 s94, s52, s72\n"
"s_addc_u32 s95, s53, 0\n"
"s_mov_b32 s86, 6144\n"
"s_cmp_eq_u32 s26, 0\n"
"s_cbranch_scc0 .Lfused_block_kernel_s61\n"
"s_mov_b32 s87, 0x4000\n"
".Lfused_block_kernel_poll62:\n"
"buffer_load_dwordx2 v[72:73], v50, s[8:11], s31 offen offset:512 sc1\n"
"buffer_load_dwordx4 v[74:77], v66, s[8:11], s86 offen offset:0 sc1\n"
"s_waitcnt vmcnt(0)\n"
"v_cmp_ne_u64 vcc, s[24:25], v[72:73]\n"
"v_cmp_ne_u64 s[84:85], s[24:25], v[74:75]\n"
"s_nop 0\n"
"s_or_b64 vcc, vcc, s[84:85]\n"
"v_cmp_ne_u64 s[84:85], s[24:25], v[76:77]\n"
"s_nop 0\n"
"s_or_b64 vcc, vcc, s[84:85]\n"
"s_cmp_eq_u64 vcc, 0\n"
"s_cbranch_scc1 .Lfused_block_kernel_pok63\n"
"s_sub_u32 s87, s87, 1\n"
"s_cmp_lg_u32 s87, 0\n"
"s_cbranch_scc1 .Lfused_block_kernel_poll62\n"
".Lfused_block_kernel_pok63:\n"
".Lfused_block_kernel_s61:\n"
"s_waitcnt vmcnt(0)\n"
"s_barrier\n"
"s_cmp_eq_u32 s26, 1\n"
"s_cbranch_scc0 .Lfused_block_kernel_s64\n"
"s_lshl_b32 s72, s33, 3\n"
"s_add_u32 s72, s72, 8192\n"
"buffer_store_dwordx2 v[22:23], v52, s[8:11], s72 offen offset:0 sc1\n"
".Lfused_block_kernel_s64:\n"
"s_add_u32 s72, s71, 10485760\n"
"v_mov_b32 v56, v57\n"
"buffer_load_dwordx4 a[0:3], v1, s[8:11], s72 offen offset:0 sc1\n"
"ds_read_b128 a[72:75], v53 offset:0\n"
"ds_read_b128 a[76:79], v53 offset:1024\n"
"ds_read_b128 a[80:83], v53 offset:2048\n"
"ds_read_b128 a[84:87], v53 offset:3072\n"
"ds_read_b128 a[88:91], v56\n"
"v_add_u32 v56, v56, v59\n"
"buffer_load_dwordx4 a[4:7], v1, s[8:11], s72 offen offset:1024 sc1\n"
"ds_read_b128 a[92:95], v53 offset:4608\n"
"ds_read_b128 a[96:99], v53 offset:5632\n"
"ds_read_b128 a[100:103], v53 offset:6656\n"
"ds_read_b128 a[104:107], v53 offset:7680\n"
"buffer_load_dwordx4 a[8:11], v1, s[8:11], s72 offen offset:2048 sc1\n"
"ds_read_b128 a[108:111], v56\n"
"v_add_u32 v56, v56, v59\n"
"s_waitcnt lgkmcnt(9)\n"
"ds_read_b128 a[112:115], v53 offset:9216\n"
"ds_read_b128 a[116:119], v53 offset:10240\n"
"ds_read_b128 a[120:123], v53 offset:11264\n"
"ds_read_b128 a[124:127], v53 offset:12288\n"
"buffer_load_dwordx4 a[12:15], v1, s[8:11], s72 offen offset:3072 sc1\n"
"ds_read_b128 a[128:131], v56\n"
"v_add_u32 v56, v56, v59\n"
"s_waitcnt lgkmcnt(9)\n"
"ds_read_b128 a[132:135], v53 offset:13824\n"
"ds_read_b128 a[136:139], v53 offset:14848\n"
"ds_read_b128 a[140:143], v53 offset:15872\n"
"s_add_u32 s72, s72, 4096\n"
"buffer_load_dwordx4 a[16:19], v1, s[8:11], s72 offen offset:0 sc1\n"
"ds_read_b128 a[144:147], v53 offset:16896\n"
"ds_read_b128 a[148:151], v56\n"
"v_add_u32 v56, v56, v59\n"
"s_waitcnt lgkmcnt(9)\n"
"ds_read_b128 a[152:155], v53 offset:18432\n"
"ds_read_b128 a[156:159], v53 offset:19456\n"
"ds_read_b128 a[160:163], v53 offset:20480\n"
"buffer_load_dwordx4 a[20:23], v1, s[8:11], s72 offen offset:1024 sc1\n"
"ds_read_b128 a[164:167], v53 offset:21504\n"
"ds_read_b128 a[168:171], v56\n"
"v_add_u32 v56, v56, v59\n"
"s_waitcnt lgkmcnt(9)\n"
"ds_read_b128 a[172:175], v53 offset:23040\n"
"ds_read_b128 a[176:179], v53 offset:24064\n"
"buffer_load_dwordx4 a[24:27], v1, s[8:11], s72 offen offset:2048 sc1\n"
"ds_read_b128 a[180:183], v53 offset:25088\n"
"ds_read_b128 a[184:187], v53 offset:26112\n"
"ds_read_b128 a[188:191], v56\n"
"v_add_u32 v56, v56, v59\n"
"s_waitcnt lgkmcnt(9)\n"
"ds_read_b128 a[192:195], v53 offset:27648\n"
"ds_read_b128 a[196:199], v53 offset:28672\n"
"buffer_load_dwordx4 a[28:31], v1, s[8:11], s72 offen offset:3072 sc1\n"
"ds_read_b128 a[200:203], v53 offset:29696\n"
"ds_read_b128 a[204:207], v53 offset:30720\n"
"ds_read_b128 a[208:211], v56\n"
"v_add_u32 v56, v56, v59\n"
"s_waitcnt lgkmcnt(9)\n"
"ds_read_b128 a[212:215], v53 offset:32256\n"
"s_add_u32 s72, s72, 4096\n"
"buffer_load_dwordx4 a[32:35], v1, s[8:11], s72 offen offset:0 sc1\n"
"ds_read_b128 a[216:219], v53 offset:33280\n"
"ds_read_b128 a[220:223], v53 offset:34304\n"
"ds_read_b128 a[224:227], v53 offset:35328\n"
"ds_read_b128 a[228:231], v56\n"
"v_add_u32 v56, v56, v59\n"
"s_waitcnt lgkmcnt(9)\n"
"ds_read_b128 a[232:235], v53 offset:36864\n"
"buffer_load_dwordx4 a[36:39], v1, s[8:11], s72 offen offset:1024 sc1\n"
"ds_read_b128 a[236:239], v53 offset:37888\n"
"ds_read_b128 a[240:243], v53 offset:38912\n"
"ds_read_b128 a[244:247], v53 offset:39936\n"
"ds_read_b128 a[248:251], v56\n"
"v_add_u32 v56, v56, v59\n"
"s_waitcnt vmcnt(9) lgkmcnt(15)\n"
"v_mfma_f32_16x16x32_bf16 v[112:115], a[72:75], a[0:3], v[112:115]\n"
"ds_read_b128 a[252:255], v53 offset:41472\n"
"v_mfma_f32_16x16x32_bf16 v[116:119], a[76:79], a[0:3], v[116:119]\n"
"buffer_load_dwordx4 a[40:43], v1, s[8:11], s72 offen offset:2048 sc1\n"
"ds_read_b128 a[72:75], v53 offset:42496\n"
"v_mfma_f32_16x16x32_bf16 v[120:123], a[80:83], a[0:3], v[120:123]\n"
"ds_read_b128 a[76:79], v53 offset:43520\n"
"v_mfma_f32_16x16x32_bf16 v[124:127], a[84:87], a[0:3], v[124:127]\n"
"ds_read_b128 a[80:83], v53 offset:44544\n"
"v_mfma_f32_16x16x32_bf16 v[128:131], a[88:91], a[0:3], v[128:131]\n"
"s_waitcnt vmcnt(9) lgkmcnt(9)\n"
"ds_read_b128 a[84:87], v56\n"
"v_add_u32 v56, v56, v59\n"
"v_mfma_f32_16x16x32_bf16 v[112:115], a[92:95], a[4:7], v[112:115]\n"
"ds_read_b128 a[88:91], v53 offset:46080\n"
"v_mfma_f32_16x16x32_bf16 v[116:119], a[96:99], a[4:7], v[116:119]\n"
"buffer_load_dwordx4 a[44:47], v1, s[8:11], s72 offen offset:3072 sc1\n"
"ds_read_b128 a[92:95], v53 offset:47104\n"
"v_mfma_f32_16x16x32_bf16 v[120:123], a[100:103], a[4:7], v[120:123]\n"
"ds_read_b128 a[96:99], v53 offset:48128\n"
"v_mfma_f32_16x16x32_bf16 v[124:127], a[104:107], a[4:7], v[124:127]\n"
"ds_read_b128 a[100:103], v53 offset:49152\n"
"v_mfma_f32_16x16x32_bf16 v[128:131], a[108:111], a[4:7], v[128:131]\n"
"s_waitcnt vmcnt(9) lgkmcnt(9)\n"
"ds_read_b128 a[104:107], v56\n"
"v_add_u32 v56, v56, v59\n"
"v_mfma_f32_16x16x32_bf16 v[112:115], a[112:115], a[8:11], v[112:115]\n"
"ds_read_b128 a[108:111], v53 offset:50688\n"
"v_mfma_f32_16x16x32_bf16 v[116:119], a[116:119], a[8:11], v[116:119]\n"
"s_add_u32 s72, s72, 4096\n"
"buffer_load_dwordx4 a[48:51], v1, s[8:11], s72 offen offset:0 sc1\n"
"ds_read_b128 a[112:115], v53 offset:51712\n"
"v_mfma_f32_16x16x32_bf16 v[120:123], a[120:123], a[8:11], v[120:123]\n"
"ds_read_b128 a[116:119], v53 offset:52736\n"
"v_mfma_f32_16x16x32_bf16 v[124:127], a[124:127], a[8:11], v[124:127]\n"
"ds_read_b128 a[120:123], v53 offset:53760\n"
"v_mfma_f32_16x16x32_bf16 v[128:131], a[128:131], a[8:11], v[128:131]\n"
"s_waitcnt vmcnt(9) lgkmcnt(9)\n"
"ds_read_b128 a[124:127], v56\n"
"v_add_u32 v56, v56, v59\n"
"v_mfma_f32_16x16x32_bf16 v[112:115], a[132:135], a[12:15], v[112:115]\n"
"ds_read_b128 a[128:131], v53 offset:55296\n"
"v_mfma_f32_16x16x32_bf16 v[116:119], a[136:139], a[12:15], v[116:119]\n"
"buffer_load_dwordx4 a[52:55], v1, s[8:11], s72 offen offset:1024 sc1\n"
"ds_read_b128 a[132:135], v53 offset:56320\n"
"v_mfma_f32_16x16x32_bf16 v[120:123], a[140:143], a[12:15], v[120:123]\n"
"ds_read_b128 a[136:139], v53 offset:57344\n"
"v_mfma_f32_16x16x32_bf16 v[124:127], a[144:147], a[12:15], v[124:127]\n"
"ds_read_b128 a[140:143], v53 offset:58368\n"
"v_mfma_f32_16x16x32_bf16 v[128:131], a[148:151], a[12:15], v[128:131]\n"
"s_waitcnt vmcnt(9) lgkmcnt(9)\n"
"ds_read_b128 a[144:147], v56\n"
"v_add_u32 v56, v56, v59\n"
"v_mfma_f32_16x16x32_bf16 v[112:115], a[152:155], a[16:19], v[112:115]\n"
"ds_read_b128 a[148:151], v53 offset:59904\n"
"v_mfma_f32_16x16x32_bf16 v[116:119], a[156:159], a[16:19], v[116:119]\n"
"buffer_load_dwordx4 a[56:59], v1, s[8:11], s72 offen offset:2048 sc1\n"
"ds_read_b128 a[152:155], v53 offset:60928\n"
"v_mfma_f32_16x16x32_bf16 v[120:123], a[160:163], a[16:19], v[120:123]\n"
"ds_read_b128 a[156:159], v53 offset:61952\n"
"v_mfma_f32_16x16x32_bf16 v[124:127], a[164:167], a[16:19], v[124:127]\n"
"ds_read_b128 a[160:163], v53 offset:62976\n"
"v_mfma_f32_16x16x32_bf16 v[128:131], a[168:171], a[16:19], v[128:131]\n"
"s_waitcnt vmcnt(9) lgkmcnt(9)\n"
"ds_read_b128 a[164:167], v56\n"
"v_add_u32 v56, v56, v59\n"
"v_mfma_f32_16x16x32_bf16 v[112:115], a[172:175], a[20:23], v[112:115]\n"
"ds_read_b128 a[168:171], v53 offset:64512\n"
"v_mfma_f32_16x16x32_bf16 v[116:119], a[176:179], a[20:23], v[116:119]\n"
"buffer_load_dwordx4 a[60:63], v1, s[8:11], s72 offen offset:3072 sc1\n"
"ds_read_b128 a[172:175], v54 offset:0\n"
"v_mfma_f32_16x16x32_bf16 v[120:123], a[180:183], a[20:23], v[120:123]\n"
"ds_read_b128 a[176:179], v54 offset:1024\n"
"v_mfma_f32_16x16x32_bf16 v[124:127], a[184:187], a[20:23], v[124:127]\n"
"ds_read_b128 a[180:183], v54 offset:2048\n"
"v_mfma_f32_16x16x32_bf16 v[128:131], a[188:191], a[20:23], v[128:131]\n"
"s_waitcnt vmcnt(9) lgkmcnt(9)\n"
"ds_read_b128 a[184:187], v56\n"
"v_add_u32 v56, v56, v59\n"
"v_mfma_f32_16x16x32_bf16 v[112:115], a[192:195], a[24:27], v[112:115]\n"
"ds_read_b128 a[188:191], v54 offset:3584\n"
"v_mfma_f32_16x16x32_bf16 v[116:119], a[196:199], a[24:27], v[116:119]\n"
"s_add_u32 s72, s72, 4096\n"
"buffer_load_dwordx4 a[64:67], v1, s[8:11], s72 offen offset:0 sc1\n"
"ds_read_b128 a[192:195], v54 offset:4608\n"
"v_mfma_f32_16x16x32_bf16 v[120:123], a[200:203], a[24:27], v[120:123]\n"
"ds_read_b128 a[196:199], v54 offset:5632\n"
"v_mfma_f32_16x16x32_bf16 v[124:127], a[204:207], a[24:27], v[124:127]\n"
"ds_read_b128 a[200:203], v54 offset:6656\n"
"v_mfma_f32_16x16x32_bf16 v[128:131], a[208:211], a[24:27], v[128:131]\n"
"s_waitcnt vmcnt(9) lgkmcnt(9)\n"
"ds_read_b128 a[204:207], v56\n"
"v_add_u32 v56, v56, v59\n"
"v_mfma_f32_16x16x32_bf16 v[112:115], a[212:215], a[28:31], v[112:115]\n"
"ds_read_b128 a[208:211], v54 offset:8192\n"
"v_mfma_f32_16x16x32_bf16 v[116:119], a[216:219], a[28:31], v[116:119]\n"
"buffer_load_dwordx4 a[68:71], v1, s[8:11], s72 offen offset:1024 sc1\n"
"ds_read_b128 a[212:215], v54 offset:9216\n"
"v_mfma_f32_16x16x32_bf16 v[120:123], a[220:223], a[28:31], v[120:123]\n"
"ds_read_b128 a[216:219], v54 offset:10240\n"
"v_mfma_f32_16x16x32_bf16 v[124:127], a[224:227], a[28:31], v[124:127]\n"
"ds_read_b128 a[220:223], v54 offset:11264\n"
"v_mfma_f32_16x16x32_bf16 v[128:131], a[228:231], a[28:31], v[128:131]\n"
"s_waitcnt vmcnt(9) lgkmcnt(9)\n"
"ds_read_b128 a[224:227], v56\n"
"v_add_u32 v56, v56, v59\n"
"v_mfma_f32_16x16x32_bf16 v[112:115], a[232:235], a[32:35], v[112:115]\n"
"ds_read_b128 a[228:231], v54 offset:12800\n"
"v_mfma_f32_16x16x32_bf16 v[116:119], a[236:239], a[32:35], v[116:119]\n"
"global_load_dwordx4 v[72:75], v4, s[94:95] offset:-4096 sc1\n"
"ds_read_b128 a[232:235], v54 offset:13824\n"
"v_mfma_f32_16x16x32_bf16 v[120:123], a[240:243], a[32:35], v[120:123]\n"
"ds_read_b128 a[236:239], v54 offset:14848\n"
"v_mfma_f32_16x16x32_bf16 v[124:127], a[244:247], a[32:35], v[124:127]\n"
"ds_read_b128 a[240:243], v54 offset:15872\n"
"v_mfma_f32_16x16x32_bf16 v[128:131], a[248:251], a[32:35], v[128:131]\n"
"s_waitcnt vmcnt(9) lgkmcnt(9)\n"
"ds_read_b128 a[244:247], v56\n"
"v_add_u32 v56, v56, v59\n"
"v_mfma_f32_16x16x32_bf16 v[112:115], a[252:255], a[36:39], v[112:115]\n"
"v_mfma_f32_16x16x32_bf16 v[116:119], a[72:75], a[36:39], v[116:119]\n"
"global_load_dwordx4 v[76:79], v4, s[94:95] offset:-3072 sc1\n"
"v_mfma_f32_16x16x32_bf16 v[120:123], a[76:79], a[36:39], v[120:123]\n"
"v_mfma_f32_16x16x32_bf16 v[124:127], a[80:83], a[36:39], v[124:127]\n"
"v_mfma_f32_16x16x32_bf16 v[128:131], a[84:87], a[36:39], v[128:131]\n"
"s_waitcnt vmcnt(9) lgkmcnt(15)\n"
"v_mfma_f32_16x16x32_bf16 v[112:115], a[88:91], a[40:43], v[112:115]\n"
"v_mfma_f32_16x16x32_bf16 v[116:119], a[92:95], a[40:43], v[116:119]\n"
"global_load_dwordx4 v[80:83], v4, s[94:95] offset:-2048 sc1\n"
"v_mfma_f32_16x16x32_bf16 v[120:123], a[96:99], a[40:43], v[120:123]\n"
"v_mfma_f32_16x16x32_bf16 v[124:127], a[100:103], a[40:43], v[124:127]\n"
"v_mfma_f32_16x16x32_bf16 v[128:131], a[104:107], a[40:43], v[128:131]\n"
"s_waitcnt vmcnt(9) lgkmcnt(15)\n"
"v_mfma_f32_16x16x32_bf16 v[112:115], a[108:111], a[44:47], v[112:115]\n"
"v_mfma_f32_16x16x32_bf16 v[116:119], a[112:115], a[44:47], v[116:119]\n"
"global_load_dwordx4 v[84:87], v4, s[94:95] offset:-1024 sc1\n"
"v_mfma_f32_16x16x32_bf16 v[120:123], a[116:119], a[44:47], v[120:123]\n"
"v_mfma_f32_16x16x32_bf16 v[124:127], a[120:123], a[44:47], v[124:127]\n"
"v_mfma_f32_16x16x32_bf16 v[128:131], a[124:127], a[44:47], v[128:131]\n"
"s_waitcnt vmcnt(9) lgkmcnt(15)\n"
"v_mfma_f32_16x16x32_bf16 v[112:115], a[128:131], a[48:51], v[112:115]\n"
"v_mfma_f32_16x16x32_bf16 v[116:119], a[132:135], a[48:51], v[116:119]\n"
"global_load_dwordx4 v[88:91], v4, s[94:95] offset:0 sc1\n"
"v_mfma_f32_16x16x32_bf16 v[120:123], a[136:139], a[48:51], v[120:123]\n"
"v_mfma_f32_16x16x32_bf16 v[124:127], a[140:143], a[48:51], v[124:127]\n"
"v_mfma_f32_16x16x32_bf16 v[128:131], a[144:147], a[48:51], v[128:131]\n"
"s_waitcnt vmcnt(9) lgkmcnt(15)\n"
"v_mfma_f32_16x16x32_bf16 v[112:115], a[148:151], a[52:55], v[112:115]\n"
"v_mfma_f32_16x16x32_bf16 v[116:119], a[152:155], a[52:55], v[116:119]\n"
"global_load_dwordx4 v[92:95], v4, s[94:95] offset:1024 sc1\n"
"v_mfma_f32_16x16x32_bf16 v[120:123], a[156:159], a[52:55], v[120:123]\n"
"v_mfma_f32_16x16x32_bf16 v[124:127], a[160:163], a[52:55], v[124:127]\n"
"v_mfma_f32_16x16x32_bf16 v[128:131], a[164:167], a[52:55], v[128:131]\n"
"s_waitcnt vmcnt(9) lgkmcnt(15)\n"
"v_mfma_f32_16x16x32_bf16 v[112:115], a[168:171], a[56:59], v[112:115]\n"
"v_mfma_f32_16x16x32_bf16 v[116:119], a[172:175], a[56:59], v[116:119]\n"
"global_load_dwordx4 v[96:99], v4, s[94:95] offset:2048 sc1\n"
"v_mfma_f32_16x16x32_bf16 v[120:123], a[176:179], a[56:59], v[120:123]\n"
"v_mfma_f32_16x16x32_bf16 v[124:127], a[180:183], a[56:59], v[124:127]\n"
"v_mfma_f32_16x16x32_bf16 v[128:131], a[184:187], a[56:59], v[128:131]\n"
"s_waitcnt vmcnt(9) lgkmcnt(10)\n"
"v_mfma_f32_16x16x32_bf16 v[112:115], a[188:191], a[60:63], v[112:115]\n"
"v_mfma_f32_16x16x32_bf16 v[116:119], a[192:195], a[60:63], v[116:119]\n"
"global_load_dwordx4 v[100:103], v4, s[94:95] offset:3072 sc1\n"
"v_mfma_f32_16x16x32_bf16 v[120:123], a[196:199], a[60:63], v[120:123]\n"
"v_mfma_f32_16x16x32_bf16 v[124:127], a[200:203], a[60:63], v[124:127]\n"
"v_mfma_f32_16x16x32_bf16 v[128:131], a[204:207], a[60:63], v[128:131]\n"
"s_waitcnt vmcnt(9) lgkmcnt(5)\n"
"v_mfma_f32_16x16x32_bf16 v[112:115], a[208:211], a[64:67], v[112:115]\n"
"v_mfma_f32_16x16x32_bf16 v[116:119], a[212:215], a[64:67], v[116:119]\n"
"global_load_dwordx4 v[104:107], v5, s[94:95] offset:-4096 sc1\n"
"v_mfma_f32_16x16x32_bf16 v[120:123], a[216:219], a[64:67], v[120:123]\n"
"v_mfma_f32_16x16x32_bf16 v[124:127], a[220:223], a[64:67], v[124:127]\n"
"v_mfma_f32_16x16x32_bf16 v[128:131], a[224:227], a[64:67], v[128:131]\n"
"s_waitcnt vmcnt(9) lgkmcnt(0)\n"
"v_mfma_f32_16x16x32_bf16 v[112:115], a[228:231], a[68:71], v[112:115]\n"
"v_mfma_f32_16x16x32_bf16 v[116:119], a[232:235], a[68:71], v[116:119]\n"
"global_load_dwordx4 v[108:111], v5, s[94:95] offset:-3072 sc1\n"
"v_mfma_f32_16x16x32_bf16 v[120:123], a[236:239], a[68:71], v[120:123]\n"
"v_mfma_f32_16x16x32_bf16 v[124:127], a[240:243], a[68:71], v[124:127]\n"
"v_mfma_f32_16x16x32_bf16 v[128:131], a[244:247], a[68:71], v[128:131]\n"
"s_barrier\n"
"s_lshl_b32 s92, s29, 11\n"
"s_add_u32 s92, s92, 13107200\n"
"s_lshl_b32 s93, s29, 8\n"
"s_add_u32 s93, s93, 13123584\n"
"v_cmp_gt_u32 vcc, 32, v0\n"
"s_nop 1\n"
"v_add_f32 v184, v128, v129\n"
"v_mov_b32 v185, v184\n"
"s_nop 1\n"
"v_permlane32_swap_b32 v185, v184\n"
"v_add_f32 v62, v62, v184\n"
"v_pk_add_f32 v[152:153], v[112:113], v[62:63] op_sel_hi:[1,0] neg_lo:[0,1] neg_hi:[0,1]\n"
"v_pk_add_f32 v[154:155], v[114:115], v[62:63] op_sel_hi:[1,0] neg_lo:[0,1] neg_hi:[0,1]\n"
"v_cvt_pk_bf16_f32 v172, v152, v153\n"
"v_cvt_pk_bf16_f32 v173, v154, v155\n"
"v_pk_add_f32 v[156:157], v[116:117], v[62:63] op_sel_hi:[1,0] neg_lo:[0,1] neg_hi:[0,1]\n"
"v_pk_add_f32 v[158:159], v[118:119], v[62:63] op_sel_hi:[1,0] neg_lo:[0,1] neg_hi:[0,1]\n"
"v_cvt_pk_bf16_f32 v174, v156, v157\n"
"v_cvt_pk_bf16_f32 v175, v158, v159\n"
"buffer_store_dwordx4 v[172:175], v42, s[8:11], s92 offen sc1\n"
"v_pk_add_f32 v[160:161], v[120:121], v[62:63] op_sel_hi:[1,0] neg_lo:[0,1] neg_hi:[0,1]\n"
"v_pk_add_f32 v[162:163], v[122:123], v[62:63] op_sel_hi:[1,0] neg_lo:[0,1] neg_hi:[0,1]\n"
"v_cvt_pk_bf16_f32 v176, v160, v161\n"
"v_cvt_pk_bf16_f32 v177, v162, v163\n"
"v_pk_add_f32 v[164:165], v[124:125], v[62:63] op_sel_hi:[1,0] neg_lo:[0,1] neg_hi:[0,1]\n"
"v_pk_add_f32 v[166:167], v[126:127], v[62:63] op_sel_hi:[1,0] neg_lo:[0,1] neg_hi:[0,1]\n"
"v_cvt_pk_bf16_f32 v178, v164, v165\n"
"v_cvt_pk_bf16_f32 v179, v166, v167\n"
"buffer_store_dwordx4 v[176:179], v42, s[8:11], s92 offen offset:1024 sc1\n"
"v_pk_add_f32 v[168:169], v[128:129], v[62:63] op_sel_hi:[1,0] neg_lo:[0,1] neg_hi:[0,1]\n"
"v_pk_add_f32 v[170:171], v[130:131], v[62:63] op_sel_hi:[1,0] neg_lo:[0,1] neg_hi:[0,1]\n"
"v_cvt_pk_bf16_f32 v180, v168, v169\n"
"v_cvt_pk_bf16_f32 v181, v170, v171\n"
"v_cndmask_b32 v168, 0, v168, vcc\n"
"v_cndmask_b32 v169, 0, v169, vcc\n"
"v_permlane16_swap_b32 v180, v182\n"
"v_permlane16_swap_b32 v181, v183\n"
"buffer_store_dwordx4 v[180:183], v43, s[8:11], s93 offen sc1\n"
"v_cndmask_b32 v170, 0, v170, vcc\n"
"v_cndmask_b32 v171, 0, v171, vcc\n"
"v_pk_mul_f32 v[184:185], v[152:153], v[152:153]\n"
"v_pk_fma_f32 v[184:185], v[154:155], v[154:155], v[184:185]\n"
"v_pk_fma_f32 v[184:185], v[156:157], v[156:157], v[184:185]\n"
"v_pk_fma_f32 v[184:185], v[158:159], v[158:159], v[184:185]\n"
"v_pk_fma_f32 v[184:185], v[160:161], v[160:161], v[184:185]\n"
"v_pk_fma_f32 v[184:185], v[162:163], v[162:163], v[184:185]\n"
"v_pk_fma_f32 v[184:185], v[164:165], v[164:165], v[184:185]\n"
"v_pk_fma_f32 v[184:185], v[166:167], v[166:167], v[184:185]\n"
"v_pk_fma_f32 v[184:185], v[168:169], v[168:169], v[184:185]\n"
"v_pk_fma_f32 v[184:185], v[170:171], v[170:171], v[184:185]\n"
"v_add_f32 v63, v184, v185\n"
"buffer_store_dword v63, v44, s[8:11], s93 offen offset:2048 sc1\n"
"v_pk_add_f32 v[148:149], v[148:149], v[128:129]\n"
"v_pk_add_f32 v[150:151], v[150:151], v[130:131]\n"
"v_pk_add_f32 v[132:133], v[132:133], v[112:113]\n"
"v_pk_add_f32 v[134:135], v[134:135], v[114:115]\n"
"v_pk_add_f32 v[136:137], v[136:137], v[116:117]\n"
"v_pk_add_f32 v[138:139], v[138:139], v[118:119]\n"
"v_pk_add_f32 v[140:141], v[140:141], v[120:121]\n"
"v_pk_add_f32 v[142:143], v[142:143], v[122:123]\n"
"v_pk_add_f32 v[144:145], v[144:145], v[124:125]\n"
"v_pk_add_f32 v[146:147], v[146:147], v[126:127]\n"
"v_cvt_pk_bf16_f32 v192, v148, v149\n"
"v_cvt_pk_bf16_f32 v193, v150, v151\n"
"v_cvt_pk_bf16_f32 v184, v132, v133\n"
"v_cvt_pk_bf16_f32 v185, v134, v135\n"
"v_cvt_pk_bf16_f32 v186, v136, v137\n"
"v_cvt_pk_bf16_f32 v187, v138, v139\n"
"v_cvt_pk_bf16_f32 v188, v140, v141\n"
"v_cvt_pk_bf16_f32 v189, v142, v143\n"
"v_cvt_pk_bf16_f32 v190, v144, v145\n"
"v_cvt_pk_bf16_f32 v191, v146, v147\n"
"v_permlane16_swap_b32 v192, v194\n"
"v_permlane16_swap_b32 v193, v195\n"
"s_add_u32 s93, s93, 2621440\n"
"buffer_store_dwordx4 v[192:195], v43, s[8:11], s93 offen sc1\n"
"s_lshl_b32 s72, s26, 11\n"
"s_add_u32 s72, s72, 155648\n"
"v_add_u32 v68, s72, v1\n"
"ds_write_b128 v68, v[184:187]\n"
"ds_write_b128 v68, v[188:191] offset:1024\n"
"global_load_dwordx4 v[112:115], v5, s[94:95] offset:-2048 sc1\n"
"global_load_dwordx4 v[116:119], v5, s[94:95] offset:-1024 sc1\n"
"global_load_dwordx4 v[120:123], v5, s[94:95] offset:0 sc1\n"
"global_load_dwordx4 v[124:127], v5, s[94:95] offset:1024 sc1\n"
"global_load_dwordx4 v[128:131], v5, s[94:95] offset:2048 sc1\n"
"s_waitcnt vmcnt(5) lgkmcnt(0)\n"
"s_barrier\n"
"s_cmp_eq_u32 s26, 0\n"
"s_cbranch_scc0 .Lfused_block_kernel_s65\n"
"buffer_store_dwordx2 v[22:23], v47, s[8:11], s30 offen offset:640 sc1\n"
".Lfused_block_kernel_s65:\n"
"global_load_dwordx4 v[132:135], v5, s[94:95] offset:3072 sc1\n"
"global_load_dwordx4 v[136:139], v6, s[94:95] offset:-4096 sc1\n"
"global_load_dwordx4 v[140:143], v6, s[94:95] offset:-3072 sc1\n"
"s_add_u32 s94, s94, 18432\n"
"s_addc_u32 s95, s95, 0\n"
"global_load_dwordx4 v[144:147], v4, s[94:95] offset:-4096 sc1\n"
"global_load_dwordx4 v[148:151], v4, s[94:95] offset:-3072 sc1\n"
"global_load_dwordx4 v[152:155], v4, s[94:95] offset:-2048 sc1\n"
"global_load_dwordx4 v[156:159], v4, s[94:95] offset:-1024 sc1\n"
"global_load_dwordx4 v[160:163], v4, s[94:95] offset:0 sc1\n"
"global_load_dwordx4 v[164:167], v4, s[94:95] offset:1024 sc1\n"
"global_load_dwordx4 v[168:171], v4, s[94:95] offset:2048 sc1\n"
"global_load_dwordx4 v[172:175], v4, s[94:95] offset:3072 sc1\n"
"global_load_dwordx4 v[176:179], v5, s[94:95] offset:-4096 sc1\n"
"global_load_dwordx4 v[180:183], v5, s[94:95] offset:-3072 sc1\n"
"global_load_dwordx4 v[184:187], v5, s[94:95] offset:-2048 sc1\n"
"global_load_dwordx4 v[188:191], v5, s[94:95] offset:-1024 sc1\n"
"global_load_dwordx4 v[192:195], v5, s[94:95] offset:0 sc1\n"
"global_load_dwordx4 v[196:199], v5, s[94:95] offset:1024 sc1\n"
"global_load_dwordx4 v[200:203], v5, s[94:95] offset:2048 sc1\n"
"global_load_dwordx4 v[204:207], v5, s[94:95] offset:3072 sc1\n"
"global_load_dwordx4 v[208:211], v6, s[94:95] offset:-4096 sc1\n"
"global_load_dwordx4 v[212:215], v6, s[94:95] offset:-3072 sc1\n"
"s_add_u32 s94, s94, 18432\n"
"s_addc_u32 s95, s95, 0\n"
"s_add_u32 s90, s71, 13107200\n"
"s_mul_i32 s91, s26, 20480\n"
"s_mov_b32 s86, 8192\n"
"v_add_u32 v13, 18432, v65\n"
"v_mov_b32 v220, v52\n"
"v_mov_b32 v221, v49\n"
"s_cmp_eq_u32 s26, 0\n"
"s_cbranch_scc0 .Lfused_block_kernel_s66\n"
"s_mov_b32 s87, 0x4000\n"
".Lfused_block_kernel_poll67:\n"
"buffer_load_dwordx2 v[24:25], v49, s[8:11], s31 offen offset:640 sc1\n"
"buffer_load_dwordx4 v[26:29], v66, s[8:11], s86 offen offset:0 sc1\n"
"s_waitcnt vmcnt(0)\n"
"v_cmp_ne_u64 vcc, s[24:25], v[24:25]\n"
"v_cmp_ne_u64 s[84:85], s[24:25], v[26:27]\n"
"s_nop 0\n"
"s_or_b64 vcc, vcc, s[84:85]\n"
"v_cmp_ne_u64 s[84:85], s[24:25], v[28:29]\n"
"s_nop 0\n"
"s_or_b64 vcc, vcc, s[84:85]\n"
"s_cmp_eq_u64 vcc, 0\n"
"s_cbranch_scc1 .Lfused_block_kernel_pok68\n"
"s_sub_u32 s87, s87, 1\n"
"s_cmp_lg_u32 s87, 0\n"
"s_cbranch_scc1 .Lfused_block_kernel_poll67\n"
".Lfused_block_kernel_pok68:\n"
".Lfused_block_kernel_s66:\n"
"s_barrier\n"
"s_add_u32 m0, s91, 0\n"
"s_nop 0\n"
"buffer_load_dwordx4 v1, s[8:11], s90 offen offset:0 sc1 lds\n"
"s_lshr_b32 s72, s26, 1\n"
"buffer_load_dwordx4 v1, s[8:11], s90 offen offset:1024 sc1 lds\n"
"s_lshl_b32 s72, s72, 12\n"
"buffer_load_dwordx4 v1, s[8:11], s90 offen offset:2048 sc1 lds\n"
"s_and_b32 s73, s26, 1\n"
"buffer_load_dwordx4 v1, s[8:11], s90 offen offset:3072 sc1 lds\n"
"s_lshl_b32 s73, s73, 3\n"
"s_add_u32 s90, s90, 4096\n"
"s_add_u32 m0, s91, 4096\n"
"s_nop 0\n"
"buffer_load_dwordx4 v1, s[8:11], s90 offen offset:0 sc1 lds\n"
"s_add_u32 s72, s72, s73\n"
"buffer_load_dwordx4 v1, s[8:11], s90 offen offset:1024 sc1 lds\n"
"s_add_u32 s72, s72, 81920\n"
"buffer_load_dwordx4 v1, s[8:11], s90 offen offset:2048 sc1 lds\n"
"v_add_u32 v7, s72, v1\n"
"buffer_load_dwordx4 v1, s[8:11], s90 offen offset:3072 sc1 lds\n"
"v_mov_b32 v10, v1\n"
"s_add_u32 s90, s90, 4096\n"
"s_add_u32 m0, s91, 8192\n"
"s_nop 0\n"
"buffer_load_dwordx4 v1, s[8:11], s90 offen offset:0 sc1 lds\n"
"v_add_u32 v11, 0x10000, v1\n"
"buffer_load_dwordx4 v1, s[8:11], s90 offen offset:1024 sc1 lds\n"
"v_add_u32 v12, 0x20000, v1\n"
"buffer_load_dwordx4 v1, s[8:11], s90 offen offset:2048 sc1 lds\n"
"buffer_load_dwordx4 v1, s[8:11], s90 offen offset:3072 sc1 lds\n"
"s_add_u32 s90, s90, 4096\n"
"s_add_u32 m0, s91, 12288\n"
"s_nop 0\n"
"buffer_load_dwordx4 v1, s[8:11], s90 offen offset:0 sc1 lds\n"
"buffer_load_dwordx4 v1, s[8:11], s90 offen offset:1024 sc1 lds\n"
"buffer_load_dwordx4 v1, s[8:11], s90 offen offset:2048 sc1 lds\n"
"buffer_load_dwordx4 v1, s[8:11], s90 offen offset:3072 sc1 lds\n"
"s_add_u32 s90, s90, 4096\n"
"s_add_u32 m0, s91, 16384\n"
"s_nop 0\n"
"buffer_load_dwordx4 v1, s[8:11], s90 offen offset:0 sc1 lds\n"
"buffer_load_dwordx4 v1, s[8:11], s90 offen offset:1024 sc1 lds\n"
"buffer_load_dwordx4 v1, s[8:11], s90 offen offset:2048 sc1 lds\n"
"buffer_load_dwordx4 v1, s[8:11], s90 offen offset:3072 sc1 lds\n"
"s_waitcnt vmcnt(10)\n"
"s_barrier\n"
"ds_read_b128 a[0:3], v10 offset:0\n"
"ds_read_b128 a[4:7], v10 offset:20480\n"
"ds_read_b128 a[8:11], v10 offset:40960\n"
"ds_read_b128 a[12:15], v10 offset:61440\n"
"ds_read_b128 a[16:19], v10 offset:1024\n"
"ds_read_b128 a[20:23], v10 offset:21504\n"
"ds_read_b128 a[24:27], v10 offset:41984\n"
"ds_read_b128 a[28:31], v10 offset:62464\n"
"s_waitcnt lgkmcnt(4)\n"
"v_mfma_f32_16x16x32_bf16 v[40:43], v[72:75], a[0:3], 0\n"
"ds_read_b128 a[32:35], v10 offset:2048\n"
"v_mfma_f32_16x16x32_bf16 v[44:47], v[72:75], a[4:7], 0\n"
"ds_read_b128 a[36:39], v10 offset:22528\n"
"v_mfma_f32_16x16x32_bf16 v[48:51], v[72:75], a[8:11], 0\n"
"ds_read_b128 a[40:43], v10 offset:43008\n"
"v_mfma_f32_16x16x32_bf16 v[52:55], v[72:75], a[12:15], 0\n"
"s_waitcnt lgkmcnt(3)\n"
"ds_read_b128 a[44:47], v10 offset:63488\n"
"global_load_dwordx4 v[72:75], v4, s[94:95] offset:-4096 sc1\n"
"v_mfma_f32_16x16x32_bf16 v[40:43], v[76:79], a[16:19], v[40:43]\n"
"ds_read_b128 a[48:51], v10 offset:3072\n"
"v_mfma_f32_16x16x32_bf16 v[44:47], v[76:79], a[20:23], v[44:47]\n"
"ds_read_b128 a[52:55], v10 offset:23552\n"
"v_mfma_f32_16x16x32_bf16 v[48:51], v[76:79], a[24:27], v[48:51]\n"
"ds_read_b128 a[56:59], v10 offset:44032\n"
"v_mfma_f32_16x16x32_bf16 v[52:55], v[76:79], a[28:31], v[52:55]\n"
"s_waitcnt lgkmcnt(3)\n"
"ds_read_b128 a[60:63], v10 offset:64512\n"
"global_load_dwordx4 v[76:79], v4, s[94:95] offset:-3072 sc1\n"
"v_mfma_f32_16x16x32_bf16 v[40:43], v[80:83], a[32:35], v[40:43]\n"
"ds_read_b128 a[64:67], v10 offset:4096\n"
"v_mfma_f32_16x16x32_bf16 v[44:47], v[80:83], a[36:39], v[44:47]\n"
"ds_read_b128 a[68:71], v10 offset:24576\n"
"v_mfma_f32_16x16x32_bf16 v[48:51], v[80:83], a[40:43], v[48:51]\n"
"ds_read_b128 a[72:75], v10 offset:45056\n"
"v_mfma_f32_16x16x32_bf16 v[52:55], v[80:83], a[44:47], v[52:55]\n"
"s_waitcnt lgkmcnt(3)\n"
"ds_read_b128 a[76:79], v11 offset:0\n"
"global_load_dwordx4 v[80:83], v4, s[94:95] offset:-2048 sc1\n"
"v_mfma_f32_16x16x32_bf16 v[40:43], v[84:87], a[48:51], v[40:43]\n"
"ds_read_b128 a[80:83], v10 offset:5120\n"
"v_mfma_f32_16x16x32_bf16 v[44:47], v[84:87], a[52:55], v[44:47]\n"
"ds_read_b128 a[84:87], v10 offset:25600\n"
"v_mfma_f32_16x16x32_bf16 v[48:51], v[84:87], a[56:59], v[48:51]\n"
"ds_read_b128 a[88:91], v10 offset:46080\n"
"v_mfma_f32_16x16x32_bf16 v[52:55], v[84:87], a[60:63], v[52:55]\n"
"s_waitcnt lgkmcnt(3)\n"
"ds_read_b128 a[92:95], v11 offset:1024\n"
"global_load_dwordx4 v[84:87], v4, s[94:95] offset:-1024 sc1\n"
"v_mfma_f32_16x16x32_bf16 v[40:43], v[88:91], a[64:67], v[40:43]\n"
"ds_read_b128 a[96:99], v10 offset:6144\n"
"v_mfma_f32_16x16x32_bf16 v[44:47], v[88:91], a[68:71], v[44:47]\n"
"ds_read_b128 a[100:103], v10 offset:26624\n"
"v_mfma_f32_16x16x32_bf16 v[48:51], v[88:91], a[72:75], v[48:51]\n"
"ds_read_b128 a[104:107], v10 offset:47104\n"
"v_mfma_f32_16x16x32_bf16 v[52:55], v[88:91], a[76:79], v[52:55]\n"
"s_waitcnt lgkmcnt(3)\n"
"ds_read_b128 a[108:111], v11 offset:2048\n"
"global_load_dwordx4 v[88:91], v4, s[94:95] offset:0 sc1\n"
"v_mfma_f32_16x16x32_bf16 v[40:43], v[92:95], a[80:83], v[40:43]\n"
"ds_read_b128 a[112:115], v10 offset:7168\n"
"v_mfma_f32_16x16x32_bf16 v[44:47], v[92:95], a[84:87], v[44:47]\n"
"ds_read_b128 a[116:119], v10 offset:27648\n"
"v_mfma_f32_16x16x32_bf16 v[48:51], v[92:95], a[88:91], v[48:51]\n"
"ds_read_b128 a[120:123], v10 offset:48128\n"
"v_mfma_f32_16x16x32_bf16 v[52:55], v[92:95], a[92:95], v[52:55]\n"
"s_waitcnt lgkmcnt(3)\n"
"ds_read_b128 a[124:127], v11 offset:3072\n"
"global_load_dwordx4 v[92:95], v4, s[94:95] offset:1024 sc1\n"
"v_mfma_f32_16x16x32_bf16 v[40:43], v[96:99], a[96:99], v[40:43]\n"
"ds_read_b128 a[128:131], v10 offset:8192\n"
"v_mfma_f32_16x16x32_bf16 v[44:47], v[96:99], a[100:103], v[44:47]\n"
"ds_read_b128 a[132:135], v10 offset:28672\n"
"v_mfma_f32_16x16x32_bf16 v[48:51], v[96:99], a[104:107], v[48:51]\n"
"ds_read_b128 a[136:139], v10 offset:49152\n"
"v_mfma_f32_16x16x32_bf16 v[52:55], v[96:99], a[108:111], v[52:55]\n"
"s_waitcnt lgkmcnt(3)\n"
"ds_read_b128 a[140:143], v11 offset:4096\n"
"global_load_dwordx4 v[96:99], v4, s[94:95] offset:2048 sc1\n"
"v_mfma_f32_16x16x32_bf16 v[40:43], v[100:103], a[112:115], v[40:43]\n"
"ds_read_b128 a[144:147], v10 offset:9216\n"
"v_mfma_f32_16x16x32_bf16 v[44:47], v[100:103], a[116:119], v[44:47]\n"
"ds_read_b128 a[148:151], v10 offset:29696\n"
"v_mfma_f32_16x16x32_bf16 v[48:51], v[100:103], a[120:123], v[48:51]\n"
"ds_read_b128 a[152:155], v10 offset:50176\n"
"v_mfma_f32_16x16x32_bf16 v[52:55], v[100:103], a[124:127], v[52:55]\n"
"s_waitcnt lgkmcnt(3)\n"
"s_waitcnt vmcnt(7)\n"
"s_barrier\n"
"ds_read_b128 v[56:59], v13 offset:0\n"
"ds_read_b128 v[60:63], v13 offset:256\n"
"ds_read_b128 v[64:67], v13 offset:20480\n"
"ds_read_b128 v[68:71], v13 offset:20736\n"
"ds_read_b128 v[24:27], v13 offset:40960\n"
"ds_read_b128 v[28:31], v13 offset:41216\n"
"ds_read_b128 v[32:35], v13 offset:61440\n"
"ds_read_b128 v[36:39], v13 offset:61696\n"
"ds_read_b128 a[156:159], v11 offset:5120\n"
"global_load_dwordx4 v[100:103], v4, s[94:95] offset:3072 sc1\n"
"v_mfma_f32_16x16x32_bf16 v[40:43], v[104:107], a[128:131], v[40:43]\n"
"ds_read_b128 a[160:163], v10 offset:10240\n"
"v_mfma_f32_16x16x32_bf16 v[44:47], v[104:107], a[132:135], v[44:47]\n"
"ds_read_b128 a[164:167], v10 offset:30720\n"
"v_mfma_f32_16x16x32_bf16 v[48:51], v[104:107], a[136:139], v[48:51]\n"
"ds_read_b128 a[168:171], v10 offset:51200\n"
"v_mfma_f32_16x16x32_bf16 v[52:55], v[104:107], a[140:143], v[52:55]\n"
"s_waitcnt lgkmcnt(3)\n"
"ds_read_b128 a[172:175], v11 offset:6144\n"
"global_load_dwordx4 v[104:107], v5, s[94:95] offset:-4096 sc1\n"
"v_mfma_f32_16x16x32_bf16 v[40:43], v[108:111], a[144:147], v[40:43]\n"
"ds_read_b128 a[176:179], v10 offset:11264\n"
"v_mfma_f32_16x16x32_bf16 v[44:47], v[108:111], a[148:151], v[44:47]\n"
"ds_read_b128 a[180:183], v10 offset:31744\n"
"v_mfma_f32_16x16x32_bf16 v[48:51], v[108:111], a[152:155], v[48:51]\n"
"ds_read_b128 a[184:187], v10 offset:52224\n"
"v_mfma_f32_16x16x32_bf16 v[52:55], v[108:111], a[156:159], v[52:55]\n"
"s_waitcnt lgkmcnt(3)\n"
"ds_read_b128 a[188:191], v11 offset:7168\n"
"global_load_dwordx4 v[108:111], v5, s[94:95] offset:-3072 sc1\n"
"v_mfma_f32_16x16x32_bf16 v[40:43], v[112:115], a[160:163], v[40:43]\n"
"ds_read_b128 a[192:195], v10 offset:12288\n"
"v_add_f32 v56, v56, v60\n"
"v_add_f32 v64, v64, v68\n"
"v_add_f32 v24, v24, v28\n"
"v_mfma_f32_16x16x32_bf16 v[44:47], v[112:115], a[164:167], v[44:47]\n"
"ds_read_b128 a[196:199], v10 offset:32768\n"
"v_add_f32 v32, v32, v36\n"
"v_add_f32 v57, v57, v61\n"
"v_add_f32 v65, v65, v69\n"
"v_mfma_f32_16x16x32_bf16 v[48:51], v[112:115], a[168:171], v[48:51]\n"
"ds_read_b128 a[200:203], v10 offset:53248\n"
"v_add_f32 v25, v25, v29\n"
"v_add_f32 v33, v33, v37\n"
"v_add_f32 v58, v58, v62\n"
"v_mfma_f32_16x16x32_bf16 v[52:55], v[112:115], a[172:175], v[52:55]\n"
"s_waitcnt lgkmcnt(3)\n"
"ds_read_b128 a[204:207], v11 offset:8192\n"
"global_load_dwordx4 v[112:115], v5, s[94:95] offset:-2048 sc1\n"
"v_add_f32 v66, v66, v70\n"
"v_add_f32 v26, v26, v30\n"
"v_mfma_f32_16x16x32_bf16 v[40:43], v[116:119], a[176:179], v[40:43]\n"
"ds_read_b128 a[208:211], v10 offset:13312\n"
"v_add_f32 v34, v34, v38\n"
"v_add_f32 v59, v59, v63\n"
"v_mfma_f32_16x16x32_bf16 v[44:47], v[116:119], a[180:183], v[44:47]\n"
"ds_read_b128 a[212:215], v10 offset:33792\n"
"v_add_f32 v67, v67, v71\n"
"v_add_f32 v27, v27, v31\n"
"v_add_f32 v35, v35, v39\n"
"v_mfma_f32_16x16x32_bf16 v[48:51], v[116:119], a[184:187], v[48:51]\n"
"ds_read_b128 a[216:219], v10 offset:54272\n"
"v_add_f32 v56, v56, v57\n"
"v_add_f32 v64, v64, v65\n"
"v_add_f32 v24, v24, v25\n"
"v_mfma_f32_16x16x32_bf16 v[52:55], v[116:119], a[188:191], v[52:55]\n"
"s_waitcnt lgkmcnt(3)\n"
"ds_read_b128 a[220:223], v11 offset:9216\n"
"global_load_dwordx4 v[116:119], v5, s[94:95] offset:-1024 sc1\n"
"v_add_f32 v32, v32, v33\n"
"v_add_f32 v58, v58, v59\n"
"v_mfma_f32_16x16x32_bf16 v[40:43], v[120:123], a[192:195], v[40:43]\n"
"ds_read_b128 a[224:227], v10 offset:14336\n"
"v_add_f32 v66, v66, v67\n"
"v_add_f32 v26, v26, v27\n"
"v_add_f32 v34, v34, v35\n"
"v_mfma_f32_16x16x32_bf16 v[44:47], v[120:123], a[196:199], v[44:47]\n"
"ds_read_b128 a[228:231], v10 offset:34816\n"
"v_add_f32 v56, v56, v58\n"
"v_add_f32 v64, v64, v66\n"
"v_add_f32 v24, v24, v26\n"
"v_mfma_f32_16x16x32_bf16 v[48:51], v[120:123], a[200:203], v[48:51]\n"
"ds_read_b128 a[232:235], v10 offset:55296\n"
"v_add_f32 v32, v32, v34\n"
"v_mov_b32 v57, v56\n"
"v_mov_b32 v65, v64\n"
"v_mfma_f32_16x16x32_bf16 v[52:55], v[120:123], a[204:207], v[52:55]\n"
"s_waitcnt lgkmcnt(3)\n"
"ds_read_b128 a[236:239], v11 offset:10240\n"
"global_load_dwordx4 v[120:123], v5, s[94:95] offset:0 sc1\n"
"v_mov_b32 v25, v24\n"
"v_mov_b32 v33, v32\n"
"v_mfma_f32_16x16x32_bf16 v[40:43], v[124:127], a[208:211], v[40:43]\n"
"ds_read_b128 a[240:243], v10 offset:15360\n"
"v_permlane32_swap_b32 v57, v56\n"
"v_mfma_f32_16x16x32_bf16 v[44:47], v[124:127], a[212:215], v[44:47]\n"
"ds_read_b128 a[244:247], v10 offset:35840\n"
"v_permlane32_swap_b32 v65, v64\n"
"v_mfma_f32_16x16x32_bf16 v[48:51], v[124:127], a[216:219], v[48:51]\n"
"ds_read_b128 a[248:251], v10 offset:56320\n"
"v_permlane32_swap_b32 v25, v24\n"
"v_permlane32_swap_b32 v33, v32\n"
"v_mfma_f32_16x16x32_bf16 v[52:55], v[124:127], a[220:223], v[52:55]\n"
"s_waitcnt lgkmcnt(3)\n"
"ds_read_b128 a[252:255], v11 offset:11264\n"
"global_load_dwordx4 v[124:127], v5, s[94:95] offset:1024 sc1\n"
"v_add_f32 v56, v56, v57\n"
"v_add_f32 v64, v64, v65\n"
"v_mfma_f32_16x16x32_bf16 v[40:43], v[128:131], a[224:227], v[40:43]\n"
"ds_read_b128 v[224:227], v10 offset:16384\n"
"v_add_f32 v24, v24, v25\n"
"v_add_f32 v32, v32, v33\n"
"v_mov_b32 v57, v56\n"
"v_mfma_f32_16x16x32_bf16 v[44:47], v[128:131], a[228:231], v[44:47]\n"
"ds_read_b128 v[228:231], v10 offset:36864\n"
"v_mov_b32 v65, v64\n"
"v_mov_b32 v25, v24\n"
"v_mov_b32 v33, v32\n"
"v_mfma_f32_16x16x32_bf16 v[48:51], v[128:131], a[232:235], v[48:51]\n"
"ds_read_b128 v[232:235], v10 offset:57344\n"
"v_permlane16_swap_b32 v57, v56\n"
"v_mfma_f32_16x16x32_bf16 v[52:55], v[128:131], a[236:239], v[52:55]\n"
"s_waitcnt lgkmcnt(3)\n"
"ds_read_b128 v[236:239], v11 offset:12288\n"
"global_load_dwordx4 v[128:131], v5, s[94:95] offset:2048 sc1\n"
"v_permlane16_swap_b32 v65, v64\n"
"v_mfma_f32_16x16x32_bf16 v[40:43], v[132:135], a[240:243], v[40:43]\n"
"ds_read_b128 v[240:243], v10 offset:17408\n"
"v_permlane16_swap_b32 v25, v24\n"
"v_mfma_f32_16x16x32_bf16 v[44:47], v[132:135], a[244:247], v[44:47]\n"
"ds_read_b128 v[244:247], v10 offset:37888\n"
"v_permlane16_swap_b32 v33, v32\n"
"v_add_f32 v56, v56, v57\n"
"v_add_f32 v64, v64, v65\n"
"v_mfma_f32_16x16x32_bf16 v[48:51], v[132:135], a[248:251], v[48:51]\n"
"ds_read_b128 v[248:251], v10 offset:58368\n"
"v_add_f32 v24, v24, v25\n"
"v_add_f32 v32, v32, v33\n"
"v_mul_f32 v56, 0x3ae38e39, v56\n"
"v_mfma_f32_16x16x32_bf16 v[52:55], v[132:135], a[252:255], v[52:55]\n"
"s_waitcnt lgkmcnt(3)\n"
"ds_read_b128 v[252:255], v11 offset:13312\n"
"global_load_dwordx4 v[132:135], v5, s[94:95] offset:3072 sc1\n"
"v_mul_f32 v64, 0x3ae38e39, v64\n"
"v_mul_f32 v24, 0x3ae38e39, v24\n"
"v_mfma_f32_16x16x32_bf16 v[40:43], v[136:139], v[224:227], v[40:43]\n"
"v_mul_f32 v32, 0x3ae38e39, v32\n"
"v_add_f32 v56, 0x3727c5ac, v56\n"
"v_add_f32 v64, 0x3727c5ac, v64\n"
"v_add_f32 v24, 0x3727c5ac, v24\n"
"v_mfma_f32_16x16x32_bf16 v[44:47], v[136:139], v[228:231], v[44:47]\n"
"v_add_f32 v32, 0x3727c5ac, v32\n"
"v_rsq_f32 v216, v56\n"
"v_mfma_f32_16x16x32_bf16 v[48:51], v[136:139], v[232:235], v[48:51]\n"
"v_rsq_f32 v217, v64\n"
"v_rsq_f32 v218, v24\n"
"v_mfma_f32_16x16x32_bf16 v[52:55], v[136:139], v[236:239], v[52:55]\n"
"s_waitcnt lgkmcnt(0)\n"
"global_load_dwordx4 v[136:139], v6, s[94:95] offset:-4096 sc1\n"
"v_rsq_f32 v219, v32\n"
"v_mul_f32 v14, 0xc0135761, v216\n"
"v_mul_f32 v15, 0xc0135761, v217\n"
"v_mfma_f32_16x16x32_bf16 v[40:43], v[140:143], v[240:243], v[40:43]\n"
"v_mul_f32 v16, 0xc0135761, v218\n"
"v_mul_f32 v17, 0xc0135761, v219\n"
"v_mul_f32 v56, v216, v216\n"
"v_mfma_f32_16x16x32_bf16 v[44:47], v[140:143], v[244:247], v[44:47]\n"
"v_mul_f32 v64, v217, v217\n"
"v_mul_f32 v24, v218, v218\n"
"v_mul_f32 v32, v219, v219\n"
"v_mul_f32 v56, 0x3d372713, v56\n"
"v_mfma_f32_16x16x32_bf16 v[48:51], v[140:143], v[248:251], v[48:51]\n"
"v_mul_f32 v64, 0x3d372713, v64\n"
"v_mul_f32 v24, 0x3d372713, v24\n"
"v_mul_f32 v32, 0x3d372713, v32\n"
"v_mul_f32 v18, v56, v14\n"
"v_mfma_f32_16x16x32_bf16 v[52:55], v[140:143], v[252:255], v[52:55]\n"
"global_load_dwordx4 v[140:143], v6, s[94:95] offset:-3072 sc1\n"
"v_mul_f32 v19, v64, v15\n"
"v_mul_f32 v20, v24, v16\n"
"v_mul_f32 v21, v32, v17\n"
"s_add_u32 s94, s94, 18432\n"
"s_addc_u32 s95, s95, 0\n"
"s_mov_b32 s86, 4\n"
".p2align 3\n"
".Lfused_block_kernel_g1:\n"
"s_waitcnt vmcnt(34)\n"
"v_mfma_f32_16x16x32_bf16 v[56:59], v[144:147], a[0:3], 0\n"
"v_mfma_f32_16x16x32_bf16 v[60:63], v[144:147], a[4:7], 0\n"
"v_mfma_f32_16x16x32_bf16 v[64:67], v[144:147], a[8:11], 0\n"
"v_mfma_f32_16x16x32_bf16 v[68:71], v[144:147], a[12:15], 0\n"
"global_load_dwordx4 v[144:147], v4, s[94:95] offset:-4096 sc1\n"
"v_mul_f32 v24, v40, v40\n"
"v_mul_f32 v25, v41, v41\n"
"v_mfma_f32_16x16x32_bf16 v[56:59], v[148:151], a[16:19], v[56:59]\n"
"v_mul_f32 v26, v42, v42\n"
"v_mul_f32 v27, v43, v43\n"
"v_mfma_f32_16x16x32_bf16 v[60:63], v[148:151], a[20:23], v[60:63]\n"
"v_fma_f32 v24, v24, v18, v14\n"
"v_fma_f32 v25, v25, v18, v14\n"
"v_fma_f32 v26, v26, v18, v14\n"
"v_mfma_f32_16x16x32_bf16 v[64:67], v[148:151], a[24:27], v[64:67]\n"
"s_waitcnt vmcnt(33)\n"
"v_fma_f32 v27, v27, v18, v14\n"
"v_mul_f32 v24, v24, v40\n"
"v_mul_f32 v25, v25, v41\n"
"v_mfma_f32_16x16x32_bf16 v[68:71], v[148:151], a[28:31], v[68:71]\n"
"global_load_dwordx4 v[148:151], v4, s[94:95] offset:-3072 sc1\n"
"v_mul_f32 v26, v26, v42\n"
"v_mfma_f32_16x16x32_bf16 v[56:59], v[152:155], a[32:35], v[56:59]\n"
"v_mul_f32 v27, v27, v43\n"
"v_exp_f32 v24, v24\n"
"v_mfma_f32_16x16x32_bf16 v[60:63], v[152:155], a[36:39], v[60:63]\n"
"v_exp_f32 v25, v25\n"
"v_mfma_f32_16x16x32_bf16 v[64:67], v[152:155], a[40:43], v[64:67]\n"
"v_exp_f32 v26, v26\n"
"v_mfma_f32_16x16x32_bf16 v[68:71], v[152:155], a[44:47], v[68:71]\n"
"global_load_dwordx4 v[152:155], v4, s[94:95] offset:-2048 sc1\n"
"v_exp_f32 v27, v27\n"
"v_add_f32 v24, 1.0, v24\n"
"v_mfma_f32_16x16x32_bf16 v[56:59], v[156:159], a[48:51], v[56:59]\n"
"v_add_f32 v25, 1.0, v25\n"
"v_add_f32 v26, 1.0, v26\n"
"v_add_f32 v27, 1.0, v27\n"
"v_mfma_f32_16x16x32_bf16 v[60:63], v[156:159], a[52:55], v[60:63]\n"
"v_rcp_f32 v24, v24\n"
"v_mfma_f32_16x16x32_bf16 v[64:67], v[156:159], a[56:59], v[64:67]\n"
"s_waitcnt vmcnt(33)\n"
"v_rcp_f32 v25, v25\n"
"v_mfma_f32_16x16x32_bf16 v[68:71], v[156:159], a[60:63], v[68:71]\n"
"global_load_dwordx4 v[156:159], v4, s[94:95] offset:-1024 sc1\n"
"v_rcp_f32 v26, v26\n"
"v_mfma_f32_16x16x32_bf16 v[56:59], v[160:163], a[64:67], v[56:59]\n"
"v_rcp_f32 v27, v27\n"
"v_mul_f32 v24, v24, v40\n"
"v_mfma_f32_16x16x32_bf16 v[60:63], v[160:163], a[68:71], v[60:63]\n"
"v_mul_f32 v25, v25, v41\n"
"v_mul_f32 v26, v26, v42\n"
"v_mul_f32 v27, v27, v43\n"
"v_mfma_f32_16x16x32_bf16 v[64:67], v[160:163], a[72:75], v[64:67]\n"
"v_cvt_pk_bf16_f32 v28, v24, v25\n"
"v_cvt_pk_bf16_f32 v29, v26, v27\n"
"v_mfma_f32_16x16x32_bf16 v[68:71], v[160:163], a[76:79], v[68:71]\n"
"global_load_dwordx4 v[160:163], v4, s[94:95] offset:0 sc1\n"
"v_mfma_f32_16x16x32_bf16 v[56:59], v[164:167], a[80:83], v[56:59]\n"
"ds_write_b64 v7, v[28:29] offset:0\n"
"v_mul_f32 v32, v44, v44\n"
"v_mul_f32 v33, v45, v45\n"
"v_mfma_f32_16x16x32_bf16 v[60:63], v[164:167], a[84:87], v[60:63]\n"
"v_mul_f32 v34, v46, v46\n"
"v_mul_f32 v35, v47, v47\n"
"v_mfma_f32_16x16x32_bf16 v[64:67], v[164:167], a[88:91], v[64:67]\n"
"s_waitcnt vmcnt(33)\n"
"v_fma_f32 v32, v32, v19, v15\n"
"v_fma_f32 v33, v33, v19, v15\n"
"v_fma_f32 v34, v34, v19, v15\n"
"v_mfma_f32_16x16x32_bf16 v[68:71], v[164:167], a[92:95], v[68:71]\n"
"global_load_dwordx4 v[164:167], v4, s[94:95] offset:1024 sc1\n"
"v_fma_f32 v35, v35, v19, v15\n"
"v_mul_f32 v32, v32, v44\n"
"v_mfma_f32_16x16x32_bf16 v[56:59], v[168:171], a[96:99], v[56:59]\n"
"v_mul_f32 v33, v33, v45\n"
"v_mul_f32 v34, v34, v46\n"
"v_mfma_f32_16x16x32_bf16 v[60:63], v[168:171], a[100:103], v[60:63]\n"
"v_mul_f32 v35, v35, v47\n"
"v_exp_f32 v32, v32\n"
"v_mfma_f32_16x16x32_bf16 v[64:67], v[168:171], a[104:107], v[64:67]\n"
"v_exp_f32 v33, v33\n"
"v_mfma_f32_16x16x32_bf16 v[68:71], v[168:171], a[108:111], v[68:71]\n"
"global_load_dwordx4 v[168:171], v4, s[94:95] offset:2048 sc1\n"
"v_exp_f32 v34, v34\n"
"v_mfma_f32_16x16x32_bf16 v[56:59], v[172:175], a[112:115], v[56:59]\n"
"v_exp_f32 v35, v35\n"
"v_add_f32 v32, 1.0, v32\n"
"v_mfma_f32_16x16x32_bf16 v[60:63], v[172:175], a[116:119], v[60:63]\n"
"v_add_f32 v33, 1.0, v33\n"
"v_add_f32 v34, 1.0, v34\n"
"v_add_f32 v35, 1.0, v35\n"
"v_mfma_f32_16x16x32_bf16 v[64:67], v[172:175], a[120:123], v[64:67]\n"
"s_waitcnt vmcnt(33)\n"
"v_rcp_f32 v32, v32\n"
"v_mfma_f32_16x16x32_bf16 v[68:71], v[172:175], a[124:127], v[68:71]\n"
"global_load_dwordx4 v[172:175], v4, s[94:95] offset:3072 sc1\n"
"v_rcp_f32 v33, v33\n"
"v_mfma_f32_16x16x32_bf16 v[56:59], v[176:179], a[128:131], v[56:59]\n"
"v_rcp_f32 v34, v34\n"
"v_mfma_f32_16x16x32_bf16 v[60:63], v[176:179], a[132:135], v[60:63]\n"
"v_rcp_f32 v35, v35\n"
"v_mul_f32 v32, v32, v44\n"
"v_mfma_f32_16x16x32_bf16 v[64:67], v[176:179], a[136:139], v[64:67]\n"
"v_mul_f32 v33, v33, v45\n"
"v_mul_f32 v34, v34, v46\n"
"v_mul_f32 v35, v35, v47\n"
"v_mfma_f32_16x16x32_bf16 v[68:71], v[176:179], a[140:143], v[68:71]\n"
"global_load_dwordx4 v[176:179], v5, s[94:95] offset:-4096 sc1\n"
"v_cvt_pk_bf16_f32 v36, v32, v33\n"
"v_cvt_pk_bf16_f32 v37, v34, v35\n"
"v_mfma_f32_16x16x32_bf16 v[56:59], v[180:183], a[144:147], v[56:59]\n"
"ds_write_b64 v7, v[36:37] offset:1024\n"
"v_mfma_f32_16x16x32_bf16 v[60:63], v[180:183], a[148:151], v[60:63]\n"
"v_mul_f32 v24, v48, v48\n"
"v_mul_f32 v25, v49, v49\n"
"v_mfma_f32_16x16x32_bf16 v[64:67], v[180:183], a[152:155], v[64:67]\n"
"s_waitcnt vmcnt(33)\n"
"v_mul_f32 v26, v50, v50\n"
"v_mul_f32 v27, v51, v51\n"
"v_mfma_f32_16x16x32_bf16 v[68:71], v[180:183], a[156:159], v[68:71]\n"
"global_load_dwordx4 v[180:183], v5, s[94:95] offset:-3072 sc1\n"
"v_fma_f32 v24, v24, v20, v16\n"
"v_fma_f32 v25, v25, v20, v16\n"
"v_mfma_f32_16x16x32_bf16 v[56:59], v[184:187], a[160:163], v[56:59]\n"
"v_fma_f32 v26, v26, v20, v16\n"
"v_fma_f32 v27, v27, v20, v16\n"
"v_mul_f32 v24, v24, v48\n"
"v_mfma_f32_16x16x32_bf16 v[60:63], v[184:187], a[164:167], v[60:63]\n"
"v_mul_f32 v25, v25, v49\n"
"v_mul_f32 v26, v26, v50\n"
"v_mfma_f32_16x16x32_bf16 v[64:67], v[184:187], a[168:171], v[64:67]\n"
"v_mul_f32 v27, v27, v51\n"
"v_exp_f32 v24, v24\n"
"v_mfma_f32_16x16x32_bf16 v[68:71], v[184:187], a[172:175], v[68:71]\n"
"global_load_dwordx4 v[184:187], v5, s[94:95] offset:-2048 sc1\n"
"v_exp_f32 v25, v25\n"
"v_mfma_f32_16x16x32_bf16 v[56:59], v[188:191], a[176:179], v[56:59]\n"
"v_exp_f32 v26, v26\n"
"v_mfma_f32_16x16x32_bf16 v[60:63], v[188:191], a[180:183], v[60:63]\n"
"v_exp_f32 v27, v27\n"
"v_add_f32 v24, 1.0, v24\n"
"v_mfma_f32_16x16x32_bf16 v[64:67], v[188:191], a[184:187], v[64:67]\n"
"s_waitcnt vmcnt(33)\n"
"v_add_f32 v25, 1.0, v25\n"
"v_add_f32 v26, 1.0, v26\n"
"v_add_f32 v27, 1.0, v27\n"
"v_mfma_f32_16x16x32_bf16 v[68:71], v[188:191], a[188:191], v[68:71]\n"
"global_load_dwordx4 v[188:191], v5, s[94:95] offset:-1024 sc1\n"
"v_mfma_f32_16x16x32_bf16 v[56:59], v[192:195], a[192:195], v[56:59]\n"
"v_rcp_f32 v24, v24\n"
"v_rcp_f32 v25, v25\n"
"v_mfma_f32_16x16x32_bf16 v[60:63], v[192:195], a[196:199], v[60:63]\n"
"v_rcp_f32 v26, v26\n"
"v_mfma_f32_16x16x32_bf16 v[64:67], v[192:195], a[200:203], v[64:67]\n"
"v_rcp_f32 v27, v27\n"
"v_mul_f32 v24, v24, v48\n"
"v_mfma_f32_16x16x32_bf16 v[68:71], v[192:195], a[204:207], v[68:71]\n"
"global_load_dwordx4 v[192:195], v5, s[94:95] offset:0 sc1\n"
"v_mul_f32 v25, v25, v49\n"
"v_mul_f32 v26, v26, v50\n"
"v_mfma_f32_16x16x32_bf16 v[56:59], v[196:199], a[208:211], v[56:59]\n"
"v_mul_f32 v27, v27, v51\n"
"v_cvt_pk_bf16_f32 v28, v24, v25\n"
"v_cvt_pk_bf16_f32 v29, v26, v27\n"
"v_mfma_f32_16x16x32_bf16 v[60:63], v[196:199], a[212:215], v[60:63]\n"
"ds_write_b64 v7, v[28:29] offset:2048\n"
"v_mfma_f32_16x16x32_bf16 v[64:67], v[196:199], a[216:219], v[64:67]\n"
"s_waitcnt vmcnt(33)\n"
"v_mul_f32 v32, v52, v52\n"
"v_mul_f32 v33, v53, v53\n"
"v_mfma_f32_16x16x32_bf16 v[68:71], v[196:199], a[220:223], v[68:71]\n"
"global_load_dwordx4 v[196:199], v5, s[94:95] offset:1024 sc1\n"
"v_mul_f32 v34, v54, v54\n"
"v_mul_f32 v35, v55, v55\n"
"v_mfma_f32_16x16x32_bf16 v[56:59], v[200:203], a[224:227], v[56:59]\n"
"v_fma_f32 v32, v32, v21, v17\n"
"v_fma_f32 v33, v33, v21, v17\n"
"v_mfma_f32_16x16x32_bf16 v[60:63], v[200:203], a[228:231], v[60:63]\n"
"v_fma_f32 v34, v34, v21, v17\n"
"v_fma_f32 v35, v35, v21, v17\n"
"v_mul_f32 v32, v32, v52\n"
"v_mfma_f32_16x16x32_bf16 v[64:67], v[200:203], a[232:235], v[64:67]\n"
"v_mul_f32 v33, v33, v53\n"
"v_mul_f32 v34, v34, v54\n"
"v_mul_f32 v35, v35, v55\n"
"v_mfma_f32_16x16x32_bf16 v[68:71], v[200:203], a[236:239], v[68:71]\n"
"global_load_dwordx4 v[200:203], v5, s[94:95] offset:2048 sc1\n"
"v_mfma_f32_16x16x32_bf16 v[56:59], v[204:207], a[240:243], v[56:59]\n"
"v_exp_f32 v32, v32\n"
"v_exp_f32 v33, v33\n"
"v_mfma_f32_16x16x32_bf16 v[60:63], v[204:207], a[244:247], v[60:63]\n"
"v_exp_f32 v34, v34\n"
"v_mfma_f32_16x16x32_bf16 v[64:67], v[204:207], a[248:251], v[64:67]\n"
"s_waitcnt vmcnt(33)\n"
"v_exp_f32 v35, v35\n"
"v_add_f32 v32, 1.0, v32\n"
"v_mfma_f32_16x16x32_bf16 v[68:71], v[204:207], a[252:255], v[68:71]\n"
"global_load_dwordx4 v[204:207], v5, s[94:95] offset:3072 sc1\n"
"v_add_f32 v33, 1.0, v33\n"
"v_add_f32 v34, 1.0, v34\n"
"v_mfma_f32_16x16x32_bf16 v[56:59], v[208:211], v[224:227], v[56:59]\n"
"v_add_f32 v35, 1.0, v35\n"
"v_mfma_f32_16x16x32_bf16 v[60:63], v[208:211], v[228:231], v[60:63]\n"
"v_rcp_f32 v32, v32\n"
"v_rcp_f32 v33, v33\n"
"v_mfma_f32_16x16x32_bf16 v[64:67], v[208:211], v[232:235], v[64:67]\n"
"v_rcp_f32 v34, v34\n"
"v_mfma_f32_16x16x32_bf16 v[68:71], v[208:211], v[236:239], v[68:71]\n"
"global_load_dwordx4 v[208:211], v6, s[94:95] offset:-4096 sc1\n"
"v_rcp_f32 v35, v35\n"
"v_mfma_f32_16x16x32_bf16 v[56:59], v[212:215], v[240:243], v[56:59]\n"
"v_mul_f32 v32, v32, v52\n"
"v_mul_f32 v33, v33, v53\n"
"v_mul_f32 v34, v34, v54\n"
"v_mfma_f32_16x16x32_bf16 v[60:63], v[212:215], v[244:247], v[60:63]\n"
"v_mul_f32 v35, v35, v55\n"
"v_cvt_pk_bf16_f32 v36, v32, v33\n"
"v_cvt_pk_bf16_f32 v37, v34, v35\n"
"v_mfma_f32_16x16x32_bf16 v[64:67], v[212:215], v[248:251], v[64:67]\n"
"ds_write_b64 v7, v[36:37] offset:3072\n"
"v_mfma_f32_16x16x32_bf16 v[68:71], v[212:215], v[252:255], v[68:71]\n"
"global_load_dwordx4 v[212:215], v6, s[94:95] offset:-3072 sc1\n"
"v_add_u32 v7, 8192, v7\n"
"s_add_u32 s94, s94, 18432\n"
"s_addc_u32 s95, s95, 0\n"
"s_waitcnt vmcnt(34)\n"
"v_mfma_f32_16x16x32_bf16 v[40:43], v[72:75], a[0:3], 0\n"
"v_mfma_f32_16x16x32_bf16 v[44:47], v[72:75], a[4:7], 0\n"
"v_mfma_f32_16x16x32_bf16 v[48:51], v[72:75], a[8:11], 0\n"
"v_mfma_f32_16x16x32_bf16 v[52:55], v[72:75], a[12:15], 0\n"
"global_load_dwordx4 v[72:75], v4, s[94:95] offset:-4096 sc1\n"
"v_mul_f32 v24, v56, v56\n"
"v_mul_f32 v25, v57, v57\n"
"v_mfma_f32_16x16x32_bf16 v[40:43], v[76:79], a[16:19], v[40:43]\n"
"v_mul_f32 v26, v58, v58\n"
"v_mul_f32 v27, v59, v59\n"
"v_mfma_f32_16x16x32_bf16 v[44:47], v[76:79], a[20:23], v[44:47]\n"
"v_fma_f32 v24, v24, v18, v14\n"
"v_fma_f32 v25, v25, v18, v14\n"
"v_fma_f32 v26, v26, v18, v14\n"
"v_mfma_f32_16x16x32_bf16 v[48:51], v[76:79], a[24:27], v[48:51]\n"
"s_waitcnt vmcnt(33)\n"
"v_fma_f32 v27, v27, v18, v14\n"
"v_mul_f32 v24, v24, v56\n"
"v_mul_f32 v25, v25, v57\n"
"v_mfma_f32_16x16x32_bf16 v[52:55], v[76:79], a[28:31], v[52:55]\n"
"global_load_dwordx4 v[76:79], v4, s[94:95] offset:-3072 sc1\n"
"v_mul_f32 v26, v26, v58\n"
"v_mfma_f32_16x16x32_bf16 v[40:43], v[80:83], a[32:35], v[40:43]\n"
"v_mul_f32 v27, v27, v59\n"
"v_exp_f32 v24, v24\n"
"v_mfma_f32_16x16x32_bf16 v[44:47], v[80:83], a[36:39], v[44:47]\n"
"v_exp_f32 v25, v25\n"
"v_mfma_f32_16x16x32_bf16 v[48:51], v[80:83], a[40:43], v[48:51]\n"
"v_exp_f32 v26, v26\n"
"v_mfma_f32_16x16x32_bf16 v[52:55], v[80:83], a[44:47], v[52:55]\n"
"global_load_dwordx4 v[80:83], v4, s[94:95] offset:-2048 sc1\n"
"v_exp_f32 v27, v27\n"
"v_add_f32 v24, 1.0, v24\n"
"v_mfma_f32_16x16x32_bf16 v[40:43], v[84:87], a[48:51], v[40:43]\n"
"v_add_f32 v25, 1.0, v25\n"
"v_add_f32 v26, 1.0, v26\n"
"v_add_f32 v27, 1.0, v27\n"
"v_mfma_f32_16x16x32_bf16 v[44:47], v[84:87], a[52:55], v[44:47]\n"
"v_rcp_f32 v24, v24\n"
"v_mfma_f32_16x16x32_bf16 v[48:51], v[84:87], a[56:59], v[48:51]\n"
"s_waitcnt vmcnt(33)\n"
"v_rcp_f32 v25, v25\n"
"v_mfma_f32_16x16x32_bf16 v[52:55], v[84:87], a[60:63], v[52:55]\n"
"global_load_dwordx4 v[84:87], v4, s[94:95] offset:-1024 sc1\n"
"v_rcp_f32 v26, v26\n"
"v_mfma_f32_16x16x32_bf16 v[40:43], v[88:91], a[64:67], v[40:43]\n"
"v_rcp_f32 v27, v27\n"
"v_mul_f32 v24, v24, v56\n"
"v_mfma_f32_16x16x32_bf16 v[44:47], v[88:91], a[68:71], v[44:47]\n"
"v_mul_f32 v25, v25, v57\n"
"v_mul_f32 v26, v26, v58\n"
"v_mul_f32 v27, v27, v59\n"
"v_mfma_f32_16x16x32_bf16 v[48:51], v[88:91], a[72:75], v[48:51]\n"
"v_cvt_pk_bf16_f32 v28, v24, v25\n"
"v_cvt_pk_bf16_f32 v29, v26, v27\n"
"v_mfma_f32_16x16x32_bf16 v[52:55], v[88:91], a[76:79], v[52:55]\n"
"global_load_dwordx4 v[88:91], v4, s[94:95] offset:0 sc1\n"
"v_mfma_f32_16x16x32_bf16 v[40:43], v[92:95], a[80:83], v[40:43]\n"
"ds_write_b64 v7, v[28:29] offset:0\n"
"v_mul_f32 v32, v60, v60\n"
"v_mul_f32 v33, v61, v61\n"
"v_mfma_f32_16x16x32_bf16 v[44:47], v[92:95], a[84:87], v[44:47]\n"
"v_mul_f32 v34, v62, v62\n"
"v_mul_f32 v35, v63, v63\n"
"v_mfma_f32_16x16x32_bf16 v[48:51], v[92:95], a[88:91], v[48:51]\n"
"s_waitcnt vmcnt(33)\n"
"v_fma_f32 v32, v32, v19, v15\n"
"v_fma_f32 v33, v33, v19, v15\n"
"v_fma_f32 v34, v34, v19, v15\n"
"v_mfma_f32_16x16x32_bf16 v[52:55], v[92:95], a[92:95], v[52:55]\n"
"global_load_dwordx4 v[92:95], v4, s[94:95] offset:1024 sc1\n"
"v_fma_f32 v35, v35, v19, v15\n"
"v_mul_f32 v32, v32, v60\n"
"v_mfma_f32_16x16x32_bf16 v[40:43], v[96:99], a[96:99], v[40:43]\n"
"v_mul_f32 v33, v33, v61\n"
"v_mul_f32 v34, v34, v62\n"
"v_mfma_f32_16x16x32_bf16 v[44:47], v[96:99], a[100:103], v[44:47]\n"
"v_mul_f32 v35, v35, v63\n"
"v_exp_f32 v32, v32\n"
"v_mfma_f32_16x16x32_bf16 v[48:51], v[96:99], a[104:107], v[48:51]\n"
"v_exp_f32 v33, v33\n"
"v_mfma_f32_16x16x32_bf16 v[52:55], v[96:99], a[108:111], v[52:55]\n"
"global_load_dwordx4 v[96:99], v4, s[94:95] offset:2048 sc1\n"
"v_exp_f32 v34, v34\n"
"v_mfma_f32_16x16x32_bf16 v[40:43], v[100:103], a[112:115], v[40:43]\n"
"v_exp_f32 v35, v35\n"
"v_add_f32 v32, 1.0, v32\n"
"v_mfma_f32_16x16x32_bf16 v[44:47], v[100:103], a[116:119], v[44:47]\n"
"v_add_f32 v33, 1.0, v33\n"
"v_add_f32 v34, 1.0, v34\n"
"v_add_f32 v35, 1.0, v35\n"
"v_mfma_f32_16x16x32_bf16 v[48:51], v[100:103], a[120:123], v[48:51]\n"
"s_waitcnt vmcnt(33)\n"
"v_rcp_f32 v32, v32\n"
"v_mfma_f32_16x16x32_bf16 v[52:55], v[100:103], a[124:127], v[52:55]\n"
"global_load_dwordx4 v[100:103], v4, s[94:95] offset:3072 sc1\n"
"v_rcp_f32 v33, v33\n"
"v_mfma_f32_16x16x32_bf16 v[40:43], v[104:107], a[128:131], v[40:43]\n"
"v_rcp_f32 v34, v34\n"
"v_mfma_f32_16x16x32_bf16 v[44:47], v[104:107], a[132:135], v[44:47]\n"
"v_rcp_f32 v35, v35\n"
"v_mul_f32 v32, v32, v60\n"
"v_mfma_f32_16x16x32_bf16 v[48:51], v[104:107], a[136:139], v[48:51]\n"
"v_mul_f32 v33, v33, v61\n"
"v_mul_f32 v34, v34, v62\n"
"v_mul_f32 v35, v35, v63\n"
"v_mfma_f32_16x16x32_bf16 v[52:55], v[104:107], a[140:143], v[52:55]\n"
"global_load_dwordx4 v[104:107], v5, s[94:95] offset:-4096 sc1\n"
"v_cvt_pk_bf16_f32 v36, v32, v33\n"
"v_cvt_pk_bf16_f32 v37, v34, v35\n"
"v_mfma_f32_16x16x32_bf16 v[40:43], v[108:111], a[144:147], v[40:43]\n"
"ds_write_b64 v7, v[36:37] offset:1024\n"
"v_mfma_f32_16x16x32_bf16 v[44:47], v[108:111], a[148:151], v[44:47]\n"
"v_mul_f32 v24, v64, v64\n"
"v_mul_f32 v25, v65, v65\n"
"v_mfma_f32_16x16x32_bf16 v[48:51], v[108:111], a[152:155], v[48:51]\n"
"s_waitcnt vmcnt(33)\n"
"v_mul_f32 v26, v66, v66\n"
"v_mul_f32 v27, v67, v67\n"
"v_mfma_f32_16x16x32_bf16 v[52:55], v[108:111], a[156:159], v[52:55]\n"
"global_load_dwordx4 v[108:111], v5, s[94:95] offset:-3072 sc1\n"
"v_fma_f32 v24, v24, v20, v16\n"
"v_fma_f32 v25, v25, v20, v16\n"
"v_mfma_f32_16x16x32_bf16 v[40:43], v[112:115], a[160:163], v[40:43]\n"
"v_fma_f32 v26, v26, v20, v16\n"
"v_fma_f32 v27, v27, v20, v16\n"
"v_mul_f32 v24, v24, v64\n"
"v_mfma_f32_16x16x32_bf16 v[44:47], v[112:115], a[164:167], v[44:47]\n"
"v_mul_f32 v25, v25, v65\n"
"v_mul_f32 v26, v26, v66\n"
"v_mfma_f32_16x16x32_bf16 v[48:51], v[112:115], a[168:171], v[48:51]\n"
"v_mul_f32 v27, v27, v67\n"
"v_exp_f32 v24, v24\n"
"v_mfma_f32_16x16x32_bf16 v[52:55], v[112:115], a[172:175], v[52:55]\n"
"global_load_dwordx4 v[112:115], v5, s[94:95] offset:-2048 sc1\n"
"v_exp_f32 v25, v25\n"
"v_mfma_f32_16x16x32_bf16 v[40:43], v[116:119], a[176:179], v[40:43]\n"
"v_exp_f32 v26, v26\n"
"v_mfma_f32_16x16x32_bf16 v[44:47], v[116:119], a[180:183], v[44:47]\n"
"v_exp_f32 v27, v27\n"
"v_add_f32 v24, 1.0, v24\n"
"v_mfma_f32_16x16x32_bf16 v[48:51], v[116:119], a[184:187], v[48:51]\n"
"s_waitcnt vmcnt(33)\n"
"v_add_f32 v25, 1.0, v25\n"
"v_add_f32 v26, 1.0, v26\n"
"v_add_f32 v27, 1.0, v27\n"
"v_mfma_f32_16x16x32_bf16 v[52:55], v[116:119], a[188:191], v[52:55]\n"
"global_load_dwordx4 v[116:119], v5, s[94:95] offset:-1024 sc1\n"
"v_mfma_f32_16x16x32_bf16 v[40:43], v[120:123], a[192:195], v[40:43]\n"
"v_rcp_f32 v24, v24\n"
"v_rcp_f32 v25, v25\n"
"v_mfma_f32_16x16x32_bf16 v[44:47], v[120:123], a[196:199], v[44:47]\n"
"v_rcp_f32 v26, v26\n"
"v_mfma_f32_16x16x32_bf16 v[48:51], v[120:123], a[200:203], v[48:51]\n"
"v_rcp_f32 v27, v27\n"
"v_mul_f32 v24, v24, v64\n"
"v_mfma_f32_16x16x32_bf16 v[52:55], v[120:123], a[204:207], v[52:55]\n"
"global_load_dwordx4 v[120:123], v5, s[94:95] offset:0 sc1\n"
"v_mul_f32 v25, v25, v65\n"
"v_mul_f32 v26, v26, v66\n"
"v_mfma_f32_16x16x32_bf16 v[40:43], v[124:127], a[208:211], v[40:43]\n"
"v_mul_f32 v27, v27, v67\n"
"v_cvt_pk_bf16_f32 v28, v24, v25\n"
"v_cvt_pk_bf16_f32 v29, v26, v27\n"
"v_mfma_f32_16x16x32_bf16 v[44:47], v[124:127], a[212:215], v[44:47]\n"
"ds_write_b64 v7, v[28:29] offset:2048\n"
"v_mfma_f32_16x16x32_bf16 v[48:51], v[124:127], a[216:219], v[48:51]\n"
"s_waitcnt vmcnt(33)\n"
"v_mul_f32 v32, v68, v68\n"
"v_mul_f32 v33, v69, v69\n"
"v_mfma_f32_16x16x32_bf16 v[52:55], v[124:127], a[220:223], v[52:55]\n"
"global_load_dwordx4 v[124:127], v5, s[94:95] offset:1024 sc1\n"
"v_mul_f32 v34, v70, v70\n"
"v_mul_f32 v35, v71, v71\n"
"v_mfma_f32_16x16x32_bf16 v[40:43], v[128:131], a[224:227], v[40:43]\n"
"v_fma_f32 v32, v32, v21, v17\n"
"v_fma_f32 v33, v33, v21, v17\n"
"v_mfma_f32_16x16x32_bf16 v[44:47], v[128:131], a[228:231], v[44:47]\n"
"v_fma_f32 v34, v34, v21, v17\n"
"v_fma_f32 v35, v35, v21, v17\n"
"v_mul_f32 v32, v32, v68\n"
"v_mfma_f32_16x16x32_bf16 v[48:51], v[128:131], a[232:235], v[48:51]\n"
"v_mul_f32 v33, v33, v69\n"
"v_mul_f32 v34, v34, v70\n"
"v_mul_f32 v35, v35, v71\n"
"v_mfma_f32_16x16x32_bf16 v[52:55], v[128:131], a[236:239], v[52:55]\n"
"global_load_dwordx4 v[128:131], v5, s[94:95] offset:2048 sc1\n"
"v_mfma_f32_16x16x32_bf16 v[40:43], v[132:135], a[240:243], v[40:43]\n"
"v_exp_f32 v32, v32\n"
"v_exp_f32 v33, v33\n"
"v_mfma_f32_16x16x32_bf16 v[44:47], v[132:135], a[244:247], v[44:47]\n"
"v_exp_f32 v34, v34\n"
"v_mfma_f32_16x16x32_bf16 v[48:51], v[132:135], a[248:251], v[48:51]\n"
"s_waitcnt vmcnt(33)\n"
"v_exp_f32 v35, v35\n"
"v_add_f32 v32, 1.0, v32\n"
"v_mfma_f32_16x16x32_bf16 v[52:55], v[132:135], a[252:255], v[52:55]\n"
"global_load_dwordx4 v[132:135], v5, s[94:95] offset:3072 sc1\n"
"v_add_f32 v33, 1.0, v33\n"
"v_add_f32 v34, 1.0, v34\n"
"v_mfma_f32_16x16x32_bf16 v[40:43], v[136:139], v[224:227], v[40:43]\n"
"v_add_f32 v35, 1.0, v35\n"
"v_mfma_f32_16x16x32_bf16 v[44:47], v[136:139], v[228:231], v[44:47]\n"
"v_rcp_f32 v32, v32\n"
"v_rcp_f32 v33, v33\n"
"v_mfma_f32_16x16x32_bf16 v[48:51], v[136:139], v[232:235], v[48:51]\n"
"v_rcp_f32 v34, v34\n"
"v_mfma_f32_16x16x32_bf16 v[52:55], v[136:139], v[236:239], v[52:55]\n"
"global_load_dwordx4 v[136:139], v6, s[94:95] offset:-4096 sc1\n"
"v_rcp_f32 v35, v35\n"
"v_mfma_f32_16x16x32_bf16 v[40:43], v[140:143], v[240:243], v[40:43]\n"
"v_mul_f32 v32, v32, v68\n"
"v_mul_f32 v33, v33, v69\n"
"v_mul_f32 v34, v34, v70\n"
"v_mfma_f32_16x16x32_bf16 v[44:47], v[140:143], v[244:247], v[44:47]\n"
"v_mul_f32 v35, v35, v71\n"
"v_cvt_pk_bf16_f32 v36, v32, v33\n"
"v_cvt_pk_bf16_f32 v37, v34, v35\n"
"v_mfma_f32_16x16x32_bf16 v[48:51], v[140:143], v[248:251], v[48:51]\n"
"ds_write_b64 v7, v[36:37] offset:3072\n"
"v_mfma_f32_16x16x32_bf16 v[52:55], v[140:143], v[252:255], v[52:55]\n"
"global_load_dwordx4 v[140:143], v6, s[94:95] offset:-3072 sc1\n"
"v_add_u32 v7, 8192, v7\n"
"s_add_u32 s94, s94, 18432\n"
"s_addc_u32 s95, s95, 0\n"
"s_sub_u32 s86, s86, 1\n"
"s_cmp_lg_u32 s86, 0\n"
"s_cbranch_scc1 .Lfused_block_kernel_g1\n"
"s_lshl_b32 s80, s26, 11\n"
"s_mul_i32 s72, s28, 458752\n"
"s_add_u32 s72, s72, s80\n"
"s_add_u32 s72, s72, 30408704\n"
"s_mul_i32 s73, s29, 57344\n"
"s_add_u32 s73, s73, s72\n"
"s_lshl_b32 s74, s28, 16\n"
"s_add_u32 s74, s74, s80\n"
"s_add_u32 s74, s74, 45088768\n"
"s_lshl_b32 s80, s26, 7\n"
"s_add_u32 s79, s31, s80\n"
"s_lshl_b32 s75, s28, 14\n"
"s_add_u32 s75, s75, s80\n"
"s_add_u32 s75, s75, 65536\n"
"s_lshr_b32 s78, s29, 1\n"
"s_and_b32 s80, s29, 1\n"
"v_lshrrev_b32 v13, 3, v2\n"
"v_cmp_eq_u32 s[76:77], s80, v13\n"
"s_lshr_b32 s80, s78, 1\n"
"s_lshl_b32 s80, s80, 10\n"
"s_and_b32 s81, s78, 1\n"
"s_lshl_b32 s81, s81, 3\n"
"s_add_u32 s80, s80, s81\n"
"v_add_u32 v13, s80, v1\n"
"v_cndmask_b32 v13, v8, v13, s[76:77]\n"
"s_waitcnt lgkmcnt(0)\n"
"s_barrier\n"
"ds_read_b128 a[0:3], v11 offset:16384\n"
"ds_read_b128 a[4:7], v11 offset:17408\n"
"ds_read_b128 a[8:11], v11 offset:18432\n"
"ds_read_b128 a[12:15], v11 offset:19456\n"
"ds_read_b128 a[16:19], v11 offset:20480\n"
"ds_read_b128 a[20:23], v11 offset:21504\n"
"ds_read_b128 a[24:27], v11 offset:22528\n"
"ds_read_b128 a[28:31], v11 offset:23552\n"
"s_waitcnt lgkmcnt(4)\n"
"s_waitcnt vmcnt(34)\n"
"v_mfma_f32_16x16x32_bf16 v[56:59], v[144:147], a[0:3], 0\n"
"ds_read_b128 a[32:35], v11 offset:24576\n"
"v_mfma_f32_16x16x32_bf16 v[60:63], v[144:147], a[4:7], 0\n"
"ds_read_b128 a[36:39], v11 offset:25600\n"
"v_mfma_f32_16x16x32_bf16 v[64:67], v[144:147], a[8:11], 0\n"
"ds_read_b128 a[40:43], v11 offset:26624\n"
"v_mfma_f32_16x16x32_bf16 v[68:71], v[144:147], a[12:15], 0\n"
"s_waitcnt lgkmcnt(3)\n"
"ds_read_b128 a[44:47], v11 offset:27648\n"
"global_load_dwordx4 v[144:147], v4, s[94:95] offset:-4096 sc1\n"
"v_mul_f32 v24, v40, v40\n"
"v_mul_f32 v25, v41, v41\n"
"v_mfma_f32_16x16x32_bf16 v[56:59], v[148:151], a[16:19], v[56:59]\n"
"ds_read_b128 a[48:51], v11 offset:28672\n"
"v_mul_f32 v26, v42, v42\n"
"v_mul_f32 v27, v43, v43\n"
"v_fma_f32 v24, v24, v18, v14\n"
"v_fma_f32 v25, v25, v18, v14\n"
"v_mfma_f32_16x16x32_bf16 v[60:63], v[148:151], a[20:23], v[60:63]\n"
"ds_read_b128 a[52:55], v11 offset:29696\n"
"v_fma_f32 v26, v26, v18, v14\n"
"v_fma_f32 v27, v27, v18, v14\n"
"v_mul_f32 v24, v24, v40\n"
"v_mfma_f32_16x16x32_bf16 v[64:67], v[148:151], a[24:27], v[64:67]\n"
"s_waitcnt vmcnt(33)\n"
"ds_read_b128 a[56:59], v11 offset:30720\n"
"v_mul_f32 v25, v25, v41\n"
"v_mul_f32 v26, v26, v42\n"
"v_mul_f32 v27, v27, v43\n"
"v_mfma_f32_16x16x32_bf16 v[68:71], v[148:151], a[28:31], v[68:71]\n"
"s_waitcnt lgkmcnt(3)\n"
"ds_read_b128 a[60:63], v11 offset:31744\n"
"global_load_dwordx4 v[148:151], v4, s[94:95] offset:-3072 sc1\n"
"v_exp_f32 v24, v24\n"
"v_mfma_f32_16x16x32_bf16 v[56:59], v[152:155], a[32:35], v[56:59]\n"
"ds_read_b128 a[64:67], v11 offset:32768\n"
"v_exp_f32 v25, v25\n"
"v_exp_f32 v26, v26\n"
"v_mfma_f32_16x16x32_bf16 v[60:63], v[152:155], a[36:39], v[60:63]\n"
"ds_read_b128 a[68:71], v11 offset:33792\n"
"v_exp_f32 v27, v27\n"
"v_add_f32 v24, 1.0, v24\n"
"v_add_f32 v25, 1.0, v25\n"
"v_mfma_f32_16x16x32_bf16 v[64:67], v[152:155], a[40:43], v[64:67]\n"
"ds_read_b128 a[72:75], v11 offset:34816\n"
"v_add_f32 v26, 1.0, v26\n"
"v_add_f32 v27, 1.0, v27\n"
"v_mfma_f32_16x16x32_bf16 v[68:71], v[152:155], a[44:47], v[68:71]\n"
"s_waitcnt lgkmcnt(3)\n"
"ds_read_b128 a[76:79], v11 offset:35840\n"
"global_load_dwordx4 v[152:155], v4, s[94:95] offset:-2048 sc1\n"
"v_rcp_f32 v24, v24\n"
"v_mfma_f32_16x16x32_bf16 v[56:59], v[156:159], a[48:51], v[56:59]\n"
"ds_read_b128 a[80:83], v11 offset:36864\n"
"v_rcp_f32 v25, v25\n"
"v_rcp_f32 v26, v26\n"
"v_mfma_f32_16x16x32_bf16 v[60:63], v[156:159], a[52:55], v[60:63]\n"
"ds_read_b128 a[84:87], v11 offset:37888\n"
"v_rcp_f32 v27, v27\n"
"v_mul_f32 v24, v24, v40\n"
"v_mul_f32 v25, v25, v41\n"
"v_mfma_f32_16x16x32_bf16 v[64:67], v[156:159], a[56:59], v[64:67]\n"
"s_waitcnt vmcnt(33)\n"
"ds_read_b128 a[88:91], v11 offset:38912\n"
"v_mul_f32 v26, v26, v42\n"
"v_mul_f32 v27, v27, v43\n"
"v_cvt_pk_bf16_f32 v28, v24, v25\n"
"v_cvt_pk_bf16_f32 v29, v26, v27\n"
"v_mfma_f32_16x16x32_bf16 v[68:71], v[156:159], a[60:63], v[68:71]\n"
"s_waitcnt lgkmcnt(3)\n"
"ds_read_b128 a[92:95], v11 offset:39936\n"
"global_load_dwordx4 v[156:159], v4, s[94:95] offset:-1024 sc1\n"
"v_mfma_f32_16x16x32_bf16 v[56:59], v[160:163], a[64:67], v[56:59]\n"
"ds_read_b128 a[96:99], v11 offset:40960\n"
"v_mfma_f32_16x16x32_bf16 v[60:63], v[160:163], a[68:71], v[60:63]\n"
"ds_read_b128 a[100:103], v11 offset:41984\n"
"v_mfma_f32_16x16x32_bf16 v[64:67], v[160:163], a[72:75], v[64:67]\n"
"ds_read_b128 a[104:107], v11 offset:43008\n"
"v_mfma_f32_16x16x32_bf16 v[68:71], v[160:163], a[76:79], v[68:71]\n"
"s_waitcnt lgkmcnt(3)\n"
"ds_read_b128 a[108:111], v11 offset:44032\n"
"global_load_dwordx4 v[160:163], v4, s[94:95] offset:0 sc1\n"
"v_mfma_f32_16x16x32_bf16 v[56:59], v[164:167], a[80:83], v[56:59]\n"
"ds_read_b128 a[112:115], v11 offset:45056\n"
"v_mfma_f32_16x16x32_bf16 v[60:63], v[164:167], a[84:87], v[60:63]\n"
"ds_read_b128 a[116:119], v11 offset:46080\n"
"v_mfma_f32_16x16x32_bf16 v[64:67], v[164:167], a[88:91], v[64:67]\n"
"s_waitcnt vmcnt(33)\n"
"ds_read_b128 a[120:123], v11 offset:47104\n"
"v_mfma_f32_16x16x32_bf16 v[68:71], v[164:167], a[92:95], v[68:71]\n"
"s_waitcnt lgkmcnt(3)\n"
"ds_read_b128 a[124:127], v11 offset:48128\n"
"global_load_dwordx4 v[164:167], v4, s[94:95] offset:1024 sc1\n"
"v_mfma_f32_16x16x32_bf16 v[56:59], v[168:171], a[96:99], v[56:59]\n"
"ds_read_b128 a[128:131], v11 offset:49152\n"
"v_mfma_f32_16x16x32_bf16 v[60:63], v[168:171], a[100:103], v[60:63]\n"
"ds_read_b128 a[132:135], v11 offset:50176\n"
"v_mfma_f32_16x16x32_bf16 v[64:67], v[168:171], a[104:107], v[64:67]\n"
"ds_read_b128 a[136:139], v11 offset:51200\n"
"v_mfma_f32_16x16x32_bf16 v[68:71], v[168:171], a[108:111], v[68:71]\n"
"s_waitcnt lgkmcnt(3)\n"
"ds_read_b128 a[140:143], v11 offset:52224\n"
"global_load_dwordx4 v[168:171], v4, s[94:95] offset:2048 sc1\n"
"v_mfma_f32_16x16x32_bf16 v[56:59], v[172:175], a[112:115], v[56:59]\n"
"ds_read_b128 a[144:147], v11 offset:53248\n"
"v_mfma_f32_16x16x32_bf16 v[60:63], v[172:175], a[116:119], v[60:63]\n"
"ds_read_b128 a[148:151], v11 offset:54272\n"
"v_mfma_f32_16x16x32_bf16 v[64:67], v[172:175], a[120:123], v[64:67]\n"
"s_waitcnt vmcnt(33)\n"
"ds_read_b128 a[152:155], v11 offset:55296\n"
"v_mfma_f32_16x16x32_bf16 v[68:71], v[172:175], a[124:127], v[68:71]\n"
"s_waitcnt lgkmcnt(3)\n"
"ds_read_b128 a[156:159], v11 offset:56320\n"
"global_load_dwordx4 v[172:175], v4, s[94:95] offset:3072 sc1\n"
"v_mfma_f32_16x16x32_bf16 v[56:59], v[176:179], a[128:131], v[56:59]\n"
"ds_read_b128 a[160:163], v11 offset:57344\n"
"v_mfma_f32_16x16x32_bf16 v[60:63], v[176:179], a[132:135], v[60:63]\n"
"ds_read_b128 a[164:167], v11 offset:58368\n"
"v_mfma_f32_16x16x32_bf16 v[64:67], v[176:179], a[136:139], v[64:67]\n"
"ds_read_b128 a[168:171], v11 offset:59392\n"
"v_mfma_f32_16x16x32_bf16 v[68:71], v[176:179], a[140:143], v[68:71]\n"
"s_waitcnt lgkmcnt(3)\n"
"ds_read_b128 a[172:175], v11 offset:60416\n"
"global_load_dwordx4 v[176:179], v5, s[94:95] offset:-4096 sc1\n"
"v_mfma_f32_16x16x32_bf16 v[56:59], v[180:183], a[144:147], v[56:59]\n"
"ds_read_b128 a[176:179], v11 offset:61440\n"
"v_mfma_f32_16x16x32_bf16 v[60:63], v[180:183], a[148:151], v[60:63]\n"
"ds_read_b128 a[180:183], v11 offset:62464\n"
"v_mfma_f32_16x16x32_bf16 v[64:67], v[180:183], a[152:155], v[64:67]\n"
"s_waitcnt vmcnt(33)\n"
"ds_read_b128 a[184:187], v11 offset:63488\n"
"v_mfma_f32_16x16x32_bf16 v[68:71], v[180:183], a[156:159], v[68:71]\n"
"s_waitcnt lgkmcnt(3)\n"
"ds_read_b128 a[188:191], v11 offset:64512\n"
"global_load_dwordx4 v[180:183], v5, s[94:95] offset:-3072 sc1\n"
"v_mfma_f32_16x16x32_bf16 v[56:59], v[184:187], a[160:163], v[56:59]\n"
"ds_read_b128 a[192:195], v12 offset:0\n"
"v_mfma_f32_16x16x32_bf16 v[60:63], v[184:187], a[164:167], v[60:63]\n"
"ds_read_b128 a[196:199], v12 offset:1024\n"
"v_mfma_f32_16x16x32_bf16 v[64:67], v[184:187], a[168:171], v[64:67]\n"
"ds_read_b128 a[200:203], v12 offset:2048\n"
"v_mfma_f32_16x16x32_bf16 v[68:71], v[184:187], a[172:175], v[68:71]\n"
"s_waitcnt lgkmcnt(3)\n"
"ds_read_b128 a[204:207], v12 offset:3072\n"
"global_load_dwordx4 v[184:187], v5, s[94:95] offset:-2048 sc1\n"
"v_mfma_f32_16x16x32_bf16 v[56:59], v[188:191], a[176:179], v[56:59]\n"
"ds_read_b128 a[208:211], v12 offset:4096\n"
"v_mfma_f32_16x16x32_bf16 v[60:63], v[188:191], a[180:183], v[60:63]\n"
"ds_read_b128 a[212:215], v12 offset:5120\n"
"v_mfma_f32_16x16x32_bf16 v[64:67], v[188:191], a[184:187], v[64:67]\n"
"s_waitcnt vmcnt(33)\n"
"ds_read_b128 a[216:219], v12 offset:6144\n"
"v_mfma_f32_16x16x32_bf16 v[68:71], v[188:191], a[188:191], v[68:71]\n"
"s_waitcnt lgkmcnt(3)\n"
"ds_read_b128 a[220:223], v12 offset:7168\n"
"global_load_dwordx4 v[188:191], v5, s[94:95] offset:-1024 sc1\n"
"v_mfma_f32_16x16x32_bf16 v[56:59], v[192:195], a[192:195], v[56:59]\n"
"ds_read_b128 a[224:227], v12 offset:8192\n"
"v_mfma_f32_16x16x32_bf16 v[60:63], v[192:195], a[196:199], v[60:63]\n"
"ds_read_b128 a[228:231], v12 offset:9216\n"
"v_mfma_f32_16x16x32_bf16 v[64:67], v[192:195], a[200:203], v[64:67]\n"
"ds_read_b128 a[232:235], v12 offset:10240\n"
"v_mfma_f32_16x16x32_bf16 v[68:71], v[192:195], a[204:207], v[68:71]\n"
"s_waitcnt lgkmcnt(3)\n"
"ds_read_b128 a[236:239], v12 offset:11264\n"
"global_load_dwordx4 v[192:195], v5, s[94:95] offset:0 sc1\n"
"v_mfma_f32_16x16x32_bf16 v[56:59], v[196:199], a[208:211], v[56:59]\n"
"ds_read_b128 a[240:243], v12 offset:12288\n"
"v_mfma_f32_16x16x32_bf16 v[60:63], v[196:199], a[212:215], v[60:63]\n"
"ds_read_b128 a[244:247], v12 offset:13312\n"
"v_mfma_f32_16x16x32_bf16 v[64:67], v[196:199], a[216:219], v[64:67]\n"
"s_waitcnt vmcnt(33)\n"
"ds_read_b128 a[248:251], v12 offset:14336\n"
"v_mfma_f32_16x16x32_bf16 v[68:71], v[196:199], a[220:223], v[68:71]\n"
"s_waitcnt lgkmcnt(3)\n"
"ds_read_b128 a[252:255], v12 offset:15360\n"
"global_load_dwordx4 v[196:199], v5, s[94:95] offset:1024 sc1\n"
"ds_write_b64 v7, v[28:29] offset:0\n"
"v_mul_f32 v32, v44, v44\n"
"v_mul_f32 v33, v45, v45\n"
"v_mul_f32 v34, v46, v46\n"
"v_mul_f32 v35, v47, v47\n"
"v_fma_f32 v32, v32, v19, v15\n"
"v_fma_f32 v33, v33, v19, v15\n"
"v_fma_f32 v34, v34, v19, v15\n"
"v_fma_f32 v35, v35, v19, v15\n"
"v_mul_f32 v32, v32, v44\n"
"v_mul_f32 v33, v33, v45\n"
"v_mul_f32 v34, v34, v46\n"
"v_mul_f32 v35, v35, v47\n"
"v_exp_f32 v32, v32\n"
"v_exp_f32 v33, v33\n"
"v_exp_f32 v34, v34\n"
"v_exp_f32 v35, v35\n"
"v_add_f32 v32, 1.0, v32\n"
"v_add_f32 v33, 1.0, v33\n"
"v_add_f32 v34, 1.0, v34\n"
"v_add_f32 v35, 1.0, v35\n"
"v_rcp_f32 v32, v32\n"
"v_rcp_f32 v33, v33\n"
"v_rcp_f32 v34, v34\n"
"v_rcp_f32 v35, v35\n"
"v_mul_f32 v32, v32, v44\n"
"v_mul_f32 v33, v33, v45\n"
"v_mul_f32 v34, v34, v46\n"
"v_mul_f32 v35, v35, v47\n"
"v_cvt_pk_bf16_f32 v36, v32, v33\n"
"v_cvt_pk_bf16_f32 v37, v34, v35\n"
"ds_write_b64 v7, v[36:37] offset:1024\n"
"v_mul_f32 v24, v48, v48\n"
"v_mul_f32 v25, v49, v49\n"
"v_mul_f32 v26, v50, v50\n"
"v_mul_f32 v27, v51, v51\n"
"v_fma_f32 v24, v24, v20, v16\n"
"v_fma_f32 v25, v25, v20, v16\n"
"v_fma_f32 v26, v26, v20, v16\n"
"v_fma_f32 v27, v27, v20, v16\n"
"v_mul_f32 v24, v24, v48\n"
"v_mul_f32 v25, v25, v49\n"
"v_mul_f32 v26, v26, v50\n"
"v_mul_f32 v27, v27, v51\n"
"v_exp_f32 v24, v24\n"
"v_exp_f32 v25, v25\n"
"v_exp_f32 v26, v26\n"
"v_exp_f32 v27, v27\n"
"v_add_f32 v24, 1.0, v24\n"
"v_add_f32 v25, 1.0, v25\n"
"v_add_f32 v26, 1.0, v26\n"
"v_add_f32 v27, 1.0, v27\n"
"v_rcp_f32 v24, v24\n"
"v_rcp_f32 v25, v25\n"
"v_rcp_f32 v26, v26\n"
"v_rcp_f32 v27, v27\n"
"v_mul_f32 v24, v24, v48\n"
"v_mul_f32 v25, v25, v49\n"
"v_mul_f32 v26, v26, v50\n"
"v_mul_f32 v27, v27, v51\n"
"v_cvt_pk_bf16_f32 v28, v24, v25\n"
"v_cvt_pk_bf16_f32 v29, v26, v27\n"
"ds_write_b64 v7, v[28:29] offset:2048\n"
"v_mul_f32 v32, v52, v52\n"
"v_mul_f32 v33, v53, v53\n"
"v_mul_f32 v34, v54, v54\n"
"v_mul_f32 v35, v55, v55\n"
"v_fma_f32 v32, v32, v21, v17\n"
"v_fma_f32 v33, v33, v21, v17\n"
"v_fma_f32 v34, v34, v21, v17\n"
"v_fma_f32 v35, v35, v21, v17\n"
"v_mul_f32 v32, v32, v52\n"
"v_mul_f32 v33, v33, v53\n"
"v_mul_f32 v34, v34, v54\n"
"v_mul_f32 v35, v35, v55\n"
"v_exp_f32 v32, v32\n"
"v_exp_f32 v33, v33\n"
"v_exp_f32 v34, v34\n"
"v_exp_f32 v35, v35\n"
"v_add_f32 v32, 1.0, v32\n"
"v_add_f32 v33, 1.0, v33\n"
"v_add_f32 v34, 1.0, v34\n"
"v_add_f32 v35, 1.0, v35\n"
"v_rcp_f32 v32, v32\n"
"v_rcp_f32 v33, v33\n"
"v_rcp_f32 v34, v34\n"
"v_rcp_f32 v35, v35\n"
"v_mul_f32 v32, v32, v52\n"
"v_mul_f32 v33, v33, v53\n"
"v_mul_f32 v34, v34, v54\n"
"v_mul_f32 v35, v35, v55\n"
"v_cvt_pk_bf16_f32 v36, v32, v33\n"
"v_cvt_pk_bf16_f32 v37, v34, v35\n"
"ds_write_b64 v7, v[36:37] offset:3072\n"
"v_add_u32 v7, 8192, v7\n"
"s_waitcnt lgkmcnt(0)\n"
"s_barrier\n"
"v_mfma_f32_16x16x32_bf16 v[56:59], v[200:203], a[224:227], v[56:59]\n"
"ds_read_b128 v[224:227], v12 offset:16384\n"
"v_mfma_f32_16x16x32_bf16 v[60:63], v[200:203], a[228:231], v[60:63]\n"
"ds_read_b128 v[228:231], v12 offset:17408\n"
"v_mfma_f32_16x16x32_bf16 v[64:67], v[200:203], a[232:235], v[64:67]\n"
"ds_read_b128 v[232:235], v12 offset:18432\n"
"v_mfma_f32_16x16x32_bf16 v[68:71], v[200:203], a[236:239], v[68:71]\n"
"ds_read_b128 v[236:239], v12 offset:19456\n"
"global_load_dwordx4 v[200:203], v5, s[94:95] offset:2048 sc1\n"
"v_mfma_f32_16x16x32_bf16 v[56:59], v[204:207], a[240:243], v[56:59]\n"
"ds_read_b128 v[240:243], v12 offset:20480\n"
"v_mfma_f32_16x16x32_bf16 v[60:63], v[204:207], a[244:247], v[60:63]\n"
"ds_read_b128 v[244:247], v12 offset:21504\n"
"v_mfma_f32_16x16x32_bf16 v[64:67], v[204:207], a[248:251], v[64:67]\n"
"s_waitcnt vmcnt(33)\n"
"ds_read_b128 v[248:251], v12 offset:22528\n"
"v_mfma_f32_16x16x32_bf16 v[68:71], v[204:207], a[252:255], v[68:71]\n"
"s_waitcnt lgkmcnt(3)\n"
"ds_read_b128 v[252:255], v12 offset:23552\n"
"global_load_dwordx4 v[204:207], v5, s[94:95] offset:3072 sc1\n"
"v_mfma_f32_16x16x32_bf16 v[56:59], v[208:211], v[224:227], v[56:59]\n"
"v_mfma_f32_16x16x32_bf16 v[60:63], v[208:211], v[228:231], v[60:63]\n"
"v_mfma_f32_16x16x32_bf16 v[64:67], v[208:211], v[232:235], v[64:67]\n"
"v_mfma_f32_16x16x32_bf16 v[68:71], v[208:211], v[236:239], v[68:71]\n"
"s_waitcnt lgkmcnt(0)\n"
"global_load_dwordx4 v[208:211], v6, s[94:95] offset:-4096 sc1\n"
"v_mfma_f32_16x16x32_bf16 v[56:59], v[212:215], v[240:243], v[56:59]\n"
"v_mfma_f32_16x16x32_bf16 v[60:63], v[212:215], v[244:247], v[60:63]\n"
"v_mfma_f32_16x16x32_bf16 v[64:67], v[212:215], v[248:251], v[64:67]\n"
"v_mfma_f32_16x16x32_bf16 v[68:71], v[212:215], v[252:255], v[68:71]\n"
"global_load_dwordx4 v[212:215], v6, s[94:95] offset:-3072 sc1\n"
"s_add_u32 s94, s94, 18432\n"
"s_addc_u32 s95, s95, 0\n"
"s_waitcnt vmcnt(34)\n"
"v_mfma_f32_16x16x32_bf16 v[40:43], v[72:75], a[0:3], 0\n"
"v_mfma_f32_16x16x32_bf16 v[44:47], v[72:75], a[4:7], 0\n"
"v_mfma_f32_16x16x32_bf16 v[48:51], v[72:75], a[8:11], 0\n"
"v_mfma_f32_16x16x32_bf16 v[52:55], v[72:75], a[12:15], 0\n"
"global_load_dwordx4 v[72:75], v4, s[94:95] offset:-4096 sc1\n"
"s_lshl_b32 s80, s29, 13\n"
"v_cvt_pk_bf16_f32 v24, v56, v57\n"
"s_add_u32 s80, s80, s74\n"
"v_mfma_f32_16x16x32_bf16 v[40:43], v[76:79], a[16:19], v[40:43]\n"
"v_cvt_pk_bf16_f32 v25, v58, v59\n"
"v_cvt_pk_bf16_f32 v26, v60, v61\n"
"v_mfma_f32_16x16x32_bf16 v[44:47], v[76:79], a[20:23], v[44:47]\n"
"v_cvt_pk_bf16_f32 v27, v62, v63\n"
"v_cvt_pk_bf16_f32 v28, v64, v65\n"
"v_mfma_f32_16x16x32_bf16 v[48:51], v[76:79], a[24:27], v[48:51]\n"
"s_waitcnt vmcnt(33)\n"
"v_cvt_pk_bf16_f32 v29, v66, v67\n"
"v_cvt_pk_bf16_f32 v30, v68, v69\n"
"v_mfma_f32_16x16x32_bf16 v[52:55], v[76:79], a[28:31], v[52:55]\n"
"v_cvt_pk_bf16_f32 v31, v70, v71\n"
"v_mfma_f32_16x16x32_bf16 v[40:43], v[80:83], a[32:35], v[40:43]\n"
"buffer_store_dwordx4 v[24:27], v1, s[8:11], s80 offen offset:0 sc1\n"
"v_mfma_f32_16x16x32_bf16 v[44:47], v[80:83], a[36:39], v[44:47]\n"
"buffer_store_dwordx4 v[28:31], v1, s[8:11], s80 offen offset:1024 sc1\n"
"v_mfma_f32_16x16x32_bf16 v[48:51], v[80:83], a[40:43], v[48:51]\n"
"v_mfma_f32_16x16x32_bf16 v[52:55], v[80:83], a[44:47], v[52:55]\n"
"v_mfma_f32_16x16x32_bf16 v[40:43], v[84:87], a[48:51], v[40:43]\n"
"v_mfma_f32_16x16x32_bf16 v[44:47], v[84:87], a[52:55], v[44:47]\n"
"v_mfma_f32_16x16x32_bf16 v[48:51], v[84:87], a[56:59], v[48:51]\n"
"s_waitcnt vmcnt(33)\n"
"v_mfma_f32_16x16x32_bf16 v[52:55], v[84:87], a[60:63], v[52:55]\n"
"v_mfma_f32_16x16x32_bf16 v[40:43], v[88:91], a[64:67], v[40:43]\n"
"v_mfma_f32_16x16x32_bf16 v[44:47], v[88:91], a[68:71], v[44:47]\n"
"v_mfma_f32_16x16x32_bf16 v[48:51], v[88:91], a[72:75], v[48:51]\n"
"v_mfma_f32_16x16x32_bf16 v[52:55], v[88:91], a[76:79], v[52:55]\n"
"global_load_dwordx4 v[88:91], v4, s[94:95] offset:-3072 sc1\n"
"v_mfma_f32_16x16x32_bf16 v[40:43], v[92:95], a[80:83], v[40:43]\n"
"v_mfma_f32_16x16x32_bf16 v[44:47], v[92:95], a[84:87], v[44:47]\n"
"v_mfma_f32_16x16x32_bf16 v[48:51], v[92:95], a[88:91], v[48:51]\n"
"s_waitcnt vmcnt(32)\n"
"v_mfma_f32_16x16x32_bf16 v[52:55], v[92:95], a[92:95], v[52:55]\n"
"global_load_dwordx4 v[92:95], v4, s[94:95] offset:-2048 sc1\n"
"v_mfma_f32_16x16x32_bf16 v[40:43], v[96:99], a[96:99], v[40:43]\n"
"v_mfma_f32_16x16x32_bf16 v[44:47], v[96:99], a[100:103], v[44:47]\n"
"v_mfma_f32_16x16x32_bf16 v[48:51], v[96:99], a[104:107], v[48:51]\n"
"v_mfma_f32_16x16x32_bf16 v[52:55], v[96:99], a[108:111], v[52:55]\n"
"global_load_dwordx4 v[96:99], v4, s[94:95] offset:-1024 sc1\n"
"v_mfma_f32_16x16x32_bf16 v[40:43], v[100:103], a[112:115], v[40:43]\n"
"v_mfma_f32_16x16x32_bf16 v[44:47], v[100:103], a[116:119], v[44:47]\n"
"v_mfma_f32_16x16x32_bf16 v[48:51], v[100:103], a[120:123], v[48:51]\n"
"s_waitcnt vmcnt(32)\n"
"v_mfma_f32_16x16x32_bf16 v[52:55], v[100:103], a[124:127], v[52:55]\n"
"global_load_dwordx4 v[100:103], v4, s[94:95] offset:0 sc1\n"
"v_mfma_f32_16x16x32_bf16 v[40:43], v[104:107], a[128:131], v[40:43]\n"
"v_mfma_f32_16x16x32_bf16 v[44:47], v[104:107], a[132:135], v[44:47]\n"
"v_mfma_f32_16x16x32_bf16 v[48:51], v[104:107], a[136:139], v[48:51]\n"
"v_mfma_f32_16x16x32_bf16 v[52:55], v[104:107], a[140:143], v[52:55]\n"
"global_load_dwordx4 v[104:107], v4, s[94:95] offset:1024 sc1\n"
"v_mfma_f32_16x16x32_bf16 v[40:43], v[108:111], a[144:147], v[40:43]\n"
"v_mfma_f32_16x16x32_bf16 v[44:47], v[108:111], a[148:151], v[44:47]\n"
"v_mfma_f32_16x16x32_bf16 v[48:51], v[108:111], a[152:155], v[48:51]\n"
"s_waitcnt vmcnt(32)\n"
"v_mfma_f32_16x16x32_bf16 v[52:55], v[108:111], a[156:159], v[52:55]\n"
"global_load_dwordx4 v[108:111], v4, s[94:95] offset:2048 sc1\n"
"v_mfma_f32_16x16x32_bf16 v[40:43], v[112:115], a[160:163], v[40:43]\n"
"v_mfma_f32_16x16x32_bf16 v[44:47], v[112:115], a[164:167], v[44:47]\n"
"v_mfma_f32_16x16x32_bf16 v[48:51], v[112:115], a[168:171], v[48:51]\n"
"v_mfma_f32_16x16x32_bf16 v[52:55], v[112:115], a[172:175], v[52:55]\n"
"global_load_dwordx4 v[112:115], v4, s[94:95] offset:3072 sc1\n"
"v_mfma_f32_16x16x32_bf16 v[40:43], v[116:119], a[176:179], v[40:43]\n"
"v_mfma_f32_16x16x32_bf16 v[44:47], v[116:119], a[180:183], v[44:47]\n"
"v_mfma_f32_16x16x32_bf16 v[48:51], v[116:119], a[184:187], v[48:51]\n"
"s_waitcnt vmcnt(32)\n"
"v_mfma_f32_16x16x32_bf16 v[52:55], v[116:119], a[188:191], v[52:55]\n"
"global_load_dwordx4 v[116:119], v5, s[94:95] offset:-4096 sc1\n"
"v_mfma_f32_16x16x32_bf16 v[40:43], v[120:123], a[192:195], v[40:43]\n"
"v_mfma_f32_16x16x32_bf16 v[44:47], v[120:123], a[196:199], v[44:47]\n"
"v_mfma_f32_16x16x32_bf16 v[48:51], v[120:123], a[200:203], v[48:51]\n"
"v_mfma_f32_16x16x32_bf16 v[52:55], v[120:123], a[204:207], v[52:55]\n"
"global_load_dwordx4 v[120:123], v5, s[94:95] offset:-3072 sc1\n"
"v_mfma_f32_16x16x32_bf16 v[40:43], v[124:127], a[208:211], v[40:43]\n"
"v_mfma_f32_16x16x32_bf16 v[44:47], v[124:127], a[212:215], v[44:47]\n"
"v_mfma_f32_16x16x32_bf16 v[48:51], v[124:127], a[216:219], v[48:51]\n"
"s_waitcnt vmcnt(32)\n"
"v_mfma_f32_16x16x32_bf16 v[52:55], v[124:127], a[220:223], v[52:55]\n"
"global_load_dwordx4 v[124:127], v5, s[94:95] offset:-2048 sc1\n"
"v_mfma_f32_16x16x32_bf16 v[40:43], v[128:131], a[224:227], v[40:43]\n"
"v_mfma_f32_16x16x32_bf16 v[44:47], v[128:131], a[228:231], v[44:47]\n"
"v_mfma_f32_16x16x32_bf16 v[48:51], v[128:131], a[232:235], v[48:51]\n"
"v_mfma_f32_16x16x32_bf16 v[52:55], v[128:131], a[236:239], v[52:55]\n"
"global_load_dwordx4 v[128:131], v5, s[94:95] offset:-1024 sc1\n"
"v_mfma_f32_16x16x32_bf16 v[40:43], v[132:135], a[240:243], v[40:43]\n"
"v_mfma_f32_16x16x32_bf16 v[44:47], v[132:135], a[244:247], v[44:47]\n"
"v_mfma_f32_16x16x32_bf16 v[48:51], v[132:135], a[248:251], v[48:51]\n"
"s_waitcnt vmcnt(32)\n"
"v_mfma_f32_16x16x32_bf16 v[52:55], v[132:135], a[252:255], v[52:55]\n"
"global_load_dwordx4 v[132:135], v5, s[94:95] offset:0 sc1\n"
"v_mfma_f32_16x16x32_bf16 v[40:43], v[136:139], v[224:227], v[40:43]\n"
"v_mfma_f32_16x16x32_bf16 v[44:47], v[136:139], v[228:231], v[44:47]\n"
"v_mfma_f32_16x16x32_bf16 v[48:51], v[136:139], v[232:235], v[48:51]\n"
"v_mfma_f32_16x16x32_bf16 v[52:55], v[136:139], v[236:239], v[52:55]\n"
"global_load_dwordx4 v[136:139], v5, s[94:95] offset:1024 sc1\n"
"v_mfma_f32_16x16x32_bf16 v[40:43], v[140:143], v[240:243], v[40:43]\n"
"v_mfma_f32_16x16x32_bf16 v[44:47], v[140:143], v[244:247], v[44:47]\n"
"v_mfma_f32_16x16x32_bf16 v[48:51], v[140:143], v[248:251], v[48:51]\n"
"v_mfma_f32_16x16x32_bf16 v[52:55], v[140:143], v[252:255], v[52:55]\n"
"global_load_dwordx4 v[140:143], v5, s[94:95] offset:2048 sc1\n"
"s_waitcnt vmcnt(33)\n"
"v_mfma_f32_16x16x32_bf16 v[56:59], v[144:147], a[0:3], 0\n"
"v_mfma_f32_16x16x32_bf16 v[60:63], v[144:147], a[4:7], 0\n"
"v_mfma_f32_16x16x32_bf16 v[64:67], v[144:147], a[8:11], 0\n"
"v_mfma_f32_16x16x32_bf16 v[68:71], v[144:147], a[12:15], 0\n"
"global_load_dwordx4 v[144:147], v5, s[94:95] offset:3072 sc1\n"
"s_add_u32 s80, s29, 1\n"
"v_cvt_pk_bf16_f32 v24, v40, v41\n"
"s_and_b32 s80, s80, 7\n"
"v_mfma_f32_16x16x32_bf16 v[56:59], v[148:151], a[16:19], v[56:59]\n"
"v_cvt_pk_bf16_f32 v25, v42, v43\n"
"s_mul_i32 s80, s80, 57344\n"
"v_cvt_pk_bf16_f32 v26, v44, v45\n"
"s_add_u32 s80, s80, s72\n"
"v_mfma_f32_16x16x32_bf16 v[60:63], v[148:151], a[20:23], v[60:63]\n"
"v_cvt_pk_bf16_f32 v27, v46, v47\n"
"s_add_u32 s80, s80, 0\n"
"v_cvt_pk_bf16_f32 v28, v48, v49\n"
"v_mfma_f32_16x16x32_bf16 v[64:67], v[148:151], a[24:27], v[64:67]\n"
"s_waitcnt vmcnt(32)\n"
"v_cvt_pk_bf16_f32 v29, v50, v51\n"
"v_cvt_pk_bf16_f32 v30, v52, v53\n"
"v_mfma_f32_16x16x32_bf16 v[68:71], v[148:151], a[28:31], v[68:71]\n"
"v_cvt_pk_bf16_f32 v31, v54, v55\n"
"v_mfma_f32_16x16x32_bf16 v[56:59], v[152:155], a[32:35], v[56:59]\n"
"buffer_store_dwordx4 v[24:27], v1, s[8:11], s80 offen offset:0 sc1\n"
"v_mfma_f32_16x16x32_bf16 v[60:63], v[152:155], a[36:39], v[60:63]\n"
"buffer_store_dwordx4 v[28:31], v1, s[8:11], s80 offen offset:1024 sc1\n"
"v_mfma_f32_16x16x32_bf16 v[64:67], v[152:155], a[40:43], v[64:67]\n"
"v_mfma_f32_16x16x32_bf16 v[68:71], v[152:155], a[44:47], v[68:71]\n"
"v_mfma_f32_16x16x32_bf16 v[56:59], v[156:159], a[48:51], v[56:59]\n"
"v_mfma_f32_16x16x32_bf16 v[60:63], v[156:159], a[52:55], v[60:63]\n"
"v_mfma_f32_16x16x32_bf16 v[64:67], v[156:159], a[56:59], v[64:67]\n"
"s_waitcnt vmcnt(32)\n"
"v_mfma_f32_16x16x32_bf16 v[68:71], v[156:159], a[60:63], v[68:71]\n"
"v_mfma_f32_16x16x32_bf16 v[56:59], v[160:163], a[64:67], v[56:59]\n"
"v_mfma_f32_16x16x32_bf16 v[60:63], v[160:163], a[68:71], v[60:63]\n"
"v_mfma_f32_16x16x32_bf16 v[64:67], v[160:163], a[72:75], v[64:67]\n"
"v_mfma_f32_16x16x32_bf16 v[68:71], v[160:163], a[76:79], v[68:71]\n"
"global_load_dwordx4 v[156:159], v6, s[94:95] offset:-4096 sc1\n"
"v_mfma_f32_16x16x32_bf16 v[56:59], v[164:167], a[80:83], v[56:59]\n"
"v_mfma_f32_16x16x32_bf16 v[60:63], v[164:167], a[84:87], v[60:63]\n"
"v_mfma_f32_16x16x32_bf16 v[64:67], v[164:167], a[88:91], v[64:67]\n"
"s_waitcnt vmcnt(31)\n"
"v_mfma_f32_16x16x32_bf16 v[68:71], v[164:167], a[92:95], v[68:71]\n"
"global_load_dwordx4 v[160:163], v6, s[94:95] offset:-3072 sc1\n"
"s_add_u32 s94, s94, 18432\n"
"s_addc_u32 s95, s95, 0\n"
"v_mfma_f32_16x16x32_bf16 v[56:59], v[168:171], a[96:99], v[56:59]\n"
"v_mfma_f32_16x16x32_bf16 v[60:63], v[168:171], a[100:103], v[60:63]\n"
"v_mfma_f32_16x16x32_bf16 v[64:67], v[168:171], a[104:107], v[64:67]\n"
"v_mfma_f32_16x16x32_bf16 v[68:71], v[168:171], a[108:111], v[68:71]\n"
"global_load_dwordx4 v[164:167], v4, s[94:95] offset:-4096 sc1\n"
"v_mfma_f32_16x16x32_bf16 v[56:59], v[172:175], a[112:115], v[56:59]\n"
"v_mfma_f32_16x16x32_bf16 v[60:63], v[172:175], a[116:119], v[60:63]\n"
"v_mfma_f32_16x16x32_bf16 v[64:67], v[172:175], a[120:123], v[64:67]\n"
"s_waitcnt vmcnt(31)\n"
"v_mfma_f32_16x16x32_bf16 v[68:71], v[172:175], a[124:127], v[68:71]\n"
"global_load_dwordx4 v[168:171], v4, s[94:95] offset:-3072 sc1\n"
"v_mfma_f32_16x16x32_bf16 v[56:59], v[176:179], a[128:131], v[56:59]\n"
"v_mfma_f32_16x16x32_bf16 v[60:63], v[176:179], a[132:135], v[60:63]\n"
"v_mfma_f32_16x16x32_bf16 v[64:67], v[176:179], a[136:139], v[64:67]\n"
"v_mfma_f32_16x16x32_bf16 v[68:71], v[176:179], a[140:143], v[68:71]\n"
"global_load_dwordx4 v[172:175], v4, s[94:95] offset:-2048 sc1\n"
"v_mfma_f32_16x16x32_bf16 v[56:59], v[180:183], a[144:147], v[56:59]\n"
"v_mfma_f32_16x16x32_bf16 v[60:63], v[180:183], a[148:151], v[60:63]\n"
"v_mfma_f32_16x16x32_bf16 v[64:67], v[180:183], a[152:155], v[64:67]\n"
"s_waitcnt vmcnt(31)\n"
"v_mfma_f32_16x16x32_bf16 v[68:71], v[180:183], a[156:159], v[68:71]\n"
"global_load_dwordx4 v[176:179], v4, s[94:95] offset:-1024 sc1\n"
"v_mfma_f32_16x16x32_bf16 v[56:59], v[184:187], a[160:163], v[56:59]\n"
"v_mfma_f32_16x16x32_bf16 v[60:63], v[184:187], a[164:167], v[60:63]\n"
"v_mfma_f32_16x16x32_bf16 v[64:67], v[184:187], a[168:171], v[64:67]\n"
"v_mfma_f32_16x16x32_bf16 v[68:71], v[184:187], a[172:175], v[68:71]\n"
"global_load_dwordx4 v[180:183], v4, s[94:95] offset:0 sc1\n"
"v_mfma_f32_16x16x32_bf16 v[56:59], v[188:191], a[176:179], v[56:59]\n"
"v_mfma_f32_16x16x32_bf16 v[60:63], v[188:191], a[180:183], v[60:63]\n"
"v_mfma_f32_16x16x32_bf16 v[64:67], v[188:191], a[184:187], v[64:67]\n"
"s_waitcnt vmcnt(31)\n"
"v_mfma_f32_16x16x32_bf16 v[68:71], v[188:191], a[188:191], v[68:71]\n"
"global_load_dwordx4 v[184:187], v4, s[94:95] offset:1024 sc1\n"
"v_mfma_f32_16x16x32_bf16 v[56:59], v[192:195], a[192:195], v[56:59]\n"
"v_mfma_f32_16x16x32_bf16 v[60:63], v[192:195], a[196:199], v[60:63]\n"
"v_mfma_f32_16x16x32_bf16 v[64:67], v[192:195], a[200:203], v[64:67]\n"
"v_mfma_f32_16x16x32_bf16 v[68:71], v[192:195], a[204:207], v[68:71]\n"
"global_load_dwordx4 v[188:191], v4, s[94:95] offset:2048 sc1\n"
"v_mfma_f32_16x16x32_bf16 v[56:59], v[196:199], a[208:211], v[56:59]\n"
"v_mfma_f32_16x16x32_bf16 v[60:63], v[196:199], a[212:215], v[60:63]\n"
"v_mfma_f32_16x16x32_bf16 v[64:67], v[196:199], a[216:219], v[64:67]\n"
"s_waitcnt vmcnt(31)\n"
"v_mfma_f32_16x16x32_bf16 v[68:71], v[196:199], a[220:223], v[68:71]\n"
"global_load_dwordx4 v[192:195], v4, s[94:95] offset:3072 sc1\n"
"v_mfma_f32_16x16x32_bf16 v[56:59], v[200:203], a[224:227], v[56:59]\n"
"v_mfma_f32_16x16x32_bf16 v[60:63], v[200:203], a[228:231], v[60:63]\n"
"v_mfma_f32_16x16x32_bf16 v[64:67], v[200:203], a[232:235], v[64:67]\n"
"v_mfma_f32_16x16x32_bf16 v[68:71], v[200:203], a[236:239], v[68:71]\n"
"global_load_dwordx4 v[196:199], v5, s[94:95] offset:-4096 sc1\n"
"v_mfma_f32_16x16x32_bf16 v[56:59], v[204:207], a[240:243], v[56:59]\n"
"v_mfma_f32_16x16x32_bf16 v[60:63], v[204:207], a[244:247], v[60:63]\n"
"v_mfma_f32_16x16x32_bf16 v[64:67], v[204:207], a[248:251], v[64:67]\n"
"s_waitcnt vmcnt(31)\n"
"v_mfma_f32_16x16x32_bf16 v[68:71], v[204:207], a[252:255], v[68:71]\n"
"global_load_dwordx4 v[200:203], v5, s[94:95] offset:-3072 sc1\n"
"v_mfma_f32_16x16x32_bf16 v[56:59], v[208:211], v[224:227], v[56:59]\n"
"v_mfma_f32_16x16x32_bf16 v[60:63], v[208:211], v[228:231], v[60:63]\n"
"v_mfma_f32_16x16x32_bf16 v[64:67], v[208:211], v[232:235], v[64:67]\n"
"v_mfma_f32_16x16x32_bf16 v[68:71], v[208:211], v[236:239], v[68:71]\n"
"global_load_dwordx4 v[204:207], v5, s[94:95] offset:-2048 sc1\n"
"v_mfma_f32_16x16x32_bf16 v[56:59], v[212:215], v[240:243], v[56:59]\n"
"v_mfma_f32_16x16x32_bf16 v[60:63], v[212:215], v[244:247], v[60:63]\n"
"v_mfma_f32_16x16x32_bf16 v[64:67], v[212:215], v[248:251], v[64:67]\n"
"v_mfma_f32_16x16x32_bf16 v[68:71], v[212:215], v[252:255], v[68:71]\n"
"global_load_dwordx4 v[208:211], v5, s[94:95] offset:-1024 sc1\n"
"s_waitcnt vmcnt(30)\n"
"v_mfma_f32_16x16x32_bf16 v[40:43], v[72:75], a[0:3], 0\n"
"v_mfma_f32_16x16x32_bf16 v[44:47], v[72:75], a[4:7], 0\n"
"v_mfma_f32_16x16x32_bf16 v[48:51], v[72:75], a[8:11], 0\n"
"v_mfma_f32_16x16x32_bf16 v[52:55], v[72:75], a[12:15], 0\n"
"global_load_dwordx4 v[212:215], v5, s[94:95] offset:0 sc1\n"
"s_add_u32 s80, s29, 2\n"
"v_cvt_pk_bf16_f32 v24, v56, v57\n"
"s_and_b32 s80, s80, 7\n"
"v_mfma_f32_16x16x32_bf16 v[40:43], v[88:91], a[16:19], v[40:43]\n"
"v_cvt_pk_bf16_f32 v25, v58, v59\n"
"s_mul_i32 s80, s80, 57344\n"
"v_cvt_pk_bf16_f32 v26, v60, v61\n"
"s_add_u32 s80, s80, s72\n"
"v_mfma_f32_16x16x32_bf16 v[44:47], v[88:91], a[20:23], v[44:47]\n"
"v_cvt_pk_bf16_f32 v27, v62, v63\n"
"s_add_u32 s80, s80, 8192\n"
"v_cvt_pk_bf16_f32 v28, v64, v65\n"
"v_mfma_f32_16x16x32_bf16 v[48:51], v[88:91], a[24:27], v[48:51]\n"
"s_waitcnt vmcnt(29)\n"
"v_cvt_pk_bf16_f32 v29, v66, v67\n"
"v_cvt_pk_bf16_f32 v30, v68, v69\n"
"v_mfma_f32_16x16x32_bf16 v[52:55], v[88:91], a[28:31], v[52:55]\n"
"v_cvt_pk_bf16_f32 v31, v70, v71\n"
"v_mfma_f32_16x16x32_bf16 v[40:43], v[92:95], a[32:35], v[40:43]\n"
"buffer_store_dwordx4 v[24:27], v1, s[8:11], s80 offen offset:0 sc1\n"
"v_mfma_f32_16x16x32_bf16 v[44:47], v[92:95], a[36:39], v[44:47]\n"
"buffer_store_dwordx4 v[28:31], v1, s[8:11], s80 offen offset:1024 sc1\n"
"s_add_u32 s80, s29, 1\n"
"s_and_b32 s80, s80, 7\n"
"s_lshl_b32 s80, s80, 11\n"
"s_add_u32 s80, s80, s75\n"
"s_waitcnt vmcnt(17)\n"
"v_mfma_f32_16x16x32_bf16 v[48:51], v[92:95], a[40:43], v[48:51]\n"
"buffer_store_dwordx2 v[22:23], v220, s[8:11], s80 offen offset:776 sc1\n"
"s_sub_u32 s81, s29, 0\n"
"s_and_b32 s81, s81, 7\n"
"s_lshl_b32 s81, s81, 13\n"
"s_add_u32 s81, s81, s74\n"
"v_mfma_f32_16x16x32_bf16 v[52:55], v[92:95], a[44:47], v[52:55]\n"
"buffer_load_dwordx2 v[32:33], v13, s[8:11], s81 offen sc1\n"
"v_mfma_f32_16x16x32_bf16 v[40:43], v[96:99], a[48:51], v[40:43]\n"
"v_mfma_f32_16x16x32_bf16 v[44:47], v[96:99], a[52:55], v[44:47]\n"
"v_mfma_f32_16x16x32_bf16 v[48:51], v[96:99], a[56:59], v[48:51]\n"
"s_waitcnt vmcnt(31)\n"
"v_mfma_f32_16x16x32_bf16 v[52:55], v[96:99], a[60:63], v[52:55]\n"
"v_mfma_f32_16x16x32_bf16 v[40:43], v[100:103], a[64:67], v[40:43]\n"
"v_mfma_f32_16x16x32_bf16 v[44:47], v[100:103], a[68:71], v[44:47]\n"
"v_mfma_f32_16x16x32_bf16 v[48:51], v[100:103], a[72:75], v[48:51]\n"
"v_mfma_f32_16x16x32_bf16 v[52:55], v[100:103], a[76:79], v[52:55]\n"
"global_load_dwordx4 v[72:75], v5, s[94:95] offset:1024 sc1\n"
"v_mfma_f32_16x16x32_bf16 v[40:43], v[104:107], a[80:83], v[40:43]\n"
"v_mfma_f32_16x16x32_bf16 v[44:47], v[104:107], a[84:87], v[44:47]\n"
"v_mfma_f32_16x16x32_bf16 v[48:51], v[104:107], a[88:91], v[48:51]\n"
"s_waitcnt vmcnt(30)\n"
"v_mfma_f32_16x16x32_bf16 v[52:55], v[104:107], a[92:95], v[52:55]\n"
"global_load_dwordx4 v[88:91], v5, s[94:95] offset:2048 sc1\n"
"v_mfma_f32_16x16x32_bf16 v[40:43], v[108:111], a[96:99], v[40:43]\n"
"v_mfma_f32_16x16x32_bf16 v[44:47], v[108:111], a[100:103], v[44:47]\n"
"v_mfma_f32_16x16x32_bf16 v[48:51], v[108:111], a[104:107], v[48:51]\n"
"v_mfma_f32_16x16x32_bf16 v[52:55], v[108:111], a[108:111], v[52:55]\n"
"global_load_dwordx4 v[92:95], v5, s[94:95] offset:3072 sc1\n"
"v_mfma_f32_16x16x32_bf16 v[40:43], v[112:115], a[112:115], v[40:43]\n"
"v_mfma_f32_16x16x32_bf16 v[44:47], v[112:115], a[116:119], v[44:47]\n"
"v_mfma_f32_16x16x32_bf16 v[48:51], v[112:115], a[120:123], v[48:51]\n"
"s_waitcnt vmcnt(30)\n"
"v_mfma_f32_16x16x32_bf16 v[52:55], v[112:115], a[124:127], v[52:55]\n"
"global_load_dwordx4 v[96:99], v6, s[94:95] offset:-4096 sc1\n"
"v_mfma_f32_16x16x32_bf16 v[40:43], v[116:119], a[128:131], v[40:43]\n"
"v_mfma_f32_16x16x32_bf16 v[44:47], v[116:119], a[132:135], v[44:47]\n"
"v_mfma_f32_16x16x32_bf16 v[48:51], v[116:119], a[136:139], v[48:51]\n"
"v_mfma_f32_16x16x32_bf16 v[52:55], v[116:119], a[140:143], v[52:55]\n"
"global_load_dwordx4 v[100:103], v6, s[94:95] offset:-3072 sc1\n"
"s_add_u32 s94, s94, 18432\n"
"s_addc_u32 s95, s95, 0\n"
"v_mfma_f32_16x16x32_bf16 v[40:43], v[120:123], a[144:147], v[40:43]\n"
"v_mfma_f32_16x16x32_bf16 v[44:47], v[120:123], a[148:151], v[44:47]\n"
"v_mfma_f32_16x16x32_bf16 v[48:51], v[120:123], a[152:155], v[48:51]\n"
"s_waitcnt vmcnt(30)\n"
"v_mfma_f32_16x16x32_bf16 v[52:55], v[120:123], a[156:159], v[52:55]\n"
"global_load_dwordx4 v[104:107], v4, s[94:95] offset:-4096 sc1\n"
"v_mfma_f32_16x16x32_bf16 v[40:43], v[124:127], a[160:163], v[40:43]\n"
"buffer_load_dwordx2 v[222:223], v221, s[8:11], s79 offen offset:768 sc1\n"
"v_mfma_f32_16x16x32_bf16 v[44:47], v[124:127], a[164:167], v[44:47]\n"
"v_mfma_f32_16x16x32_bf16 v[48:51], v[124:127], a[168:171], v[48:51]\n"
"v_mfma_f32_16x16x32_bf16 v[52:55], v[124:127], a[172:175], v[52:55]\n"
"global_load_dwordx4 v[108:111], v4, s[94:95] offset:-3072 sc1\n"
"v_mfma_f32_16x16x32_bf16 v[40:43], v[128:131], a[176:179], v[40:43]\n"
"v_mfma_f32_16x16x32_bf16 v[44:47], v[128:131], a[180:183], v[44:47]\n"
"v_mfma_f32_16x16x32_bf16 v[48:51], v[128:131], a[184:187], v[48:51]\n"
"s_waitcnt vmcnt(31)\n"
"v_mfma_f32_16x16x32_bf16 v[52:55], v[128:131], a[188:191], v[52:55]\n"
"global_load_dwordx4 v[112:115], v4, s[94:95] offset:-2048 sc1\n"
"v_mfma_f32_16x16x32_bf16 v[40:43], v[132:135], a[192:195], v[40:43]\n"
"v_mfma_f32_16x16x32_bf16 v[44:47], v[132:135], a[196:199], v[44:47]\n"
"v_mfma_f32_16x16x32_bf16 v[48:51], v[132:135], a[200:203], v[48:51]\n"
"v_mfma_f32_16x16x32_bf16 v[52:55], v[132:135], a[204:207], v[52:55]\n"
"global_load_dwordx4 v[116:119], v4, s[94:95] offset:-1024 sc1\n"
"v_mfma_f32_16x16x32_bf16 v[40:43], v[136:139], a[208:211], v[40:43]\n"
"v_mfma_f32_16x16x32_bf16 v[44:47], v[136:139], a[212:215], v[44:47]\n"
"v_mfma_f32_16x16x32_bf16 v[48:51], v[136:139], a[216:219], v[48:51]\n"
"s_waitcnt vmcnt(31)\n"
"v_mfma_f32_16x16x32_bf16 v[52:55], v[136:139], a[220:223], v[52:55]\n"
"global_load_dwordx4 v[120:123], v4, s[94:95] offset:0 sc1\n"
"v_mfma_f32_16x16x32_bf16 v[40:43], v[140:143], a[224:227], v[40:43]\n"
"v_mfma_f32_16x16x32_bf16 v[44:47], v[140:143], a[228:231], v[44:47]\n"
"v_mfma_f32_16x16x32_bf16 v[48:51], v[140:143], a[232:235], v[48:51]\n"
"v_mfma_f32_16x16x32_bf16 v[52:55], v[140:143], a[236:239], v[52:55]\n"
"global_load_dwordx4 v[124:127], v4, s[94:95] offset:1024 sc1\n"
"v_mfma_f32_16x16x32_bf16 v[40:43], v[144:147], a[240:243], v[40:43]\n"
"v_mfma_f32_16x16x32_bf16 v[44:47], v[144:147], a[244:247], v[44:47]\n"
"v_mfma_f32_16x16x32_bf16 v[48:51], v[144:147], a[248:251], v[48:51]\n"
"s_waitcnt vmcnt(29)\n"
"v_mfma_f32_16x16x32_bf16 v[52:55], v[144:147], a[252:255], v[52:55]\n"
"global_load_dwordx4 v[128:131], v4, s[94:95] offset:2048 sc1\n"
"v_mfma_f32_16x16x32_bf16 v[40:43], v[156:159], v[224:227], v[40:43]\n"
"v_mfma_f32_16x16x32_bf16 v[44:47], v[156:159], v[228:231], v[44:47]\n"
"v_mfma_f32_16x16x32_bf16 v[48:51], v[156:159], v[232:235], v[48:51]\n"
"v_mfma_f32_16x16x32_bf16 v[52:55], v[156:159], v[236:239], v[52:55]\n"
"global_load_dwordx4 v[132:135], v4, s[94:95] offset:3072 sc1\n"
"v_mfma_f32_16x16x32_bf16 v[40:43], v[160:163], v[240:243], v[40:43]\n"
"v_mfma_f32_16x16x32_bf16 v[44:47], v[160:163], v[244:247], v[44:47]\n"
"v_mfma_f32_16x16x32_bf16 v[48:51], v[160:163], v[248:251], v[48:51]\n"
"v_mfma_f32_16x16x32_bf16 v[52:55], v[160:163], v[252:255], v[52:55]\n"
"global_load_dwordx4 v[136:139], v5, s[94:95] offset:-4096 sc1\n"
"s_waitcnt vmcnt(30)\n"
"v_mfma_f32_16x16x32_bf16 v[56:59], v[164:167], a[0:3], 0\n"
"v_mfma_f32_16x16x32_bf16 v[60:63], v[164:167], a[4:7], 0\n"
"v_mfma_f32_16x16x32_bf16 v[64:67], v[164:167], a[8:11], 0\n"
"v_mfma_f32_16x16x32_bf16 v[68:71], v[164:167], a[12:15], 0\n"
"global_load_dwordx4 v[140:143], v5, s[94:95] offset:-3072 sc1\n"
"s_add_u32 s80, s29, 3\n"
"v_cvt_pk_bf16_f32 v24, v40, v41\n"
"s_and_b32 s80, s80, 7\n"
"v_mfma_f32_16x16x32_bf16 v[56:59], v[168:171], a[16:19], v[56:59]\n"
"v_cvt_pk_bf16_f32 v25, v42, v43\n"
"s_mul_i32 s80, s80, 57344\n"
"v_cvt_pk_bf16_f32 v26, v44, v45\n"
"s_add_u32 s80, s80, s72\n"
"v_mfma_f32_16x16x32_bf16 v[60:63], v[168:171], a[20:23], v[60:63]\n"
"v_cvt_pk_bf16_f32 v27, v46, v47\n"
"s_add_u32 s80, s80, 16384\n"
"v_cvt_pk_bf16_f32 v28, v48, v49\n"
"v_mfma_f32_16x16x32_bf16 v[64:67], v[168:171], a[24:27], v[64:67]\n"
"s_waitcnt vmcnt(29)\n"
"v_cvt_pk_bf16_f32 v29, v50, v51\n"
"v_cvt_pk_bf16_f32 v30, v52, v53\n"
"v_mfma_f32_16x16x32_bf16 v[68:71], v[168:171], a[28:31], v[68:71]\n"
"v_cvt_pk_bf16_f32 v31, v54, v55\n"
"v_mfma_f32_16x16x32_bf16 v[56:59], v[172:175], a[32:35], v[56:59]\n"
"buffer_store_dwordx4 v[24:27], v1, s[8:11], s80 offen offset:0 sc1\n"
"v_mfma_f32_16x16x32_bf16 v[60:63], v[172:175], a[36:39], v[60:63]\n"
"buffer_store_dwordx4 v[28:31], v1, s[8:11], s80 offen offset:1024 sc1\n"
"s_add_u32 s80, s29, 2\n"
"s_and_b32 s80, s80, 7\n"
"s_lshl_b32 s80, s80, 11\n"
"s_add_u32 s80, s80, s75\n"
"s_waitcnt vmcnt(20)\n"
"v_mfma_f32_16x16x32_bf16 v[64:67], v[172:175], a[40:43], v[64:67]\n"
"buffer_store_dwordx2 v[22:23], v220, s[8:11], s80 offen offset:784 sc1\n"
"s_waitcnt vmcnt(19)\n"
"v_mfma_f32_16x16x32_bf16 v[68:71], v[172:175], a[44:47], v[68:71]\n"
"v_lshlrev_b32 v152, 16, v32\n"
"v_and_b32 v153, 0xffff0000, v32\n"
"v_mfma_f32_16x16x32_bf16 v[56:59], v[176:179], a[48:51], v[56:59]\n"
"v_lshlrev_b32 v154, 16, v33\n"
"v_and_b32 v155, 0xffff0000, v33\n"
"s_waitcnt vmcnt(12)\n"
"v_mfma_f32_16x16x32_bf16 v[60:63], v[176:179], a[52:55], v[60:63]\n"
"v_cmp_ne_u64 vcc, s[24:25], v[222:223]\n"
"s_nop 0\n"
"s_bitcmp1_b32 vcc_lo, 1\n"
"s_cbranch_scc1 .Lfused_block_kernel_slow1\n"
".Lfused_block_kernel_back1:\n"
"s_sub_u32 s81, s29, 1\n"
"s_and_b32 s81, s81, 7\n"
"s_lshl_b32 s81, s81, 13\n"
"s_add_u32 s81, s81, s74\n"
"v_mfma_f32_16x16x32_bf16 v[64:67], v[176:179], a[56:59], v[64:67]\n"
"s_waitcnt vmcnt(30)\n"
"buffer_load_dwordx2 v[32:33], v13, s[8:11], s81 offen sc1\n"
"v_mfma_f32_16x16x32_bf16 v[68:71], v[176:179], a[60:63], v[68:71]\n"
"buffer_load_dwordx4 v[14:17], v1, s[8:11], s73 offen offset:0 sc1\n"
"v_mfma_f32_16x16x32_bf16 v[56:59], v[180:183], a[64:67], v[56:59]\n"
"buffer_load_dwordx4 v[18:21], v1, s[8:11], s73 offen offset:1024 sc1\n"
"s_add_u32 s73, s73, 8192\n"
"v_mfma_f32_16x16x32_bf16 v[60:63], v[180:183], a[68:71], v[60:63]\n"
"v_mfma_f32_16x16x32_bf16 v[64:67], v[180:183], a[72:75], v[64:67]\n"
"v_mfma_f32_16x16x32_bf16 v[68:71], v[180:183], a[76:79], v[68:71]\n"
"global_load_dwordx4 v[144:147], v5, s[94:95] offset:-2048 sc1\n"
"v_mfma_f32_16x16x32_bf16 v[56:59], v[184:187], a[80:83], v[56:59]\n"
"v_mfma_f32_16x16x32_bf16 v[60:63], v[184:187], a[84:87], v[60:63]\n"
"v_mfma_f32_16x16x32_bf16 v[64:67], v[184:187], a[88:91], v[64:67]\n"
"s_waitcnt vmcnt(32)\n"
"v_mfma_f32_16x16x32_bf16 v[68:71], v[184:187], a[92:95], v[68:71]\n"
"global_load_dwordx4 v[156:159], v5, s[94:95] offset:-1024 sc1\n"
"v_mfma_f32_16x16x32_bf16 v[56:59], v[188:191], a[96:99], v[56:59]\n"
"v_mfma_f32_16x16x32_bf16 v[60:63], v[188:191], a[100:103], v[60:63]\n"
"v_mfma_f32_16x16x32_bf16 v[64:67], v[188:191], a[104:107], v[64:67]\n"
"v_mfma_f32_16x16x32_bf16 v[68:71], v[188:191], a[108:111], v[68:71]\n"
"global_load_dwordx4 v[160:163], v5, s[94:95] offset:0 sc1\n"
"v_mfma_f32_16x16x32_bf16 v[56:59], v[192:195], a[112:115], v[56:59]\n"
"v_mfma_f32_16x16x32_bf16 v[60:63], v[192:195], a[116:119], v[60:63]\n"
"v_mfma_f32_16x16x32_bf16 v[64:67], v[192:195], a[120:123], v[64:67]\n"
"s_waitcnt vmcnt(32)\n"
"v_mfma_f32_16x16x32_bf16 v[68:71], v[192:195], a[124:127], v[68:71]\n"
"global_load_dwordx4 v[164:167], v5, s[94:95] offset:1024 sc1\n"
"v_mfma_f32_16x16x32_bf16 v[56:59], v[196:199], a[128:131], v[56:59]\n"
"v_mfma_f32_16x16x32_bf16 v[60:63], v[196:199], a[132:135], v[60:63]\n"
"v_mfma_f32_16x16x32_bf16 v[64:67], v[196:199], a[136:139], v[64:67]\n"
"v_mfma_f32_16x16x32_bf16 v[68:71], v[196:199], a[140:143], v[68:71]\n"
"global_load_dwordx4 v[168:171], v5, s[94:95] offset:2048 sc1\n"
"v_mfma_f32_16x16x32_bf16 v[56:59], v[200:203], a[144:147], v[56:59]\n"
"v_mfma_f32_16x16x32_bf16 v[60:63], v[200:203], a[148:151], v[60:63]\n"
"v_mfma_f32_16x16x32_bf16 v[64:67], v[200:203], a[152:155], v[64:67]\n"
"s_waitcnt vmcnt(32)\n"
"v_mfma_f32_16x16x32_bf16 v[68:71], v[200:203], a[156:159], v[68:71]\n"
"global_load_dwordx4 v[172:175], v5, s[94:95] offset:3072 sc1\n"
"v_mfma_f32_16x16x32_bf16 v[56:59], v[204:207], a[160:163], v[56:59]\n"
"buffer_load_dwordx2 v[222:223], v221, s[8:11], s79 offen offset:768 sc1\n"
"v_mfma_f32_16x16x32_bf16 v[60:63], v[204:207], a[164:167], v[60:63]\n"
"v_mfma_f32_16x16x32_bf16 v[64:67], v[204:207], a[168:171], v[64:67]\n"
"v_mfma_f32_16x16x32_bf16 v[68:71], v[204:207], a[172:175], v[68:71]\n"
"global_load_dwordx4 v[176:179], v6, s[94:95] offset:-4096 sc1\n"
"v_mfma_f32_16x16x32_bf16 v[56:59], v[208:211], a[176:179], v[56:59]\n"
"v_mfma_f32_16x16x32_bf16 v[60:63], v[208:211], a[180:183], v[60:63]\n"
"v_mfma_f32_16x16x32_bf16 v[64:67], v[208:211], a[184:187], v[64:67]\n"
"s_waitcnt vmcnt(29)\n"
"v_mfma_f32_16x16x32_bf16 v[68:71], v[208:211], a[188:191], v[68:71]\n"
"global_load_dwordx4 v[180:183], v6, s[94:95] offset:-3072 sc1\n"
"s_add_u32 s94, s94, 18432\n"
"s_addc_u32 s95, s95, 0\n"
"v_mfma_f32_16x16x32_bf16 v[56:59], v[212:215], a[192:195], v[56:59]\n"
"v_mfma_f32_16x16x32_bf16 v[60:63], v[212:215], a[196:199], v[60:63]\n"
"v_mfma_f32_16x16x32_bf16 v[64:67], v[212:215], a[200:203], v[64:67]\n"
"v_mfma_f32_16x16x32_bf16 v[68:71], v[212:215], a[204:207], v[68:71]\n"
"global_load_dwordx4 v[184:187], v4, s[94:95] offset:-4096 sc1\n"
"v_mfma_f32_16x16x32_bf16 v[56:59], v[72:75], a[208:211], v[56:59]\n"
"v_mfma_f32_16x16x32_bf16 v[60:63], v[72:75], a[212:215], v[60:63]\n"
"v_mfma_f32_16x16x32_bf16 v[64:67], v[72:75], a[216:219], v[64:67]\n"
"s_waitcnt vmcnt(29)\n"
"v_mfma_f32_16x16x32_bf16 v[68:71], v[72:75], a[220:223], v[68:71]\n"
"global_load_dwordx4 v[188:191], v4, s[94:95] offset:-3072 sc1\n"
"v_mfma_f32_16x16x32_bf16 v[56:59], v[88:91], a[224:227], v[56:59]\n"
"v_mfma_f32_16x16x32_bf16 v[60:63], v[88:91], a[228:231], v[60:63]\n"
"v_mfma_f32_16x16x32_bf16 v[64:67], v[88:91], a[232:235], v[64:67]\n"
"v_mfma_f32_16x16x32_bf16 v[68:71], v[88:91], a[236:239], v[68:71]\n"
"global_load_dwordx4 v[192:195], v4, s[94:95] offset:-2048 sc1\n"
"v_mfma_f32_16x16x32_bf16 v[56:59], v[92:95], a[240:243], v[56:59]\n"
"v_mfma_f32_16x16x32_bf16 v[60:63], v[92:95], a[244:247], v[60:63]\n"
"v_mfma_f32_16x16x32_bf16 v[64:67], v[92:95], a[248:251], v[64:67]\n"
"s_waitcnt vmcnt(29)\n"
"v_mfma_f32_16x16x32_bf16 v[68:71], v[92:95], a[252:255], v[68:71]\n"
"global_load_dwordx4 v[196:199], v4, s[94:95] offset:-1024 sc1\n"
"v_mfma_f32_16x16x32_bf16 v[56:59], v[96:99], v[224:227], v[56:59]\n"
"v_mfma_f32_16x16x32_bf16 v[60:63], v[96:99], v[228:231], v[60:63]\n"
"v_mfma_f32_16x16x32_bf16 v[64:67], v[96:99], v[232:235], v[64:67]\n"
"v_mfma_f32_16x16x32_bf16 v[68:71], v[96:99], v[236:239], v[68:71]\n"
"global_load_dwordx4 v[200:203], v4, s[94:95] offset:0 sc1\n"
"v_mfma_f32_16x16x32_bf16 v[56:59], v[100:103], v[240:243], v[56:59]\n"
"v_mfma_f32_16x16x32_bf16 v[60:63], v[100:103], v[244:247], v[60:63]\n"
"v_mfma_f32_16x16x32_bf16 v[64:67], v[100:103], v[248:251], v[64:67]\n"
"v_mfma_f32_16x16x32_bf16 v[68:71], v[100:103], v[252:255], v[68:71]\n"
"global_load_dwordx4 v[204:207], v4, s[94:95] offset:1024 sc1\n"
"s_waitcnt vmcnt(29)\n"
"v_mfma_f32_16x16x32_bf16 v[40:43], v[104:107], a[0:3], 0\n"
"v_mfma_f32_16x16x32_bf16 v[44:47], v[104:107], a[4:7], 0\n"
"v_mfma_f32_16x16x32_bf16 v[48:51], v[104:107], a[8:11], 0\n"
"v_mfma_f32_16x16x32_bf16 v[52:55], v[104:107], a[12:15], 0\n"
"global_load_dwordx4 v[208:211], v4, s[94:95] offset:2048 sc1\n"
"s_add_u32 s80, s29, 4\n"
"v_cvt_pk_bf16_f32 v24, v56, v57\n"
"s_and_b32 s80, s80, 7\n"
"v_mfma_f32_16x16x32_bf16 v[40:43], v[108:111], a[16:19], v[40:43]\n"
"v_cvt_pk_bf16_f32 v25, v58, v59\n"
"s_mul_i32 s80, s80, 57344\n"
"v_cvt_pk_bf16_f32 v26, v60, v61\n"
"s_add_u32 s80, s80, s72\n"
"v_mfma_f32_16x16x32_bf16 v[44:47], v[108:111], a[20:23], v[44:47]\n"
"v_cvt_pk_bf16_f32 v27, v62, v63\n"
"s_add_u32 s80, s80, 24576\n"
"v_cvt_pk_bf16_f32 v28, v64, v65\n"
"v_mfma_f32_16x16x32_bf16 v[48:51], v[108:111], a[24:27], v[48:51]\n"
"s_waitcnt vmcnt(28)\n"
"v_cvt_pk_bf16_f32 v29, v66, v67\n"
"v_cvt_pk_bf16_f32 v30, v68, v69\n"
"v_mfma_f32_16x16x32_bf16 v[52:55], v[108:111], a[28:31], v[52:55]\n"
"v_cvt_pk_bf16_f32 v31, v70, v71\n"
"v_mfma_f32_16x16x32_bf16 v[40:43], v[112:115], a[32:35], v[40:43]\n"
"buffer_store_dwordx4 v[24:27], v1, s[8:11], s80 offen offset:0 sc1\n"
"v_mfma_f32_16x16x32_bf16 v[44:47], v[112:115], a[36:39], v[44:47]\n"
"buffer_store_dwordx4 v[28:31], v1, s[8:11], s80 offen offset:1024 sc1\n"
"s_add_u32 s80, s29, 3\n"
"s_and_b32 s80, s80, 7\n"
"s_lshl_b32 s80, s80, 11\n"
"s_add_u32 s80, s80, s75\n"
"s_waitcnt vmcnt(22)\n"
"v_mfma_f32_16x16x32_bf16 v[48:51], v[112:115], a[40:43], v[48:51]\n"
"buffer_store_dwordx2 v[22:23], v220, s[8:11], s80 offen offset:792 sc1\n"
"s_waitcnt vmcnt(19)\n"
"v_mfma_f32_16x16x32_bf16 v[52:55], v[112:115], a[44:47], v[52:55]\n"
"v_lshlrev_b32 v34, 16, v32\n"
"v_and_b32 v35, 0xffff0000, v32\n"
"v_mfma_f32_16x16x32_bf16 v[40:43], v[116:119], a[48:51], v[40:43]\n"
"v_add_f32 v152, v152, v34\n"
"v_add_f32 v153, v153, v35\n"
"v_mfma_f32_16x16x32_bf16 v[44:47], v[116:119], a[52:55], v[44:47]\n"
"v_lshlrev_b32 v36, 16, v33\n"
"v_and_b32 v37, 0xffff0000, v33\n"
"v_mfma_f32_16x16x32_bf16 v[48:51], v[116:119], a[56:59], v[48:51]\n"
"s_waitcnt vmcnt(29)\n"
"v_add_f32 v154, v154, v36\n"
"v_add_f32 v155, v155, v37\n"
"v_mfma_f32_16x16x32_bf16 v[52:55], v[116:119], a[60:63], v[52:55]\n"
"v_lshlrev_b32 v76, 16, v14\n"
"v_and_b32 v77, 0xffff0000, v14\n"
"v_mfma_f32_16x16x32_bf16 v[40:43], v[120:123], a[64:67], v[40:43]\n"
"v_lshlrev_b32 v78, 16, v15\n"
"v_and_b32 v79, 0xffff0000, v15\n"
"v_mfma_f32_16x16x32_bf16 v[44:47], v[120:123], a[68:71], v[44:47]\n"
"v_lshlrev_b32 v80, 16, v16\n"
"v_and_b32 v81, 0xffff0000, v16\n"
"v_mfma_f32_16x16x32_bf16 v[48:51], v[120:123], a[72:75], v[48:51]\n"
"v_lshlrev_b32 v82, 16, v17\n"
"v_and_b32 v83, 0xffff0000, v17\n"
"v_mfma_f32_16x16x32_bf16 v[52:55], v[120:123], a[76:79], v[52:55]\n"
"global_load_dwordx4 v[212:215], v4, s[94:95] offset:3072 sc1\n"
"v_lshlrev_b32 v84, 16, v18\n"
"v_mfma_f32_16x16x32_bf16 v[40:43], v[124:127], a[80:83], v[40:43]\n"
"v_and_b32 v85, 0xffff0000, v18\n"
"v_lshlrev_b32 v86, 16, v19\n"
"v_mfma_f32_16x16x32_bf16 v[44:47], v[124:127], a[84:87], v[44:47]\n"
"v_and_b32 v87, 0xffff0000, v19\n"
"v_lshlrev_b32 v148, 16, v20\n"
"v_mfma_f32_16x16x32_bf16 v[48:51], v[124:127], a[88:91], v[48:51]\n"
"s_waitcnt vmcnt(28)\n"
"v_and_b32 v149, 0xffff0000, v20\n"
"v_lshlrev_b32 v150, 16, v21\n"
"v_mfma_f32_16x16x32_bf16 v[52:55], v[124:127], a[92:95], v[52:55]\n"
"global_load_dwordx4 v[72:75], v5, s[94:95] offset:-4096 sc1\n"
"v_and_b32 v151, 0xffff0000, v21\n"
"s_waitcnt vmcnt(14)\n"
"v_mfma_f32_16x16x32_bf16 v[40:43], v[128:131], a[96:99], v[40:43]\n"
"v_cmp_ne_u64 vcc, s[24:25], v[222:223]\n"
"s_nop 0\n"
"s_bitcmp1_b32 vcc_lo, 2\n"
"s_cbranch_scc1 .Lfused_block_kernel_slow2\n"
".Lfused_block_kernel_back2:\n"
"s_sub_u32 s81, s29, 2\n"
"s_and_b32 s81, s81, 7\n"
"s_lshl_b32 s81, s81, 13\n"
"s_add_u32 s81, s81, s74\n"
"v_mfma_f32_16x16x32_bf16 v[44:47], v[128:131], a[100:103], v[44:47]\n"
"buffer_load_dwordx2 v[32:33], v13, s[8:11], s81 offen sc1\n"
"v_mfma_f32_16x16x32_bf16 v[48:51], v[128:131], a[104:107], v[48:51]\n"
"buffer_load_dwordx4 v[14:17], v1, s[8:11], s73 offen offset:0 sc1\n"
"v_mfma_f32_16x16x32_bf16 v[52:55], v[128:131], a[108:111], v[52:55]\n"
"global_load_dwordx4 v[88:91], v5, s[94:95] offset:-3072 sc1\n"
"v_mfma_f32_16x16x32_bf16 v[40:43], v[132:135], a[112:115], v[40:43]\n"
"buffer_load_dwordx4 v[18:21], v1, s[8:11], s73 offen offset:1024 sc1\n"
"s_add_u32 s73, s73, 8192\n"
"v_mfma_f32_16x16x32_bf16 v[44:47], v[132:135], a[116:119], v[44:47]\n"
"v_mfma_f32_16x16x32_bf16 v[48:51], v[132:135], a[120:123], v[48:51]\n"
"s_waitcnt vmcnt(31)\n"
"v_mfma_f32_16x16x32_bf16 v[52:55], v[132:135], a[124:127], v[52:55]\n"
"global_load_dwordx4 v[92:95], v5, s[94:95] offset:-2048 sc1\n"
"v_mfma_f32_16x16x32_bf16 v[40:43], v[136:139], a[128:131], v[40:43]\n"
"v_mfma_f32_16x16x32_bf16 v[44:47], v[136:139], a[132:135], v[44:47]\n"
"v_mfma_f32_16x16x32_bf16 v[48:51], v[136:139], a[136:139], v[48:51]\n"
"v_mfma_f32_16x16x32_bf16 v[52:55], v[136:139], a[140:143], v[52:55]\n"
"global_load_dwordx4 v[96:99], v5, s[94:95] offset:-1024 sc1\n"
"v_mfma_f32_16x16x32_bf16 v[40:43], v[140:143], a[144:147], v[40:43]\n"
"v_mfma_f32_16x16x32_bf16 v[44:47], v[140:143], a[148:151], v[44:47]\n"
"v_mfma_f32_16x16x32_bf16 v[48:51], v[140:143], a[152:155], v[48:51]\n"
"s_waitcnt vmcnt(25)\n"
"v_mfma_f32_16x16x32_bf16 v[52:55], v[140:143], a[156:159], v[52:55]\n"
"global_load_dwordx4 v[100:103], v5, s[94:95] offset:0 sc1\n"
"v_mfma_f32_16x16x32_bf16 v[40:43], v[144:147], a[160:163], v[40:43]\n"
"buffer_load_dwordx2 v[222:223], v221, s[8:11], s79 offen offset:768 sc1\n"
"v_mfma_f32_16x16x32_bf16 v[44:47], v[144:147], a[164:167], v[44:47]\n"
"v_mfma_f32_16x16x32_bf16 v[48:51], v[144:147], a[168:171], v[48:51]\n"
"v_mfma_f32_16x16x32_bf16 v[52:55], v[144:147], a[172:175], v[52:55]\n"
"global_load_dwordx4 v[104:107], v5, s[94:95] offset:1024 sc1\n"
"v_mfma_f32_16x16x32_bf16 v[40:43], v[156:159], a[176:179], v[40:43]\n"
"v_mfma_f32_16x16x32_bf16 v[44:47], v[156:159], a[180:183], v[44:47]\n"
"v_mfma_f32_16x16x32_bf16 v[48:51], v[156:159], a[184:187], v[48:51]\n"
"s_waitcnt vmcnt(26)\n"
"v_mfma_f32_16x16x32_bf16 v[52:55], v[156:159], a[188:191], v[52:55]\n"
"global_load_dwordx4 v[108:111], v5, s[94:95] offset:2048 sc1\n"
"v_mfma_f32_16x16x32_bf16 v[40:43], v[160:163], a[192:195], v[40:43]\n"
"v_mfma_f32_16x16x32_bf16 v[44:47], v[160:163], a[196:199], v[44:47]\n"
"v_mfma_f32_16x16x32_bf16 v[48:51], v[160:163], a[200:203], v[48:51]\n"
"v_mfma_f32_16x16x32_bf16 v[52:55], v[160:163], a[204:207], v[52:55]\n"
"global_load_dwordx4 v[112:115], v5, s[94:95] offset:3072 sc1\n"
"v_mfma_f32_16x16x32_bf16 v[40:43], v[164:167], a[208:211], v[40:43]\n"
"v_mfma_f32_16x16x32_bf16 v[44:47], v[164:167], a[212:215], v[44:47]\n"
"v_mfma_f32_16x16x32_bf16 v[48:51], v[164:167], a[216:219], v[48:51]\n"
"s_waitcnt vmcnt(26)\n"
"v_mfma_f32_16x16x32_bf16 v[52:55], v[164:167], a[220:223], v[52:55]\n"
"global_load_dwordx4 v[116:119], v6, s[94:95] offset:-4096 sc1\n"
"v_mfma_f32_16x16x32_bf16 v[40:43], v[168:171], a[224:227], v[40:43]\n"
"v_mfma_f32_16x16x32_bf16 v[44:47], v[168:171], a[228:231], v[44:47]\n"
"v_mfma_f32_16x16x32_bf16 v[48:51], v[168:171], a[232:235], v[48:51]\n"
"v_mfma_f32_16x16x32_bf16 v[52:55], v[168:171], a[236:239], v[52:55]\n"
"global_load_dwordx4 v[120:123], v6, s[94:95] offset:-3072 sc1\n"
"s_add_u32 s94, s94, 18432\n"
"s_addc_u32 s95, s95, 0\n"
"v_mfma_f32_16x16x32_bf16 v[40:43], v[172:175], a[240:243], v[40:43]\n"
"v_mfma_f32_16x16x32_bf16 v[44:47], v[172:175], a[244:247], v[44:47]\n"
"v_mfma_f32_16x16x32_bf16 v[48:51], v[172:175], a[248:251], v[48:51]\n"
"s_waitcnt vmcnt(25)\n"
"v_mfma_f32_16x16x32_bf16 v[52:55], v[172:175], a[252:255], v[52:55]\n"
"global_load_dwordx4 v[124:127], v4, s[94:95] offset:-4096 sc1\n"
"v_mfma_f32_16x16x32_bf16 v[40:43], v[176:179], v[224:227], v[40:43]\n"
"v_mfma_f32_16x16x32_bf16 v[44:47], v[176:179], v[228:231], v[44:47]\n"
"v_mfma_f32_16x16x32_bf16 v[48:51], v[176:179], v[232:235], v[48:51]\n"
"v_mfma_f32_16x16x32_bf16 v[52:55], v[176:179], v[236:239], v[52:55]\n"
"global_load_dwordx4 v[128:131], v4, s[94:95] offset:-3072 sc1\n"
"v_mfma_f32_16x16x32_bf16 v[40:43], v[180:183], v[240:243], v[40:43]\n"
"v_mfma_f32_16x16x32_bf16 v[44:47], v[180:183], v[244:247], v[44:47]\n"
"v_mfma_f32_16x16x32_bf16 v[48:51], v[180:183], v[248:251], v[48:51]\n"
"v_mfma_f32_16x16x32_bf16 v[52:55], v[180:183], v[252:255], v[52:55]\n"
"global_load_dwordx4 v[132:135], v4, s[94:95] offset:-2048 sc1\n"
"s_waitcnt vmcnt(26)\n"
"v_mfma_f32_16x16x32_bf16 v[56:59], v[184:187], a[0:3], 0\n"
"v_mfma_f32_16x16x32_bf16 v[60:63], v[184:187], a[4:7], 0\n"
"v_mfma_f32_16x16x32_bf16 v[64:67], v[184:187], a[8:11], 0\n"
"v_mfma_f32_16x16x32_bf16 v[68:71], v[184:187], a[12:15], 0\n"
"global_load_dwordx4 v[136:139], v4, s[94:95] offset:-1024 sc1\n"
"s_add_u32 s80, s29, 5\n"
"v_cvt_pk_bf16_f32 v24, v40, v41\n"
"s_and_b32 s80, s80, 7\n"
"v_mfma_f32_16x16x32_bf16 v[56:59], v[188:191], a[16:19], v[56:59]\n"
"v_cvt_pk_bf16_f32 v25, v42, v43\n"
"s_mul_i32 s80, s80, 57344\n"
"v_cvt_pk_bf16_f32 v26, v44, v45\n"
"s_add_u32 s80, s80, s72\n"
"v_mfma_f32_16x16x32_bf16 v[60:63], v[188:191], a[20:23], v[60:63]\n"
"v_cvt_pk_bf16_f32 v27, v46, v47\n"
"s_add_u32 s80, s80, 32768\n"
"v_cvt_pk_bf16_f32 v28, v48, v49\n"
"v_mfma_f32_16x16x32_bf16 v[64:67], v[188:191], a[24:27], v[64:67]\n"
"s_waitcnt vmcnt(25)\n"
"v_cvt_pk_bf16_f32 v29, v50, v51\n"
"v_cvt_pk_bf16_f32 v30, v52, v53\n"
"v_mfma_f32_16x16x32_bf16 v[68:71], v[188:191], a[28:31], v[68:71]\n"
"v_cvt_pk_bf16_f32 v31, v54, v55\n"
"v_mfma_f32_16x16x32_bf16 v[56:59], v[192:195], a[32:35], v[56:59]\n"
"buffer_store_dwordx4 v[24:27], v1, s[8:11], s80 offen offset:0 sc1\n"
"v_mfma_f32_16x16x32_bf16 v[60:63], v[192:195], a[36:39], v[60:63]\n"
"buffer_store_dwordx4 v[28:31], v1, s[8:11], s80 offen offset:1024 sc1\n"
"s_add_u32 s80, s29, 4\n"
"s_and_b32 s80, s80, 7\n"
"s_lshl_b32 s80, s80, 11\n"
"s_add_u32 s80, s80, s75\n"
"s_waitcnt vmcnt(22)\n"
"v_mfma_f32_16x16x32_bf16 v[64:67], v[192:195], a[40:43], v[64:67]\n"
"buffer_store_dwordx2 v[22:23], v220, s[8:11], s80 offen offset:800 sc1\n"
"s_waitcnt vmcnt(16)\n"
"v_mfma_f32_16x16x32_bf16 v[68:71], v[192:195], a[44:47], v[68:71]\n"
"v_lshlrev_b32 v34, 16, v32\n"
"v_and_b32 v35, 0xffff0000, v32\n"
"v_mfma_f32_16x16x32_bf16 v[56:59], v[196:199], a[48:51], v[56:59]\n"
"v_add_f32 v152, v152, v34\n"
"v_add_f32 v153, v153, v35\n"
"v_mfma_f32_16x16x32_bf16 v[60:63], v[196:199], a[52:55], v[60:63]\n"
"v_lshlrev_b32 v36, 16, v33\n"
"v_and_b32 v37, 0xffff0000, v33\n"
"v_mfma_f32_16x16x32_bf16 v[64:67], v[196:199], a[56:59], v[64:67]\n"
"s_waitcnt vmcnt(26)\n"
"v_add_f32 v154, v154, v36\n"
"v_add_f32 v155, v155, v37\n"
"v_mfma_f32_16x16x32_bf16 v[68:71], v[196:199], a[60:63], v[68:71]\n"
"v_lshlrev_b32 v34, 16, v14\n"
"v_and_b32 v35, 0xffff0000, v14\n"
"v_mfma_f32_16x16x32_bf16 v[56:59], v[200:203], a[64:67], v[56:59]\n"
"v_add_f32 v76, v76, v34\n"
"v_add_f32 v77, v77, v35\n"
"v_mfma_f32_16x16x32_bf16 v[60:63], v[200:203], a[68:71], v[60:63]\n"
"v_lshlrev_b32 v36, 16, v15\n"
"v_and_b32 v37, 0xffff0000, v15\n"
"v_mfma_f32_16x16x32_bf16 v[64:67], v[200:203], a[72:75], v[64:67]\n"
"v_add_f32 v78, v78, v36\n"
"v_add_f32 v79, v79, v37\n"
"v_mfma_f32_16x16x32_bf16 v[68:71], v[200:203], a[76:79], v[68:71]\n"
"global_load_dwordx4 v[140:143], v4, s[94:95] offset:0 sc1\n"
"v_lshlrev_b32 v34, 16, v16\n"
"v_mfma_f32_16x16x32_bf16 v[56:59], v[204:207], a[80:83], v[56:59]\n"
"v_and_b32 v35, 0xffff0000, v16\n"
"v_add_f32 v80, v80, v34\n"
"v_mfma_f32_16x16x32_bf16 v[60:63], v[204:207], a[84:87], v[60:63]\n"
"v_add_f32 v81, v81, v35\n"
"v_lshlrev_b32 v36, 16, v17\n"
"v_mfma_f32_16x16x32_bf16 v[64:67], v[204:207], a[88:91], v[64:67]\n"
"s_waitcnt vmcnt(22)\n"
"v_and_b32 v37, 0xffff0000, v17\n"
"v_add_f32 v82, v82, v36\n"
"v_mfma_f32_16x16x32_bf16 v[68:71], v[204:207], a[92:95], v[68:71]\n"
"global_load_dwordx4 v[144:147], v4, s[94:95] offset:1024 sc1\n"
"v_add_f32 v83, v83, v37\n"
"v_mfma_f32_16x16x32_bf16 v[56:59], v[208:211], a[96:99], v[56:59]\n"
"v_lshlrev_b32 v34, 16, v18\n"
"v_and_b32 v35, 0xffff0000, v18\n"
"v_mfma_f32_16x16x32_bf16 v[60:63], v[208:211], a[100:103], v[60:63]\n"
"v_add_f32 v84, v84, v34\n"
"v_add_f32 v85, v85, v35\n"
"v_mfma_f32_16x16x32_bf16 v[64:67], v[208:211], a[104:107], v[64:67]\n"
"v_lshlrev_b32 v36, 16, v19\n"
"v_and_b32 v37, 0xffff0000, v19\n"
"v_mfma_f32_16x16x32_bf16 v[68:71], v[208:211], a[108:111], v[68:71]\n"
"global_load_dwordx4 v[156:159], v4, s[94:95] offset:2048 sc1\n"
"v_add_f32 v86, v86, v36\n"
"v_mfma_f32_16x16x32_bf16 v[56:59], v[212:215], a[112:115], v[56:59]\n"
"v_add_f32 v87, v87, v37\n"
"v_lshlrev_b32 v34, 16, v20\n"
"v_mfma_f32_16x16x32_bf16 v[60:63], v[212:215], a[116:119], v[60:63]\n"
"v_and_b32 v35, 0xffff0000, v20\n"
"v_add_f32 v148, v148, v34\n"
"v_mfma_f32_16x16x32_bf16 v[64:67], v[212:215], a[120:123], v[64:67]\n"
"s_waitcnt vmcnt(20)\n"
"v_add_f32 v149, v149, v35\n"
"v_lshlrev_b32 v36, 16, v21\n"
"v_mfma_f32_16x16x32_bf16 v[68:71], v[212:215], a[124:127], v[68:71]\n"
"global_load_dwordx4 v[160:163], v4, s[94:95] offset:3072 sc1\n"
"v_and_b32 v37, 0xffff0000, v21\n"
"v_mfma_f32_16x16x32_bf16 v[56:59], v[72:75], a[128:131], v[56:59]\n"
"v_add_f32 v150, v150, v36\n"
"v_add_f32 v151, v151, v37\n"
"s_waitcnt vmcnt(16)\n"
"v_mfma_f32_16x16x32_bf16 v[60:63], v[72:75], a[132:135], v[60:63]\n"
"v_cmp_ne_u64 vcc, s[24:25], v[222:223]\n"
"s_nop 0\n"
"s_bitcmp1_b32 vcc_lo, 3\n"
"s_cbranch_scc1 .Lfused_block_kernel_slow3\n"
".Lfused_block_kernel_back3:\n"
"s_sub_u32 s81, s29, 3\n"
"s_and_b32 s81, s81, 7\n"
"s_lshl_b32 s81, s81, 13\n"
"s_add_u32 s81, s81, s74\n"
"v_mfma_f32_16x16x32_bf16 v[64:67], v[72:75], a[136:139], v[64:67]\n"
"buffer_load_dwordx2 v[32:33], v13, s[8:11], s81 offen sc1\n"
"v_mfma_f32_16x16x32_bf16 v[68:71], v[72:75], a[140:143], v[68:71]\n"
"global_load_dwordx4 v[164:167], v5, s[94:95] offset:-4096 sc1\n"
"v_mfma_f32_16x16x32_bf16 v[56:59], v[88:91], a[144:147], v[56:59]\n"
"buffer_load_dwordx4 v[14:17], v1, s[8:11], s73 offen offset:0 sc1\n"
"v_mfma_f32_16x16x32_bf16 v[60:63], v[88:91], a[148:151], v[60:63]\n"
"buffer_load_dwordx4 v[18:21], v1, s[8:11], s73 offen offset:1024 sc1\n"
"s_add_u32 s73, s73, 8192\n"
"v_mfma_f32_16x16x32_bf16 v[64:67], v[88:91], a[152:155], v[64:67]\n"
"s_waitcnt vmcnt(22)\n"
"v_mfma_f32_16x16x32_bf16 v[68:71], v[88:91], a[156:159], v[68:71]\n"
"global_load_dwordx4 v[168:171], v5, s[94:95] offset:-3072 sc1\n"
"v_mfma_f32_16x16x32_bf16 v[56:59], v[92:95], a[160:163], v[56:59]\n"
"buffer_load_dwordx2 v[222:223], v221, s[8:11], s79 offen offset:768 sc1\n"
"v_mfma_f32_16x16x32_bf16 v[60:63], v[92:95], a[164:167], v[60:63]\n"
"v_mfma_f32_16x16x32_bf16 v[64:67], v[92:95], a[168:171], v[64:67]\n"
"v_mfma_f32_16x16x32_bf16 v[68:71], v[92:95], a[172:175], v[68:71]\n"
"global_load_dwordx4 v[172:175], v5, s[94:95] offset:-2048 sc1\n"
"v_mfma_f32_16x16x32_bf16 v[56:59], v[96:99], a[176:179], v[56:59]\n"
"v_mfma_f32_16x16x32_bf16 v[60:63], v[96:99], a[180:183], v[60:63]\n"
"v_mfma_f32_16x16x32_bf16 v[64:67], v[96:99], a[184:187], v[64:67]\n"
"s_waitcnt vmcnt(22)\n"
"v_mfma_f32_16x16x32_bf16 v[68:71], v[96:99], a[188:191], v[68:71]\n"
"global_load_dwordx4 v[176:179], v5, s[94:95] offset:-1024 sc1\n"
"v_mfma_f32_16x16x32_bf16 v[56:59], v[100:103], a[192:195], v[56:59]\n"
"v_mfma_f32_16x16x32_bf16 v[60:63], v[100:103], a[196:199], v[60:63]\n"
"v_mfma_f32_16x16x32_bf16 v[64:67], v[100:103], a[200:203], v[64:67]\n"
"v_mfma_f32_16x16x32_bf16 v[68:71], v[100:103], a[204:207], v[68:71]\n"
"global_load_dwordx4 v[180:183], v5, s[94:95] offset:0 sc1\n"
"v_mfma_f32_16x16x32_bf16 v[56:59], v[104:107], a[208:211], v[56:59]\n"
"v_mfma_f32_16x16x32_bf16 v[60:63], v[104:107], a[212:215], v[60:63]\n"
"v_mfma_f32_16x16x32_bf16 v[64:67], v[104:107], a[216:219], v[64:67]\n"
"s_waitcnt vmcnt(22)\n"
"v_mfma_f32_16x16x32_bf16 v[68:71], v[104:107], a[220:223], v[68:71]\n"
"global_load_dwordx4 v[184:187], v5, s[94:95] offset:1024 sc1\n"
"v_mfma_f32_16x16x32_bf16 v[56:59], v[108:111], a[224:227], v[56:59]\n"
"v_mfma_f32_16x16x32_bf16 v[60:63], v[108:111], a[228:231], v[60:63]\n"
"v_mfma_f32_16x16x32_bf16 v[64:67], v[108:111], a[232:235], v[64:67]\n"
"v_mfma_f32_16x16x32_bf16 v[68:71], v[108:111], a[236:239], v[68:71]\n"
"global_load_dwordx4 v[188:191], v5, s[94:95] offset:2048 sc1\n"
"v_mfma_f32_16x16x32_bf16 v[56:59], v[112:115], a[240:243], v[56:59]\n"
"v_mfma_f32_16x16x32_bf16 v[60:63], v[112:115], a[244:247], v[60:63]\n"
"v_mfma_f32_16x16x32_bf16 v[64:67], v[112:115], a[248:251], v[64:67]\n"
"s_waitcnt vmcnt(22)\n"
"v_mfma_f32_16x16x32_bf16 v[68:71], v[112:115], a[252:255], v[68:71]\n"
"global_load_dwordx4 v[192:195], v5, s[94:95] offset:3072 sc1\n"
"v_mfma_f32_16x16x32_bf16 v[56:59], v[116:119], v[224:227], v[56:59]\n"
"v_mfma_f32_16x16x32_bf16 v[60:63], v[116:119], v[228:231], v[60:63]\n"
"v_mfma_f32_16x16x32_bf16 v[64:67], v[116:119], v[232:235], v[64:67]\n"
"v_mfma_f32_16x16x32_bf16 v[68:71], v[116:119], v[236:239], v[68:71]\n"
"global_load_dwordx4 v[196:199], v6, s[94:95] offset:-4096 sc1\n"
"v_mfma_f32_16x16x32_bf16 v[56:59], v[120:123], v[240:243], v[56:59]\n"
"v_mfma_f32_16x16x32_bf16 v[60:63], v[120:123], v[244:247], v[60:63]\n"
"v_mfma_f32_16x16x32_bf16 v[64:67], v[120:123], v[248:251], v[64:67]\n"
"v_mfma_f32_16x16x32_bf16 v[68:71], v[120:123], v[252:255], v[68:71]\n"
"global_load_dwordx4 v[200:203], v6, s[94:95] offset:-3072 sc1\n"
"s_add_u32 s94, s94, 18432\n"
"s_addc_u32 s95, s95, 0\n"
"s_waitcnt vmcnt(23)\n"
"v_mfma_f32_16x16x32_bf16 v[40:43], v[124:127], a[0:3], 0\n"
"v_mfma_f32_16x16x32_bf16 v[44:47], v[124:127], a[4:7], 0\n"
"v_mfma_f32_16x16x32_bf16 v[48:51], v[124:127], a[8:11], 0\n"
"v_mfma_f32_16x16x32_bf16 v[52:55], v[124:127], a[12:15], 0\n"
"global_load_dwordx4 v[204:207], v4, s[94:95] offset:-4096 sc1\n"
"s_add_u32 s80, s29, 6\n"
"v_cvt_pk_bf16_f32 v24, v56, v57\n"
"s_and_b32 s80, s80, 7\n"
"v_mfma_f32_16x16x32_bf16 v[40:43], v[128:131], a[16:19], v[40:43]\n"
"v_cvt_pk_bf16_f32 v25, v58, v59\n"
"s_mul_i32 s80, s80, 57344\n"
"v_cvt_pk_bf16_f32 v26, v60, v61\n"
"s_add_u32 s80, s80, s72\n"
"v_mfma_f32_16x16x32_bf16 v[44:47], v[128:131], a[20:23], v[44:47]\n"
"v_cvt_pk_bf16_f32 v27, v62, v63\n"
"s_add_u32 s80, s80, 40960\n"
"v_cvt_pk_bf16_f32 v28, v64, v65\n"
"v_mfma_f32_16x16x32_bf16 v[48:51], v[128:131], a[24:27], v[48:51]\n"
"s_waitcnt vmcnt(22)\n"
"v_cvt_pk_bf16_f32 v29, v66, v67\n"
"v_cvt_pk_bf16_f32 v30, v68, v69\n"
"v_mfma_f32_16x16x32_bf16 v[52:55], v[128:131], a[28:31], v[52:55]\n"
"v_cvt_pk_bf16_f32 v31, v70, v71\n"
"v_mfma_f32_16x16x32_bf16 v[40:43], v[132:135], a[32:35], v[40:43]\n"
"buffer_store_dwordx4 v[24:27], v1, s[8:11], s80 offen offset:0 sc1\n"
"v_mfma_f32_16x16x32_bf16 v[44:47], v[132:135], a[36:39], v[44:47]\n"
"buffer_store_dwordx4 v[28:31], v1, s[8:11], s80 offen offset:1024 sc1\n"
"s_add_u32 s80, s29, 5\n"
"s_and_b32 s80, s80, 7\n"
"s_lshl_b32 s80, s80, 11\n"
"s_add_u32 s80, s80, s75\n"
"s_waitcnt vmcnt(22)\n"
"v_mfma_f32_16x16x32_bf16 v[48:51], v[132:135], a[40:43], v[48:51]\n"
"buffer_store_dwordx2 v[22:23], v220, s[8:11], s80 offen offset:808 sc1\n"
"s_waitcnt vmcnt(14)\n"
"v_mfma_f32_16x16x32_bf16 v[52:55], v[132:135], a[44:47], v[52:55]\n"
"v_lshlrev_b32 v34, 16, v32\n"
"v_and_b32 v35, 0xffff0000, v32\n"
"v_mfma_f32_16x16x32_bf16 v[40:43], v[136:139], a[48:51], v[40:43]\n"
"v_add_f32 v152, v152, v34\n"
"v_add_f32 v153, v153, v35\n"
"v_mfma_f32_16x16x32_bf16 v[44:47], v[136:139], a[52:55], v[44:47]\n"
"v_lshlrev_b32 v36, 16, v33\n"
"v_and_b32 v37, 0xffff0000, v33\n"
"v_mfma_f32_16x16x32_bf16 v[48:51], v[136:139], a[56:59], v[48:51]\n"
"s_waitcnt vmcnt(20)\n"
"v_add_f32 v154, v154, v36\n"
"v_add_f32 v155, v155, v37\n"
"v_mfma_f32_16x16x32_bf16 v[52:55], v[136:139], a[60:63], v[52:55]\n"
"v_lshlrev_b32 v34, 16, v14\n"
"v_and_b32 v35, 0xffff0000, v14\n"
"v_mfma_f32_16x16x32_bf16 v[40:43], v[140:143], a[64:67], v[40:43]\n"
"v_add_f32 v76, v76, v34\n"
"v_add_f32 v77, v77, v35\n"
"v_mfma_f32_16x16x32_bf16 v[44:47], v[140:143], a[68:71], v[44:47]\n"
"v_lshlrev_b32 v36, 16, v15\n"
"v_and_b32 v37, 0xffff0000, v15\n"
"v_mfma_f32_16x16x32_bf16 v[48:51], v[140:143], a[72:75], v[48:51]\n"
"v_add_f32 v78, v78, v36\n"
"v_add_f32 v79, v79, v37\n"
"v_mfma_f32_16x16x32_bf16 v[52:55], v[140:143], a[76:79], v[52:55]\n"
"global_load_dwordx4 v[208:211], v4, s[94:95] offset:-3072 sc1\n"
"v_lshlrev_b32 v34, 16, v16\n"
"v_mfma_f32_16x16x32_bf16 v[40:43], v[144:147], a[80:83], v[40:43]\n"
"v_and_b32 v35, 0xffff0000, v16\n"
"v_add_f32 v80, v80, v34\n"
"v_mfma_f32_16x16x32_bf16 v[44:47], v[144:147], a[84:87], v[44:47]\n"
"v_add_f32 v81, v81, v35\n"
"v_lshlrev_b32 v36, 16, v17\n"
"v_mfma_f32_16x16x32_bf16 v[48:51], v[144:147], a[88:91], v[48:51]\n"
"s_waitcnt vmcnt(19)\n"
"v_and_b32 v37, 0xffff0000, v17\n"
"v_add_f32 v82, v82, v36\n"
"v_mfma_f32_16x16x32_bf16 v[52:55], v[144:147], a[92:95], v[52:55]\n"
"global_load_dwordx4 v[212:215], v4, s[94:95] offset:-2048 sc1\n"
"v_add_f32 v83, v83, v37\n"
"v_mfma_f32_16x16x32_bf16 v[40:43], v[156:159], a[96:99], v[40:43]\n"
"v_lshlrev_b32 v34, 16, v18\n"
"v_and_b32 v35, 0xffff0000, v18\n"
"v_mfma_f32_16x16x32_bf16 v[44:47], v[156:159], a[100:103], v[44:47]\n"
"v_add_f32 v84, v84, v34\n"
"v_add_f32 v85, v85, v35\n"
"v_mfma_f32_16x16x32_bf16 v[48:51], v[156:159], a[104:107], v[48:51]\n"
"v_lshlrev_b32 v36, 16, v19\n"
"v_and_b32 v37, 0xffff0000, v19\n"
"v_mfma_f32_16x16x32_bf16 v[52:55], v[156:159], a[108:111], v[52:55]\n"
"global_load_dwordx4 v[72:75], v4, s[94:95] offset:-1024 sc1\n"
"v_add_f32 v86, v86, v36\n"
"v_mfma_f32_16x16x32_bf16 v[40:43], v[160:163], a[112:115], v[40:43]\n"
"v_add_f32 v87, v87, v37\n"
"v_lshlrev_b32 v34, 16, v20\n"
"v_mfma_f32_16x16x32_bf16 v[44:47], v[160:163], a[116:119], v[44:47]\n"
"v_and_b32 v35, 0xffff0000, v20\n"
"v_add_f32 v148, v148, v34\n"
"v_mfma_f32_16x16x32_bf16 v[48:51], v[160:163], a[120:123], v[48:51]\n"
"s_waitcnt vmcnt(16)\n"
"v_add_f32 v149, v149, v35\n"
"v_lshlrev_b32 v36, 16, v21\n"
"v_mfma_f32_16x16x32_bf16 v[52:55], v[160:163], a[124:127], v[52:55]\n"
"global_load_dwordx4 v[88:91], v4, s[94:95] offset:0 sc1\n"
"v_and_b32 v37, 0xffff0000, v21\n"
"v_mfma_f32_16x16x32_bf16 v[40:43], v[164:167], a[128:131], v[40:43]\n"
"v_add_f32 v150, v150, v36\n"
"v_add_f32 v151, v151, v37\n"
"s_waitcnt vmcnt(16)\n"
"v_mfma_f32_16x16x32_bf16 v[44:47], v[164:167], a[132:135], v[44:47]\n"
"v_cmp_ne_u64 vcc, s[24:25], v[222:223]\n"
"s_nop 0\n"
"s_bitcmp1_b32 vcc_lo, 4\n"
"s_cbranch_scc1 .Lfused_block_kernel_slow4\n"
".Lfused_block_kernel_back4:\n"
"s_sub_u32 s81, s29, 4\n"
"s_and_b32 s81, s81, 7\n"
"s_lshl_b32 s81, s81, 13\n"
"s_add_u32 s81, s81, s74\n"
"v_mfma_f32_16x16x32_bf16 v[48:51], v[164:167], a[136:139], v[48:51]\n"
"buffer_load_dwordx2 v[32:33], v13, s[8:11], s81 offen sc1\n"
"v_mfma_f32_16x16x32_bf16 v[52:55], v[164:167], a[140:143], v[52:55]\n"
"global_load_dwordx4 v[92:95], v4, s[94:95] offset:1024 sc1\n"
"v_mfma_f32_16x16x32_bf16 v[40:43], v[168:171], a[144:147], v[40:43]\n"
"buffer_load_dwordx4 v[14:17], v1, s[8:11], s73 offen offset:0 sc1\n"
"v_mfma_f32_16x16x32_bf16 v[44:47], v[168:171], a[148:151], v[44:47]\n"
"buffer_load_dwordx4 v[18:21], v1, s[8:11], s73 offen offset:1024 sc1\n"
"s_add_u32 s73, s73, 8192\n"
"v_mfma_f32_16x16x32_bf16 v[48:51], v[168:171], a[152:155], v[48:51]\n"
"s_waitcnt vmcnt(18)\n"
"v_mfma_f32_16x16x32_bf16 v[52:55], v[168:171], a[156:159], v[52:55]\n"
"global_load_dwordx4 v[96:99], v4, s[94:95] offset:2048 sc1\n"
"v_mfma_f32_16x16x32_bf16 v[40:43], v[172:175], a[160:163], v[40:43]\n"
"buffer_load_dwordx2 v[222:223], v221, s[8:11], s79 offen offset:768 sc1\n"
"v_mfma_f32_16x16x32_bf16 v[44:47], v[172:175], a[164:167], v[44:47]\n"
"v_mfma_f32_16x16x32_bf16 v[48:51], v[172:175], a[168:171], v[48:51]\n"
"v_mfma_f32_16x16x32_bf16 v[52:55], v[172:175], a[172:175], v[52:55]\n"
"global_load_dwordx4 v[100:103], v4, s[94:95] offset:3072 sc1\n"
"v_mfma_f32_16x16x32_bf16 v[40:43], v[176:179], a[176:179], v[40:43]\n"
"v_mfma_f32_16x16x32_bf16 v[44:47], v[176:179], a[180:183], v[44:47]\n"
"v_mfma_f32_16x16x32_bf16 v[48:51], v[176:179], a[184:187], v[48:51]\n"
"s_waitcnt vmcnt(19)\n"
"v_mfma_f32_16x16x32_bf16 v[52:55], v[176:179], a[188:191], v[52:55]\n"
"global_load_dwordx4 v[104:107], v5, s[94:95] offset:-4096 sc1\n"
"v_mfma_f32_16x16x32_bf16 v[40:43], v[180:183], a[192:195], v[40:43]\n"
"v_mfma_f32_16x16x32_bf16 v[44:47], v[180:183], a[196:199], v[44:47]\n"
"v_mfma_f32_16x16x32_bf16 v[48:51], v[180:183], a[200:203], v[48:51]\n"
"v_mfma_f32_16x16x32_bf16 v[52:55], v[180:183], a[204:207], v[52:55]\n"
"global_load_dwordx4 v[108:111], v5, s[94:95] offset:-3072 sc1\n"
"v_mfma_f32_16x16x32_bf16 v[40:43], v[184:187], a[208:211], v[40:43]\n"
"v_mfma_f32_16x16x32_bf16 v[44:47], v[184:187], a[212:215], v[44:47]\n"
"v_mfma_f32_16x16x32_bf16 v[48:51], v[184:187], a[216:219], v[48:51]\n"
"s_waitcnt vmcnt(19)\n"
"v_mfma_f32_16x16x32_bf16 v[52:55], v[184:187], a[220:223], v[52:55]\n"
"global_load_dwordx4 v[112:115], v5, s[94:95] offset:-2048 sc1\n"
"v_mfma_f32_16x16x32_bf16 v[40:43], v[188:191], a[224:227], v[40:43]\n"
"v_mfma_f32_16x16x32_bf16 v[44:47], v[188:191], a[228:231], v[44:47]\n"
"v_mfma_f32_16x16x32_bf16 v[48:51], v[188:191], a[232:235], v[48:51]\n"
"v_mfma_f32_16x16x32_bf16 v[52:55], v[188:191], a[236:239], v[52:55]\n"
"global_load_dwordx4 v[116:119], v5, s[94:95] offset:-1024 sc1\n"
"v_mfma_f32_16x16x32_bf16 v[40:43], v[192:195], a[240:243], v[40:43]\n"
"v_mfma_f32_16x16x32_bf16 v[44:47], v[192:195], a[244:247], v[44:47]\n"
"v_mfma_f32_16x16x32_bf16 v[48:51], v[192:195], a[248:251], v[48:51]\n"
"s_waitcnt vmcnt(19)\n"
"v_mfma_f32_16x16x32_bf16 v[52:55], v[192:195], a[252:255], v[52:55]\n"
"global_load_dwordx4 v[120:123], v5, s[94:95] offset:0 sc1\n"
"v_mfma_f32_16x16x32_bf16 v[40:43], v[196:199], v[224:227], v[40:43]\n"
"v_mfma_f32_16x16x32_bf16 v[44:47], v[196:199], v[228:231], v[44:47]\n"
"v_mfma_f32_16x16x32_bf16 v[48:51], v[196:199], v[232:235], v[48:51]\n"
"v_mfma_f32_16x16x32_bf16 v[52:55], v[196:199], v[236:239], v[52:55]\n"
"global_load_dwordx4 v[124:127], v5, s[94:95] offset:1024 sc1\n"
"v_mfma_f32_16x16x32_bf16 v[40:43], v[200:203], v[240:243], v[40:43]\n"
"v_mfma_f32_16x16x32_bf16 v[44:47], v[200:203], v[244:247], v[44:47]\n"
"v_mfma_f32_16x16x32_bf16 v[48:51], v[200:203], v[248:251], v[48:51]\n"
"v_mfma_f32_16x16x32_bf16 v[52:55], v[200:203], v[252:255], v[52:55]\n"
"global_load_dwordx4 v[128:131], v5, s[94:95] offset:2048 sc1\n"
"s_waitcnt vmcnt(17)\n"
"v_mfma_f32_16x16x32_bf16 v[56:59], v[204:207], a[0:3], 0\n"
"v_mfma_f32_16x16x32_bf16 v[60:63], v[204:207], a[4:7], 0\n"
"v_mfma_f32_16x16x32_bf16 v[64:67], v[204:207], a[8:11], 0\n"
"v_mfma_f32_16x16x32_bf16 v[68:71], v[204:207], a[12:15], 0\n"
"global_load_dwordx4 v[132:135], v5, s[94:95] offset:3072 sc1\n"
"s_add_u32 s80, s29, 7\n"
"v_cvt_pk_bf16_f32 v24, v40, v41\n"
"s_and_b32 s80, s80, 7\n"
"v_mfma_f32_16x16x32_bf16 v[56:59], v[208:211], a[16:19], v[56:59]\n"
"v_cvt_pk_bf16_f32 v25, v42, v43\n"
"s_mul_i32 s80, s80, 57344\n"
"v_cvt_pk_bf16_f32 v26, v44, v45\n"
"s_add_u32 s80, s80, s72\n"
"v_mfma_f32_16x16x32_bf16 v[60:63], v[208:211], a[20:23], v[60:63]\n"
"v_cvt_pk_bf16_f32 v27, v46, v47\n"
"s_add_u32 s80, s80, 49152\n"
"v_cvt_pk_bf16_f32 v28, v48, v49\n"
"v_mfma_f32_16x16x32_bf16 v[64:67], v[208:211], a[24:27], v[64:67]\n"
"s_waitcnt vmcnt(16)\n"
"v_cvt_pk_bf16_f32 v29, v50, v51\n"
"v_cvt_pk_bf16_f32 v30, v52, v53\n"
"v_mfma_f32_16x16x32_bf16 v[68:71], v[208:211], a[28:31], v[68:71]\n"
"v_cvt_pk_bf16_f32 v31, v54, v55\n"
"v_mfma_f32_16x16x32_bf16 v[56:59], v[212:215], a[32:35], v[56:59]\n"
"buffer_store_dwordx4 v[24:27], v1, s[8:11], s80 offen offset:0 sc1\n"
"v_mfma_f32_16x16x32_bf16 v[60:63], v[212:215], a[36:39], v[60:63]\n"
"buffer_store_dwordx4 v[28:31], v1, s[8:11], s80 offen offset:1024 sc1\n"
"s_add_u32 s80, s29, 6\n"
"s_and_b32 s80, s80, 7\n"
"s_lshl_b32 s80, s80, 11\n"
"s_add_u32 s80, s80, s75\n"
"s_waitcnt vmcnt(22)\n"
"v_mfma_f32_16x16x32_bf16 v[64:67], v[212:215], a[40:43], v[64:67]\n"
"buffer_store_dwordx2 v[22:23], v220, s[8:11], s80 offen offset:816 sc1\n"
"s_waitcnt vmcnt(14)\n"
"v_mfma_f32_16x16x32_bf16 v[68:71], v[212:215], a[44:47], v[68:71]\n"
"v_lshlrev_b32 v34, 16, v32\n"
"v_and_b32 v35, 0xffff0000, v32\n"
"v_mfma_f32_16x16x32_bf16 v[56:59], v[72:75], a[48:51], v[56:59]\n"
"v_add_f32 v152, v152, v34\n"
"v_add_f32 v153, v153, v35\n"
"v_mfma_f32_16x16x32_bf16 v[60:63], v[72:75], a[52:55], v[60:63]\n"
"v_lshlrev_b32 v36, 16, v33\n"
"v_and_b32 v37, 0xffff0000, v33\n"
"v_mfma_f32_16x16x32_bf16 v[64:67], v[72:75], a[56:59], v[64:67]\n"
"s_waitcnt vmcnt(16)\n"
"v_add_f32 v154, v154, v36\n"
"v_add_f32 v155, v155, v37\n"
"v_mfma_f32_16x16x32_bf16 v[68:71], v[72:75], a[60:63], v[68:71]\n"
"v_lshlrev_b32 v34, 16, v14\n"
"v_and_b32 v35, 0xffff0000, v14\n"
"v_mfma_f32_16x16x32_bf16 v[56:59], v[88:91], a[64:67], v[56:59]\n"
"v_add_f32 v76, v76, v34\n"
"v_add_f32 v77, v77, v35\n"
"v_mfma_f32_16x16x32_bf16 v[60:63], v[88:91], a[68:71], v[60:63]\n"
"v_lshlrev_b32 v36, 16, v15\n"
"v_and_b32 v37, 0xffff0000, v15\n"
"v_mfma_f32_16x16x32_bf16 v[64:67], v[88:91], a[72:75], v[64:67]\n"
"v_add_f32 v78, v78, v36\n"
"v_add_f32 v79, v79, v37\n"
"v_mfma_f32_16x16x32_bf16 v[68:71], v[88:91], a[76:79], v[68:71]\n"
"global_load_dwordx4 v[136:139], v6, s[94:95] offset:-4096 sc1\n"
"v_lshlrev_b32 v34, 16, v16\n"
"v_mfma_f32_16x16x32_bf16 v[56:59], v[92:95], a[80:83], v[56:59]\n"
"v_and_b32 v35, 0xffff0000, v16\n"
"v_add_f32 v80, v80, v34\n"
"v_mfma_f32_16x16x32_bf16 v[60:63], v[92:95], a[84:87], v[60:63]\n"
"v_add_f32 v81, v81, v35\n"
"v_lshlrev_b32 v36, 16, v17\n"
"v_mfma_f32_16x16x32_bf16 v[64:67], v[92:95], a[88:91], v[64:67]\n"
"s_waitcnt vmcnt(12)\n"
"v_and_b32 v37, 0xffff0000, v17\n"
"v_add_f32 v82, v82, v36\n"
"v_mfma_f32_16x16x32_bf16 v[68:71], v[92:95], a[92:95], v[68:71]\n"
"global_load_dwordx4 v[140:143], v6, s[94:95] offset:-3072 sc1\n"
"v_add_f32 v83, v83, v37\n"
"v_mfma_f32_16x16x32_bf16 v[56:59], v[96:99], a[96:99], v[56:59]\n"
"v_lshlrev_b32 v34, 16, v18\n"
"v_and_b32 v35, 0xffff0000, v18\n"
"v_mfma_f32_16x16x32_bf16 v[60:63], v[96:99], a[100:103], v[60:63]\n"
"v_add_f32 v84, v84, v34\n"
"v_add_f32 v85, v85, v35\n"
"v_mfma_f32_16x16x32_bf16 v[64:67], v[96:99], a[104:107], v[64:67]\n"
"v_lshlrev_b32 v36, 16, v19\n"
"v_and_b32 v37, 0xffff0000, v19\n"
"v_mfma_f32_16x16x32_bf16 v[68:71], v[96:99], a[108:111], v[68:71]\n"
"v_add_f32 v86, v86, v36\n"
"v_add_f32 v87, v87, v37\n"
"v_mfma_f32_16x16x32_bf16 v[56:59], v[100:103], a[112:115], v[56:59]\n"
"v_lshlrev_b32 v34, 16, v20\n"
"v_and_b32 v35, 0xffff0000, v20\n"
"v_mfma_f32_16x16x32_bf16 v[60:63], v[100:103], a[116:119], v[60:63]\n"
"v_add_f32 v148, v148, v34\n"
"v_add_f32 v149, v149, v35\n"
"v_mfma_f32_16x16x32_bf16 v[64:67], v[100:103], a[120:123], v[64:67]\n"
"s_waitcnt vmcnt(11)\n"
"v_lshlrev_b32 v36, 16, v21\n"
"v_and_b32 v37, 0xffff0000, v21\n"
"v_mfma_f32_16x16x32_bf16 v[68:71], v[100:103], a[124:127], v[68:71]\n"
"v_add_f32 v150, v150, v36\n"
"v_add_f32 v151, v151, v37\n"
"s_waitcnt vmcnt(14)\n"
"v_mfma_f32_16x16x32_bf16 v[56:59], v[104:107], a[128:131], v[56:59]\n"
"v_cmp_ne_u64 vcc, s[24:25], v[222:223]\n"
"s_nop 0\n"
"s_bitcmp1_b32 vcc_lo, 5\n"
"s_cbranch_scc1 .Lfused_block_kernel_slow5\n"
".Lfused_block_kernel_back5:\n"
"s_sub_u32 s81, s29, 5\n"
"s_and_b32 s81, s81, 7\n"
"s_lshl_b32 s81, s81, 13\n"
"s_add_u32 s81, s81, s74\n"
"v_mfma_f32_16x16x32_bf16 v[60:63], v[104:107], a[132:135], v[60:63]\n"
"buffer_load_dwordx2 v[32:33], v13, s[8:11], s81 offen sc1\n"
"v_mfma_f32_16x16x32_bf16 v[64:67], v[104:107], a[136:139], v[64:67]\n"
"buffer_load_dwordx4 v[14:17], v1, s[8:11], s73 offen offset:0 sc1\n"
"v_mfma_f32_16x16x32_bf16 v[68:71], v[104:107], a[140:143], v[68:71]\n"
"buffer_load_dwordx4 v[18:21], v1, s[8:11], s73 offen offset:1024 sc1\n"
"s_add_u32 s73, s73, 8192\n"
"v_mfma_f32_16x16x32_bf16 v[56:59], v[108:111], a[144:147], v[56:59]\n"
"v_mfma_f32_16x16x32_bf16 v[60:63], v[108:111], a[148:151], v[60:63]\n"
"v_mfma_f32_16x16x32_bf16 v[64:67], v[108:111], a[152:155], v[64:67]\n"
"s_waitcnt vmcnt(12)\n"
"v_mfma_f32_16x16x32_bf16 v[68:71], v[108:111], a[156:159], v[68:71]\n"
"v_mfma_f32_16x16x32_bf16 v[56:59], v[112:115], a[160:163], v[56:59]\n"
"buffer_load_dwordx2 v[222:223], v221, s[8:11], s79 offen offset:768 sc1\n"
"v_mfma_f32_16x16x32_bf16 v[60:63], v[112:115], a[164:167], v[60:63]\n"
"v_mfma_f32_16x16x32_bf16 v[64:67], v[112:115], a[168:171], v[64:67]\n"
"v_mfma_f32_16x16x32_bf16 v[68:71], v[112:115], a[172:175], v[68:71]\n"
"v_mfma_f32_16x16x32_bf16 v[56:59], v[116:119], a[176:179], v[56:59]\n"
"v_mfma_f32_16x16x32_bf16 v[60:63], v[116:119], a[180:183], v[60:63]\n"
"v_mfma_f32_16x16x32_bf16 v[64:67], v[116:119], a[184:187], v[64:67]\n"
"s_waitcnt vmcnt(11)\n"
"v_mfma_f32_16x16x32_bf16 v[68:71], v[116:119], a[188:191], v[68:71]\n"
"v_mfma_f32_16x16x32_bf16 v[56:59], v[120:123], a[192:195], v[56:59]\n"
"v_mfma_f32_16x16x32_bf16 v[60:63], v[120:123], a[196:199], v[60:63]\n"
"v_mfma_f32_16x16x32_bf16 v[64:67], v[120:123], a[200:203], v[64:67]\n"
"v_mfma_f32_16x16x32_bf16 v[68:71], v[120:123], a[204:207], v[68:71]\n"
"v_mfma_f32_16x16x32_bf16 v[56:59], v[124:127], a[208:211], v[56:59]\n"
"s_add_u32 s80, s29, 7\n"
"s_and_b32 s80, s80, 7\n"
"s_lshl_b32 s80, s80, 11\n"
"s_add_u32 s80, s80, s75\n"
"s_waitcnt vmcnt(7)\n"
"buffer_store_dwordx2 v[22:23], v220, s[8:11], s80 offen offset:824 sc1\n"
"v_mfma_f32_16x16x32_bf16 v[60:63], v[124:127], a[212:215], v[60:63]\n"
"v_mfma_f32_16x16x32_bf16 v[64:67], v[124:127], a[216:219], v[64:67]\n"
"s_waitcnt vmcnt(10)\n"
"v_mfma_f32_16x16x32_bf16 v[68:71], v[124:127], a[220:223], v[68:71]\n"
"v_mfma_f32_16x16x32_bf16 v[56:59], v[128:131], a[224:227], v[56:59]\n"
"v_mfma_f32_16x16x32_bf16 v[60:63], v[128:131], a[228:231], v[60:63]\n"
"v_mfma_f32_16x16x32_bf16 v[64:67], v[128:131], a[232:235], v[64:67]\n"
"v_mfma_f32_16x16x32_bf16 v[68:71], v[128:131], a[236:239], v[68:71]\n"
"v_mfma_f32_16x16x32_bf16 v[56:59], v[132:135], a[240:243], v[56:59]\n"
"v_mfma_f32_16x16x32_bf16 v[60:63], v[132:135], a[244:247], v[60:63]\n"
"v_mfma_f32_16x16x32_bf16 v[64:67], v[132:135], a[248:251], v[64:67]\n"
"s_waitcnt vmcnt(5)\n"
"v_mfma_f32_16x16x32_bf16 v[68:71], v[132:135], a[252:255], v[68:71]\n"
"v_mfma_f32_16x16x32_bf16 v[56:59], v[136:139], v[224:227], v[56:59]\n"
"v_mfma_f32_16x16x32_bf16 v[60:63], v[136:139], v[228:231], v[60:63]\n"
"v_mfma_f32_16x16x32_bf16 v[64:67], v[136:139], v[232:235], v[64:67]\n"
"v_mfma_f32_16x16x32_bf16 v[68:71], v[136:139], v[236:239], v[68:71]\n"
"v_mfma_f32_16x16x32_bf16 v[56:59], v[140:143], v[240:243], v[56:59]\n"
"v_mfma_f32_16x16x32_bf16 v[60:63], v[140:143], v[244:247], v[60:63]\n"
"v_mfma_f32_16x16x32_bf16 v[64:67], v[140:143], v[248:251], v[64:67]\n"
"v_mfma_f32_16x16x32_bf16 v[68:71], v[140:143], v[252:255], v[68:71]\n"
"s_mul_i32 s80, s28, 81920\n"
"s_mul_i32 s81, s78, 20480\n"
"s_add_u32 s80, s80, s81\n"
"s_lshl_b32 s81, s26, 9\n"
"s_add_u32 s80, s80, s81\n"
"s_add_u32 s80, s80, 15745024\n"
"v_lshrrev_b32 v40, 1, v3\n"
"v_lshlrev_b32 v41, 8, v40\n"
"v_lshl_add_u32 v41, v2, 4, v41\n"
"v_and_b32 v42, 1, v3\n"
"v_lshl_add_u32 v41, v42, 3, v41\n"
"v_cndmask_b32 v41, v8, v41, s[76:77]\n"
"buffer_load_dwordx2 v[38:39], v41, s[8:11], s80 offen sc1\n"
"s_waitcnt vmcnt(3)\n"
"v_lshlrev_b32 v34, 16, v32\n"
"v_and_b32 v35, 0xffff0000, v32\n"
"v_add_f32 v152, v152, v34\n"
"v_add_f32 v153, v153, v35\n"
"v_lshlrev_b32 v36, 16, v33\n"
"v_and_b32 v37, 0xffff0000, v33\n"
"v_add_f32 v154, v154, v36\n"
"v_add_f32 v155, v155, v37\n"
"v_lshlrev_b32 v34, 16, v14\n"
"v_and_b32 v35, 0xffff0000, v14\n"
"v_add_f32 v76, v76, v34\n"
"v_add_f32 v77, v77, v35\n"
"v_lshlrev_b32 v36, 16, v15\n"
"v_and_b32 v37, 0xffff0000, v15\n"
"v_add_f32 v78, v78, v36\n"
"v_add_f32 v79, v79, v37\n"
"v_lshlrev_b32 v34, 16, v16\n"
"v_and_b32 v35, 0xffff0000, v16\n"
"v_add_f32 v80, v80, v34\n"
"v_add_f32 v81, v81, v35\n"
"v_lshlrev_b32 v36, 16, v17\n"
"v_and_b32 v37, 0xffff0000, v17\n"
"v_add_f32 v82, v82, v36\n"
"v_add_f32 v83, v83, v37\n"
"v_lshlrev_b32 v34, 16, v18\n"
"v_and_b32 v35, 0xffff0000, v18\n"
"v_add_f32 v84, v84, v34\n"
"v_add_f32 v85, v85, v35\n"
"v_lshlrev_b32 v36, 16, v19\n"
"v_and_b32 v37, 0xffff0000, v19\n"
"v_add_f32 v86, v86, v36\n"
"v_add_f32 v87, v87, v37\n"
"v_lshlrev_b32 v34, 16, v20\n"
"v_and_b32 v35, 0xffff0000, v20\n"
"v_add_f32 v148, v148, v34\n"
"v_add_f32 v149, v149, v35\n"
"v_lshlrev_b32 v36, 16, v21\n"
"v_and_b32 v37, 0xffff0000, v21\n"
"v_add_f32 v150, v150, v36\n"
"v_add_f32 v151, v151, v37\n"
"s_waitcnt vmcnt(2)\n"
"v_cmp_ne_u64 vcc, s[24:25], v[222:223]\n"
"s_nop 0\n"
"s_bitcmp1_b32 vcc_lo, 6\n"
"s_cbranch_scc1 .Lfused_block_kernel_slow6\n"
".Lfused_block_kernel_back6:\n"
"s_sub_u32 s81, s29, 6\n"
"s_and_b32 s81, s81, 7\n"
"s_lshl_b32 s81, s81, 13\n"
"s_add_u32 s81, s81, s74\n"
"buffer_load_dwordx2 v[32:33], v13, s[8:11], s81 offen sc1\n"
"buffer_load_dwordx4 v[14:17], v1, s[8:11], s73 offen offset:0 sc1\n"
"buffer_load_dwordx4 v[18:21], v1, s[8:11], s73 offen offset:1024 sc1\n"
"s_add_u32 s73, s73, 8192\n"
"buffer_load_dwordx2 v[222:223], v221, s[8:11], s79 offen offset:768 sc1\n"
"s_lshr_b32 s80, s26, 1\n"
"s_lshl_b32 s80, s80, 10\n"
"s_and_b32 s81, s26, 1\n"
"s_lshl_b32 s81, s81, 3\n"
"s_add_u32 s80, s80, s81\n"
"s_add_u32 s80, s80, 155648\n"
"v_add_u32 v43, s80, v1\n"
"ds_read_b64 v[24:25], v43 offset:0\n"
"ds_read_b64 v[26:27], v43 offset:2048\n"
"ds_read_b64 v[28:29], v43 offset:4096\n"
"ds_read_b64 v[30:31], v43 offset:6144\n"
"v_add_f32 v76, v76, v56\n"
"v_add_f32 v77, v77, v57\n"
"v_add_f32 v78, v78, v58\n"
"v_add_f32 v79, v79, v59\n"
"v_add_f32 v80, v80, v60\n"
"v_add_f32 v81, v81, v61\n"
"v_add_f32 v82, v82, v62\n"
"v_add_f32 v83, v83, v63\n"
"v_add_f32 v84, v84, v64\n"
"v_add_f32 v85, v85, v65\n"
"v_add_f32 v86, v86, v66\n"
"v_add_f32 v87, v87, v67\n"
"v_add_f32 v148, v148, v68\n"
"v_add_f32 v149, v149, v69\n"
"v_add_f32 v150, v150, v70\n"
"v_add_f32 v151, v151, v71\n"
"v_mov_b32 v44, v216\n"
"s_cmp_eq_u32 s78, 1\n"
"s_cselect_b64 vcc, -1, 0\n"
"v_cndmask_b32 v44, v44, v217, vcc\n"
"s_cmp_eq_u32 s78, 2\n"
"s_cselect_b64 vcc, -1, 0\n"
"v_cndmask_b32 v44, v44, v218, vcc\n"
"s_cmp_eq_u32 s78, 3\n"
"s_cselect_b64 vcc, -1, 0\n"
"v_cndmask_b32 v44, v44, v219, vcc\n"
"v_mul_u32_u24 v45, 144, v2\n"
"v_lshl_add_u32 v45, v42, 6, v45\n"
"v_lshl_add_u32 v45, v40, 5, v45\n"
"s_lshl_b32 s80, s26, 2\n"
"v_add_u32 v45, s80, v45\n"
"s_and_b32 s80, s29, 1\n"
"s_mul_i32 s80, s80, 1152\n"
"s_sub_u32 s80, 9216, s80\n"
"v_add_u32 v46, s80, v45\n"
"v_add_u32 v47, 13824, v45\n"
"v_cndmask_b32 v46, v47, v46, s[76:77]\n"
"s_mov_b32 s20, s50\n"
"s_and_b32 s21, s51, 0xffff\n"
"s_mov_b32 s22, 2359296\n"
"s_mov_b32 s23, 0x20000\n"
"v_lshrrev_b32 v47, 3, v0\n"
"v_and_b32 v48, 7, v0\n"
"v_mul_u32_u24 v49, 144, v47\n"
"v_lshl_add_u32 v49, v48, 4, v49\n"
"v_mul_u32_u24 v50, 1152, v47\n"
"v_lshl_add_u32 v50, v48, 4, v50\n"
"s_mul_i32 s80, s26, 2304\n"
"v_add_u32 v51, s80, v49\n"
"s_mul_i32 s88, s32, 18432\n"
"s_lshl_b32 s89, s29, 7\n"
"s_add_u32 s88, s88, s89\n"
"s_add_u32 s89, s88, 9216\n"
"s_mul_i32 s90, s33, 9216\n"
"s_add_u32 s90, s90, 1024\n"
"s_waitcnt vmcnt(1)\n"
"v_lshlrev_b32 v34, 16, v32\n"
"v_and_b32 v35, 0xffff0000, v32\n"
"v_add_f32 v152, v152, v34\n"
"v_add_f32 v153, v153, v35\n"
"v_lshlrev_b32 v36, 16, v33\n"
"v_and_b32 v37, 0xffff0000, v33\n"
"v_add_f32 v154, v154, v36\n"
"v_add_f32 v155, v155, v37\n"
"v_lshlrev_b32 v34, 16, v14\n"
"v_and_b32 v35, 0xffff0000, v14\n"
"v_add_f32 v76, v76, v34\n"
"v_add_f32 v77, v77, v35\n"
"v_lshlrev_b32 v36, 16, v15\n"
"v_and_b32 v37, 0xffff0000, v15\n"
"v_add_f32 v78, v78, v36\n"
"v_add_f32 v79, v79, v37\n"
"v_lshlrev_b32 v34, 16, v16\n"
"v_and_b32 v35, 0xffff0000, v16\n"
"v_add_f32 v80, v80, v34\n"
"v_add_f32 v81, v81, v35\n"
"v_lshlrev_b32 v36, 16, v17\n"
"v_and_b32 v37, 0xffff0000, v17\n"
"v_add_f32 v82, v82, v36\n"
"v_add_f32 v83, v83, v37\n"
"v_lshlrev_b32 v34, 16, v18\n"
"v_and_b32 v35, 0xffff0000, v18\n"
"v_add_f32 v84, v84, v34\n"
"v_add_f32 v85, v85, v35\n"
"v_lshlrev_b32 v36, 16, v19\n"
"v_and_b32 v37, 0xffff0000, v19\n"
"v_add_f32 v86, v86, v36\n"
"v_add_f32 v87, v87, v37\n"
"v_lshlrev_b32 v34, 16, v20\n"
"v_and_b32 v35, 0xffff0000, v20\n"
"v_add_f32 v148, v148, v34\n"
"v_add_f32 v149, v149, v35\n"
"v_lshlrev_b32 v36, 16, v21\n"
"v_and_b32 v37, 0xffff0000, v21\n"
"v_add_f32 v150, v150, v36\n"
"v_add_f32 v151, v151, v37\n"
"s_waitcnt vmcnt(0)\n"
"v_cmp_ne_u64 vcc, s[24:25], v[222:223]\n"
"s_nop 0\n"
"s_bitcmp1_b32 vcc_lo, 7\n"
"s_cbranch_scc1 .Lfused_block_kernel_slow7\n"
".Lfused_block_kernel_back7:\n"
"s_sub_u32 s81, s29, 7\n"
"s_and_b32 s81, s81, 7\n"
"s_lshl_b32 s81, s81, 13\n"
"s_add_u32 s81, s81, s74\n"
"buffer_load_dwordx2 v[32:33], v13, s[8:11], s81 offen sc1\n"
"buffer_load_dwordx4 v[14:17], v1, s[8:11], s73 offen offset:0 sc1\n"
"buffer_load_dwordx4 v[18:21], v1, s[8:11], s73 offen offset:1024 sc1\n"
"s_add_u32 s73, s73, 8192\n"
"s_waitcnt lgkmcnt(0)\n"
"v_lshlrev_b32 v56, 16, v24\n"
"v_and_b32 v57, 0xffff0000, v24\n"
"v_lshlrev_b32 v58, 16, v25\n"
"v_and_b32 v59, 0xffff0000, v25\n"
"v_lshlrev_b32 v60, 16, v26\n"
"v_and_b32 v61, 0xffff0000, v26\n"
"v_lshlrev_b32 v62, 16, v27\n"
"v_and_b32 v63, 0xffff0000, v27\n"
"v_lshlrev_b32 v64, 16, v28\n"
"v_and_b32 v65, 0xffff0000, v28\n"
"v_lshlrev_b32 v66, 16, v29\n"
"v_and_b32 v67, 0xffff0000, v29\n"
"v_lshlrev_b32 v68, 16, v30\n"
"v_and_b32 v69, 0xffff0000, v30\n"
"v_lshlrev_b32 v70, 16, v31\n"
"v_and_b32 v71, 0xffff0000, v31\n"
"v_lshlrev_b32 v52, 16, v38\n"
"v_and_b32 v53, 0xffff0000, v38\n"
"v_lshlrev_b32 v54, 16, v39\n"
"v_and_b32 v55, 0xffff0000, v39\n"
"s_waitcnt vmcnt(0)\n"
"v_dot2c_f32_bf16 v152, s7, v32\n"
"v_dot2c_f32_bf16 v153, s34, v32\n"
"v_dot2c_f32_bf16 v154, s7, v33\n"
"v_dot2c_f32_bf16 v155, s34, v33\n"
"v_dot2c_f32_bf16 v76, s7, v14\n"
"v_dot2c_f32_bf16 v77, s34, v14\n"
"v_dot2c_f32_bf16 v78, s7, v15\n"
"v_dot2c_f32_bf16 v79, s34, v15\n"
"v_dot2c_f32_bf16 v80, s7, v16\n"
"v_dot2c_f32_bf16 v81, s34, v16\n"
"v_dot2c_f32_bf16 v82, s7, v17\n"
"v_dot2c_f32_bf16 v83, s34, v17\n"
"v_dot2c_f32_bf16 v84, s7, v18\n"
"v_dot2c_f32_bf16 v85, s34, v18\n"
"v_dot2c_f32_bf16 v86, s7, v19\n"
"v_dot2c_f32_bf16 v87, s34, v19\n"
"v_dot2c_f32_bf16 v148, s7, v20\n"
"v_dot2c_f32_bf16 v149, s34, v20\n"
"v_dot2c_f32_bf16 v150, s7, v21\n"
"v_dot2c_f32_bf16 v151, s34, v21\n"
"v_fmac_f32 v52, v152, v44\n"
"v_fmac_f32 v53, v153, v44\n"
"v_fmac_f32 v54, v154, v44\n"
"v_fmac_f32 v55, v155, v44\n"
"v_fmac_f32 v56, v76, v216\n"
"v_fmac_f32 v57, v77, v216\n"
"v_fmac_f32 v58, v78, v216\n"
"v_fmac_f32 v59, v79, v216\n"
"v_fmac_f32 v60, v80, v217\n"
"v_fmac_f32 v61, v81, v217\n"
"v_fmac_f32 v62, v82, v217\n"
"v_fmac_f32 v63, v83, v217\n"
"v_fmac_f32 v64, v84, v218\n"
"v_fmac_f32 v65, v85, v218\n"
"v_fmac_f32 v66, v86, v218\n"
"v_fmac_f32 v67, v87, v218\n"
"v_fmac_f32 v68, v148, v219\n"
"v_fmac_f32 v69, v149, v219\n"
"v_fmac_f32 v70, v150, v219\n"
"v_fmac_f32 v71, v151, v219\n"
"v_permlane32_swap_b32 v52, v54\n"
"v_permlane32_swap_b32 v53, v55\n"
"v_cvt_pk_bf16_f32 v52, v52, v54\n"
"v_cvt_pk_bf16_f32 v53, v53, v55\n"
"ds_write_b32 v46, v52 offset:0\n"
"ds_write_b32 v46, v53 offset:16\n"
"v_permlane32_swap_b32 v56, v58\n"
"v_permlane32_swap_b32 v57, v59\n"
"v_cvt_pk_bf16_f32 v56, v56, v58\n"
"v_cvt_pk_bf16_f32 v57, v57, v59\n"
"ds_write_b32 v45, v56 offset:0\n"
"ds_write_b32 v45, v57 offset:16\n"
"v_permlane32_swap_b32 v60, v62\n"
"v_permlane32_swap_b32 v61, v63\n"
"v_cvt_pk_bf16_f32 v60, v60, v62\n"
"v_cvt_pk_bf16_f32 v61, v61, v63\n"
"ds_write_b32 v45, v60 offset:2304\n"
"ds_write_b32 v45, v61 offset:2320\n"
"v_permlane32_swap_b32 v64, v66\n"
"v_permlane32_swap_b32 v65, v67\n"
"v_cvt_pk_bf16_f32 v64, v64, v66\n"
"v_cvt_pk_bf16_f32 v65, v65, v67\n"
"ds_write_b32 v45, v64 offset:4608\n"
"ds_write_b32 v45, v65 offset:4624\n"
"v_permlane32_swap_b32 v68, v70\n"
"v_permlane32_swap_b32 v69, v71\n"
"v_cvt_pk_bf16_f32 v68, v68, v70\n"
"v_cvt_pk_bf16_f32 v69, v69, v71\n"
"ds_write_b32 v45, v68 offset:6912\n"
"ds_write_b32 v45, v69 offset:6928\n"
"s_waitcnt lgkmcnt(0)\n"
"s_barrier\n"
"ds_read_b128 v[24:27], v51\n"
"ds_read_b128 v[28:31], v51 offset:1152\n"
"ds_read_b128 v[32:35], v49 offset:9216\n"
"s_waitcnt lgkmcnt(2)\n"
"buffer_store_dwordx4 v[24:27], v50, s[20:23], s88 offen\n"
"s_waitcnt lgkmcnt(1)\n"
"buffer_store_dwordx4 v[28:31], v50, s[20:23], s89 offen\n"
"s_cmp_eq_u32 s26, 0\n"
"s_cbranch_scc0 .Lfused_block_kernel_s69\n"
"s_waitcnt lgkmcnt(0)\n"
"buffer_store_dwordx4 v[32:35], v50, s[20:23], s90 offen\n"
"s_cmp_eq_u32 s27, 0\n"
"s_cbranch_scc0 .Lfused_block_kernel_s70\n"
"buffer_store_dwordx2 v[22:23], v220, s[8:11], 0 offen offset:0 sc1\n"
".Lfused_block_kernel_s70:\n"
".Lfused_block_kernel_s69:\n"
"s_branch .Lfused_block_kernel_end\n"
".Lfused_block_kernel_slow1:\n"
"s_mov_b32 s87, 0x4000\n"
".Lfused_block_kernel_slow1l:\n"
"buffer_load_dwordx2 v[222:223], v221, s[8:11], s79 offen offset:768 sc1\n"
"s_waitcnt vmcnt(0)\n"
"v_cmp_ne_u64 vcc, s[24:25], v[222:223]\n"
"s_nop 0\n"
"s_bitcmp1_b32 vcc_lo, 1\n"
"s_cbranch_scc0 .Lfused_block_kernel_back1\n"
"s_sub_u32 s87, s87, 1\n"
"s_cmp_lg_u32 s87, 0\n"
"s_cbranch_scc1 .Lfused_block_kernel_slow1l\n"
"s_branch .Lfused_block_kernel_back1\n"
".Lfused_block_kernel_slow2:\n"
"s_mov_b32 s87, 0x4000\n"
".Lfused_block_kernel_slow2l:\n"
"buffer_load_dwordx2 v[222:223], v221, s[8:11], s79 offen offset:768 sc1\n"
"s_waitcnt vmcnt(0)\n"
"v_cmp_ne_u64 vcc, s[24:25], v[222:223]\n"
"s_nop 0\n"
"s_bitcmp1_b32 vcc_lo, 2\n"
"s_cbranch_scc0 .Lfused_block_kernel_back2\n"
"s_sub_u32 s87, s87, 1\n"
"s_cmp_lg_u32 s87, 0\n"
"s_cbranch_scc1 .Lfused_block_kernel_slow2l\n"
"s_branch .Lfused_block_kernel_back2\n"
".Lfused_block_kernel_slow3:\n"
"s_mov_b32 s87, 0x4000\n"
".Lfused_block_kernel_slow3l:\n"
"buffer_load_dwordx2 v[222:223], v221, s[8:11], s79 offen offset:768 sc1\n"
"s_waitcnt vmcnt(0)\n"
"v_cmp_ne_u64 vcc, s[24:25], v[222:223]\n"
"s_nop 0\n"
"s_bitcmp1_b32 vcc_lo, 3\n"
"s_cbranch_scc0 .Lfused_block_kernel_back3\n"
"s_sub_u32 s87, s87, 1\n"
"s_cmp_lg_u32 s87, 0\n"
"s_cbranch_scc1 .Lfused_block_kernel_slow3l\n"
"s_branch .Lfused_block_kernel_back3\n"
".Lfused_block_kernel_slow4:\n"
"s_mov_b32 s87, 0x4000\n"
".Lfused_block_kernel_slow4l:\n"
"buffer_load_dwordx2 v[222:223], v221, s[8:11], s79 offen offset:768 sc1\n"
"s_waitcnt vmcnt(0)\n"
"v_cmp_ne_u64 vcc, s[24:25], v[222:223]\n"
"s_nop 0\n"
"s_bitcmp1_b32 vcc_lo, 4\n"
"s_cbranch_scc0 .Lfused_block_kernel_back4\n"
"s_sub_u32 s87, s87, 1\n"
"s_cmp_lg_u32 s87, 0\n"
"s_cbranch_scc1 .Lfused_block_kernel_slow4l\n"
"s_branch .Lfused_block_kernel_back4\n"
".Lfused_block_kernel_slow5:\n"
"s_mov_b32 s87, 0x4000\n"
".Lfused_block_kernel_slow5l:\n"
"buffer_load_dwordx2 v[222:223], v221, s[8:11], s79 offen offset:768 sc1\n"
"s_waitcnt vmcnt(0)\n"
"v_cmp_ne_u64 vcc, s[24:25], v[222:223]\n"
"s_nop 0\n"
"s_bitcmp1_b32 vcc_lo, 5\n"
"s_cbranch_scc0 .Lfused_block_kernel_back5\n"
"s_sub_u32 s87, s87, 1\n"
"s_cmp_lg_u32 s87, 0\n"
"s_cbranch_scc1 .Lfused_block_kernel_slow5l\n"
"s_branch .Lfused_block_kernel_back5\n"
".Lfused_block_kernel_slow6:\n"
"s_mov_b32 s87, 0x4000\n"
".Lfused_block_kernel_slow6l:\n"
"buffer_load_dwordx2 v[222:223], v221, s[8:11], s79 offen offset:768 sc1\n"
"s_waitcnt vmcnt(0)\n"
"v_cmp_ne_u64 vcc, s[24:25], v[222:223]\n"
"s_nop 0\n"
"s_bitcmp1_b32 vcc_lo, 6\n"
"s_cbranch_scc0 .Lfused_block_kernel_back6\n"
"s_sub_u32 s87, s87, 1\n"
"s_cmp_lg_u32 s87, 0\n"
"s_cbranch_scc1 .Lfused_block_kernel_slow6l\n"
"s_branch .Lfused_block_kernel_back6\n"
".Lfused_block_kernel_slow7:\n"
"s_mov_b32 s87, 0x4000\n"
".Lfused_block_kernel_slow7l:\n"
"buffer_load_dwordx2 v[222:223], v221, s[8:11], s79 offen offset:768 sc1\n"
"s_waitcnt vmcnt(0)\n"
"v_cmp_ne_u64 vcc, s[24:25], v[222:223]\n"
"s_nop 0\n"
"s_bitcmp1_b32 vcc_lo, 7\n"
"s_cbranch_scc0 .Lfused_block_kernel_back7\n"
"s_sub_u32 s87, s87, 1\n"
"s_cmp_lg_u32 s87, 0\n"
"s_cbranch_scc1 .Lfused_block_kernel_slow7l\n"
"s_branch .Lfused_block_kernel_back7\n"
".Lfused_block_kernel_end:\n"
  :: "s"(__builtin_amdgcn_kernarg_segment_ptr()), "s"(blockIdx.x), "v"(threadIdx.x), "s"((unsigned)(size_t)smem) :
"v1","v2","v3","v4","v5","v6","v7","v8","v9","v10","v11","v12","v13","v14","v15","v16","v17","v18","v19","v20","v21","v22","v23","v24",
"v25","v26","v27","v28","v29","v30","v31","v32","v33","v34","v35","v36","v37","v38","v39","v40","v41","v42","v43","v44","v45","v46","v47","v48",
"v49","v50","v51","v52","v53","v54","v55","v56","v57","v58","v59","v60","v61","v62","v63","v64","v65","v66","v67","v68","v69","v70","v71","v72",
"v73","v74","v75","v76","v77","v78","v79","v80","v81","v82","v83","v84","v85","v86","v87","v88","v89","v90","v91","v92","v93","v94","v95","v96",
"v97","v98","v99","v100","v101","v102","v103","v104","v105","v106","v107","v108","v109","v110","v111","v112","v113","v114","v115","v116","v117","v118","v119","v120",
"v121","v122","v123","v124","v125","v126","v127","v128","v129","v130","v131","v132","v133","v134","v135","v136","v137","v138","v139","v140","v141","v142","v143","v144",
"v145","v146","v147","v148","v149","v150","v151","v152","v153","v154","v155","v156","v157","v158","v159","v160","v161","v162","v163","v164","v165","v166","v167","v168",
"v169","v170","v171","v172","v173","v174","v175","v176","v177","v178","v179","v180","v181","v182","v183","v184","v185","v186","v187","v188","v189","v190","v191","v192",
"v193","v194","v195","v196","v197","v198","v199","v200","v201","v202","v203","v204","v205","v206","v207","v208","v209","v210","v211","v212","v213","v214","v215","v216",
"v217","v218","v219","v220","v221","v222","v223","v224","v225","v226","v227","v228","v229","v230","v231","v232","v233","v234","v235","v236","v237","v238","v239","v240",
"v241","v242","v243","v244","v245","v246","v247","v248","v249","v250","v251","v252","v253","v254","v255","a0","a1","a2","a3","a4","a5","a6","a7","a8",
"a9","a10","a11","a12","a13","a14","a15","a16","a17","a18","a19","a20","a21","a22","a23","a24","a25","a26","a27","a28","a29","a30","a31","a32",
"a33","a34","a35","a36","a37","a38","a39","a40","a41","a42","a43","a44","a45","a46","a47","a48","a49","a50","a51","a52","a53","a54","a55","a56",
"a57","a58","a59","a60","a61","a62","a63","a64","a65","a66","a67","a68","a69","a70","a71","a72","a73","a74","a75","a76","a77","a78","a79","a80",
"a81","a82","a83","a84","a85","a86","a87","a88","a89","a90","a91","a92","a93","a94","a95","a96","a97","a98","a99","a100","a101","a102","a103","a104",
"a105","a106","a107","a108","a109","a110","a111","a112","a113","a114","a115","a116","a117","a118","a119","a120","a121","a122","a123","a124","a125","a126","a127","a128",
"a129","a130","a131","a132","a133","a134","a135","a136","a137","a138","a139","a140","a141","a142","a143","a144","a145","a146","a147","a148","a149","a150","a151","a152",
"a153","a154","a155","a156","a157","a158","a159","a160","a161","a162","a163","a164","a165","a166","a167","a168","a169","a170","a171","a172","a173","a174","a175","a176",
"a177","a178","a179","a180","a181","a182","a183","a184","a185","a186","a187","a188","a189","a190","a191","a192","a193","a194","a195","a196","a197","a198","a199","a200",
"a201","a202","a203","a204","a205","a206","a207","a208","a209","a210","a211","a212","a213","a214","a215","a216","a217","a218","a219","a220","a221","a222","a223","a224",
"a225","a226","a227","a228","a229","a230","a231","a232","a233","a234","a235","a236","a237","a238","a239","a240","a241","a242","a243","a244","a245","a246","a247","a248",
"a249","a250","a251","a252","a253","a254","a255","s4","s5","s6","s7","s8","s9","s10","s11","s12","s13","s14","s15","s16","s17","s18","s19","s20",
"s21","s22","s23","s24","s25","s26","s27","s28","s29","s30","s31","s32","s33","s34","s35","s36","s37","s38","s39","s40","s41","s42","s43","s44",
"s45","s46","s47","s48","s49","s50","s51","s52","s53","s54","s55","s56","s57","s58","s59","s60","s61","s62","s63","s64","s65","s66","s67","s68",
"s69","s70","s71","s72","s73","s74","s75","s76","s77","s78","s79","s80","s81","s82","s83","s84","s85","s86","s87","s88","s89","s90","s91","s92",
"s93","s94","s95","s96","s97","s98","s99","s100","s101","vcc","scc","memory"
  );
}

extern "C" void kernel_launch(void* const* d_in, const int* in_sizes, int n_in, void* d_out, int out_size,
                              void* d_ws, size_t ws_size, hipStream_t stream) {
  (void)in_sizes; (void)n_in; (void)out_size; (void)ws_size;
  fused_block_kernel<<<256, 256, 0, stream>>>(d_in[0], d_in[1], d_in[2], d_in[4], d_in[5], d_in[6], d_in[7], d_out, d_ws);
}
```
